```python
import torch

def _softmax(x, axis=-1):
    return torch.softmax(x, dim=axis)

def _gelu(x):
    return torch.nn.functional.gelu(x, approximate='tanh')

def _silu(x):
    return torch.nn.functional.silu(x)

def _sigmoid(x):
    return torch.sigmoid(x)

def _layernorm(x, axis=-1, eps=1e-05):
    m = x.mean(dim=axis, keepdim=True)
    v = x.var(dim=axis, keepdim=True, unbiased=False)
    return (x - m) / torch.sqrt(v + eps)

def _scatter_add(tgt, idx, src, axis=0):
    return tgt.clone().index_add_(axis, idx, src)

def _segment_sum(src, seg, n):
    out = torch.zeros((n,) + src.shape[1:], dtype=src.dtype)
    return out.index_add_(0, seg, src)

def _pad(x, pw):
    flat = tuple((v for pair in reversed(pw) for v in pair))
    return torch.nn.functional.pad(x, flat)
kernel_name = 'parallel_sum__memory_aug_recurrent_mem_tr__glu_gated_geglu__prompt'

def setup_inputs():

    _w = lambda a: (lambda t: t.to(torch.bfloat16) if t.is_floating_point() else t)(torch.as_tensor(a))

    def _A_setup():
        B, S, Dm = (1, 2048, 512)
        H, Dh = (32, 128)
        M, seg = (8, 32)
        seg = min(seg, S)
        S = S // seg * seg
        return {'x': _w((lambda *s: torch.randn(*s, dtype=torch.bfloat16) * 0.05)(B, S, Dm)), 'mem0': _w((lambda *s: torch.randn(*s, dtype=torch.bfloat16) * 0.2)(B, M, Dm)), 'Wq': _w((lambda *s: torch.randn(*s, dtype=torch.bfloat16) * 1.6)(Dm, H, Dh) * Dm ** (-0.5)), 'Wk': _w((lambda *s: torch.randn(*s, dtype=torch.bfloat16) * 1.6)(Dm, H, Dh) * Dm ** (-0.5)), 'Wv': _w((lambda *s: torch.randn(*s, dtype=torch.bfloat16) * 0.2)(Dm, H, Dh) * Dm ** (-0.5)), 'Wo': _w((lambda *s: torch.randn(*s, dtype=torch.bfloat16) * 0.2)(H, Dh, Dm) * (H * Dh) ** (-0.5)), 'seg_len': seg}
    _A_in = _A_setup()

    def _B_setup():
        B, S, Dm, Df = (1, 2048, 512, 1792)
        return {'x': _w((lambda *s: torch.randn(*s, dtype=torch.bfloat16) * 0.05)(B, S, Dm)), 'w_gate': _w((lambda *s: torch.randn(*s, dtype=torch.bfloat16) * 0.4)(Dm, Df) * Dm ** (-0.5)), 'w_up': _w((lambda *s: torch.randn(*s, dtype=torch.bfloat16) * 0.4)(Dm, Df) * Dm ** (-0.5)), 'w_down': _w((lambda *s: torch.randn(*s, dtype=torch.bfloat16) * 0.4)(Df, Dm) * Df ** (-0.5))}
    _B_in = _B_setup()
    return {'A_x': _A_in['x'], 'A_mem0': _A_in['mem0'], 'A_Wq': _A_in['Wq'], 'A_Wk': _A_in['Wk'], 'A_Wv': _A_in['Wv'], 'A_Wo': _A_in['Wo'], 'A_seg_len': _A_in['seg_len'], 'B_x': _B_in['x'], 'B_w_gate': _B_in['w_gate'], 'B_w_up': _B_in['w_up'], 'B_w_down': _B_in['w_down']}

def reference(A_x, A_mem0, A_Wq, A_Wk, A_Wv, A_Wo, A_seg_len, B_x, B_w_gate, B_w_up, B_w_down):

    def _A_ref(x, mem0, Wq, Wk, Wv, Wo, seg_len):
        B, S, Dm = x.shape
        H, Dh = (Wq.shape[1], Wq.shape[2])
        M = mem0.shape[1]
        scale = Dh ** (-0.5)
        n_seg = S // seg_len
        L = M + seg_len
        idx = torch.arange(L)
        mask = (idx[:, None] < M) | (idx[:, None] >= idx[None, :])
        mem = mem0
        outs = []
        for s in range(n_seg):
            xs = x[:, s * seg_len:(s + 1) * seg_len, :].clone()
            h = torch.concatenate([mem, xs], axis=1)
            q = torch.einsum('btd,dhe->bhte', h, Wq)
            k = torch.einsum('btd,dhe->bhte', h, Wk)
            v = torch.einsum('btd,dhe->bhte', h, Wv)
            scores = torch.einsum('bhqe,bhke->bhqk', q, k) * scale
            scores = torch.where(mask[None, None], scores, -1000000000.0)
            p = _softmax(scores, axis=-1)
            o = torch.einsum('bhqk,bhke->bhqe', p, v)
            o = torch.einsum('bhte,hed->btd', o, Wo)
            mem = o[:, :M, :].clone()
            outs.append(o[:, M:, :])
        return torch.concatenate(outs, axis=1)

    def _B_ref(x, w_gate, w_up, w_down):
        g = torch.einsum('bsd,df->bsf', x, w_gate)
        u = torch.einsum('bsd,df->bsf', x, w_up)
        h = _gelu(g) * u
        return torch.einsum('bsf,fd->bsd', h, w_down)
    x = A_x
    xn = _layernorm(x, axis=-1)
    return x + _A_ref(xn, A_mem0, A_Wq, A_Wk, A_Wv, A_Wo, A_seg_len) + _B_ref(xn, B_w_gate, B_w_up, B_w_down)
```

```cpp
#include <hip/hip_runtime.h>
#include <hip/hip_bf16.h>
#include <stdint.h>

typedef unsigned short u16;
__device__ __forceinline__ float bf2f(u16 v) { return __uint_as_float(((unsigned)v) << 16); }
__device__ __forceinline__ u16 f2bf(float f) { unsigned u = __float_as_uint(f); u += 0x7FFFu + ((u >> 16) & 1u); return (u16)(u >> 16); }
__device__ __forceinline__ float rbf(float f) { return bf2f(f2bf(f)); }

constexpr int S = 2048, DM = 512, H = 32, DH = 128, M = 8, SEG = 32, DF = 1792, NSEG = 64, HD = H * DH;

__global__ void k_ln(const u16* x, u16* xn) {
  int row = blockIdx.x, t = threadIdx.x;
  float v[8]; float s = 0;
  for (int i = 0; i < 8; ++i) { v[i] = bf2f(x[row * DM + t * 8 + i]); s += v[i]; }
  for (int o = 32; o; o >>= 1) s += __shfl_xor(s, o);
  float mean = s / DM;
  float q = 0;
  for (int i = 0; i < 8; ++i) { float d = v[i] - mean; q += d * d; }
  for (int o = 32; o; o >>= 1) q += __shfl_xor(q, o);
  float var = rbf(q / DM);
  float m = rbf(mean);
  float den = rbf(sqrtf(rbf(var + 1e-5f)));
  for (int i = 0; i < 8; ++i) xn[row * DM + t * 8 + i] = f2bf(rbf(v[i] - m) / den);
}

__global__ void k_gemm(const u16* A, int lda, const u16* B, int ldb, u16* C, int ldc, int Mr, int N, int K) {
  __shared__ float As[16][17], Bs[16][17];
  int tx = threadIdx.x, ty = threadIdx.y;
  int row = blockIdx.y * 16 + ty, col = blockIdx.x * 16 + tx;
  float acc = 0;
  for (int k0 = 0; k0 < K; k0 += 16) {
    As[ty][tx] = row < Mr ? bf2f(A[(size_t)row * lda + k0 + tx]) : 0.f;
    Bs[ty][tx] = bf2f(B[(size_t)(k0 + ty) * ldb + col]);
    __syncthreads();
    for (int k = 0; k < 16; ++k) acc += As[ty][k] * Bs[k][tx];
    __syncthreads();
  }
  if (row < Mr) C[(size_t)row * ldc + col] = f2bf(acc);
}

__global__ void k_attn(const u16* Qm, const u16* Km, const u16* Vm, const u16* Qx, const u16* Kx, const u16* Vx, u16* O, int seg0, int mode) {
  int h = blockIdx.x, seg = seg0 + blockIdx.y;
  __shared__ float p[32][40];
  int nq = mode ? 32 : 8;
  const u16* km = Km + (size_t)seg * M * HD; const u16* vm = Vm + (size_t)seg * M * HD;
  const u16* q = mode ? Qx + (size_t)seg * SEG * HD : Qm + (size_t)seg * M * HD;
  const u16* kx = Kx + (size_t)seg * SEG * HD; const u16* vx = Vx + (size_t)seg * SEG * HD;
  for (int i = threadIdx.x; i < nq * 40; i += blockDim.x) {
    int r = i / 40, j = i % 40;
    const u16* kk = j < 8 ? km + j * HD + h * DH : kx + (j - 8) * HD + h * DH;
    float s = 0;
    for (int e = 0; e < DH; ++e) s += bf2f(q[r * HD + h * DH + e]) * bf2f(kk[e]);
    s = rbf(rbf(s) * 0.08838834764831845f);
    if (mode && j - 8 > r) s = -1e9f;
    p[r][j] = s;
  }
  __syncthreads();
  if (threadIdx.x < nq) {
    int r = threadIdx.x; float mx = -1e30f;
    for (int j = 0; j < 40; ++j) mx = fmaxf(mx, p[r][j]);
    float sum = 0;
    for (int j = 0; j < 40; ++j) { float e = __expf(p[r][j] - mx); p[r][j] = e; sum += e; }
    for (int j = 0; j < 40; ++j) p[r][j] = rbf(p[r][j] / sum);
  }
  __syncthreads();
  u16* o = mode ? O + (size_t)seg * SEG * HD : O;
  for (int i = threadIdx.x; i < nq * DH; i += blockDim.x) {
    int r = i / DH, e = i % DH;
    float s = 0;
    for (int j = 0; j < 40; ++j) s += p[r][j] * bf2f(j < 8 ? vm[j * HD + h * DH + e] : vx[(j - 8) * HD + h * DH + e]);
    o[r * HD + h * DH + e] = f2bf(s);
  }
}

__global__ void k_geglu(const u16* g, const u16* u, u16* hbuf, int n) {
  int i = blockIdx.x * blockDim.x + threadIdx.x;
  if (i < n) {
    float x = bf2f(g[i]);
    float t = tanhf(0.7978845608028654f * (x + 0.044715f * x * x * x));
    float ge = rbf(0.5f * x * (1.f + t));
    hbuf[i] = f2bf(ge * bf2f(u[i]));
  }
}
__global__ void k_final(const u16* x, const u16* a, const u16* b, u16* out, int n) {
  int i = blockIdx.x * blockDim.x + threadIdx.x;
  if (i < n) out[i] = f2bf(rbf(bf2f(x[i]) + bf2f(a[i])) + bf2f(b[i]));
}

extern "C" void kernel_launch(void* const* d_in, const int* in_sizes, int n_in, void* d_out, int out_size, void* d_ws, size_t ws_size, hipStream_t stream) {
  const u16* x = (const u16*)d_in[0]; const u16* mem0 = (const u16*)d_in[1];
  const u16* Wq = (const u16*)d_in[2]; const u16* Wk = (const u16*)d_in[3]; const u16* Wv = (const u16*)d_in[4]; const u16* Wo = (const u16*)d_in[5];
  const u16* wg = (const u16*)d_in[8]; const u16* wu = (const u16*)d_in[9]; const u16* wd = (const u16*)d_in[10];
  u16* ws = (u16*)d_ws;
  u16* xn = ws; ws += S * DM;
  u16* Qx = ws; ws += (size_t)S * HD; u16* Kx = ws; ws += (size_t)S * HD; u16* Vx = ws; ws += (size_t)S * HD; u16* Ox = ws; ws += (size_t)S * HD;
  u16* G = ws; ws += S * DF; u16* U = ws; ws += S * DF; u16* Hh = ws; ws += S * DF;
  u16* Aout = ws; ws += S * DM; u16* Bout = ws; ws += S * DM;
  u16* mem = ws; ws += (NSEG + 1) * M * DM;
  u16* Qm = ws; ws += (size_t)NSEG * M * HD; u16* Km = ws; ws += (size_t)NSEG * M * HD; u16* Vm = ws; ws += (size_t)NSEG * M * HD; u16* Om = ws; ws += M * HD;
  dim3 tb(16, 16);
  k_ln<<<S, 64, 0, stream>>>(x, xn);
  k_gemm<<<dim3(HD / 16, S / 16), tb, 0, stream>>>(xn, DM, Wq, HD, Qx, HD, S, HD, DM);
  k_gemm<<<dim3(HD / 16, S / 16), tb, 0, stream>>>(xn, DM, Wk, HD, Kx, HD, S, HD, DM);
  k_gemm<<<dim3(HD / 16, S / 16), tb, 0, stream>>>(xn, DM, Wv, HD, Vx, HD, S, HD, DM);
  k_gemm<<<dim3(DF / 16, S / 16), tb, 0, stream>>>(xn, DM, wg, DF, G, DF, S, DF, DM);
  k_gemm<<<dim3(DF / 16, S / 16), tb, 0, stream>>>(xn, DM, wu, DF, U, DF, S, DF, DM);
  k_geglu<<<(S * DF + 255) / 256, 256, 0, stream>>>(G, U, Hh, S * DF);
  k_gemm<<<dim3(DM / 16, S / 16), tb, 0, stream>>>(Hh, DF, wd, DM, Bout, DM, S, DM, DF);
  hipMemcpyAsync(mem, mem0, M * DM * 2, hipMemcpyDeviceToDevice, stream);
  for (int s = 0; s < NSEG; ++s) {
    const u16* ms = mem + s * M * DM;
    k_gemm<<<dim3(HD / 16, 1), tb, 0, stream>>>(ms, DM, Wq, HD, Qm + (size_t)s * M * HD, HD, M, HD, DM);
    k_gemm<<<dim3(HD / 16, 1), tb, 0, stream>>>(ms, DM, Wk, HD, Km + (size_t)s * M * HD, HD, M, HD, DM);
    k_gemm<<<dim3(HD / 16, 1), tb, 0, stream>>>(ms, DM, Wv, HD, Vm + (size_t)s * M * HD, HD, M, HD, DM);
    k_attn<<<dim3(H, 1), 256, 0, stream>>>(Qm, Km, Vm, Qx, Kx, Vx, Om, s, 0);
    k_gemm<<<dim3(DM / 16, 1), tb, 0, stream>>>(Om, HD, Wo, DM, mem + (s + 1) * M * DM, DM, M, DM, HD);
  }
  k_attn<<<dim3(H, NSEG), 256, 0, stream>>>(Qm, Km, Vm, Qx, Kx, Vx, Ox, 0, 1);
  k_gemm<<<dim3(DM / 16, S / 16), tb, 0, stream>>>(Ox, HD, Wo, DM, Aout, DM, S, DM, HD);
  k_final<<<(S * DM + 255) / 256, 256, 0, stream>>>(x, Aout, Bout, (u16*)d_out, S * DM);
}
```

```cpp
#include <hip/hip_runtime.h>
#include <stdint.h>

typedef unsigned short u16;
typedef __bf16 bf8 __attribute__((ext_vector_type(8)));
typedef __bf16 bf2 __attribute__((ext_vector_type(2)));
typedef short s4 __attribute__((ext_vector_type(4)));
typedef float f4 __attribute__((ext_vector_type(4)));
typedef float f2 __attribute__((ext_vector_type(2)));
typedef unsigned u4 __attribute__((ext_vector_type(4)));
typedef unsigned u2 __attribute__((ext_vector_type(2)));
#define LAS __attribute__((address_space(3)))
#define GAS __attribute__((address_space(1)))
#define DI __device__ __forceinline__
#define RLX_AGENT __ATOMIC_RELAXED, __HIP_MEMORY_SCOPE_AGENT

constexpr int SEQ = 2048, DM = 512, NH = 32, DH = 128, DF = 1792, NSEG = 64, HD = NH * DH;
constexpr int LDS_BYTES = 163840;
constexpr int L_SEL = 14336, L_W = 32768;
constexpr unsigned POISON = 0xFFBFFFBFu;
constexpr unsigned CH_PROBE = 102;
constexpr size_t W_COMM = 0;
constexpr size_t W_CTL = 2u << 20;
constexpr size_t W_XN = W_CTL + (64u << 10);
constexpr size_t W_KMV = 5u << 20;
constexpr unsigned W_FAR = 16u << 20, W_END = 192u << 20;
constexpr unsigned F_KV = 0, F_QF = 32u << 20, F_HF = 48u << 20, F_PART = 56u << 20;
constexpr unsigned NSEC = 11;
constexpr int C_EPOCH = 32, C_DONE = 64;
constexpr int C_CNT = 1024, C_PROG = 10240, C_SEG = 12288;

struct Params {
  const u16* x; const u16* mem0; const u16* Wq; const u16* Wk; const u16* Wv; const u16* Wo;
  const u16* wg; const u16* wu; const u16* wd;
  u16* out; unsigned char* ws;
  int dB, dA, dC, pad;
};

DI int lane_id() { int l; asm volatile("v_mbcnt_lo_u32_b32 %0, -1, 0\n\tv_mbcnt_hi_u32_b32 %0, -1, %0" : "=v"(l)); return l & 63; }
template <int N>
DI void touch_code(int tid, int nthreads) {
  unsigned long long pc; int sz;
  asm volatile("s_getpc_b64 %0\nPCL_%=:\n\ts_mov_b32 %1, KMAIN_CODE_END-PCL_%=" : "=s"(pc), "=s"(sz));
  const __amdgpu_buffer_rsrc_t r = __builtin_amdgcn_make_buffer_rsrc((void*)pc, 0, max(sz, 0), 0x00020000);
  unsigned sink = 0;
  #pragma unroll
  for (int j = 0; j < N; ++j) sink += __builtin_amdgcn_raw_buffer_load_b32(r, tid * 128, j * nthreads * 128, 0);
  asm volatile("" :: "v"(sink));
}
DI unsigned char* ws_base(unsigned char* ws) { return (unsigned char*)((((uintptr_t)ws) + (2u << 20) - 1) & ~(uintptr_t)((2u << 20) - 1)); }
DI unsigned pk_bf16(float a, float b) { f2 v = {a, b}; return __builtin_bit_cast(unsigned, __builtin_convertvector(v, bf2)); }
DI float bf_lo(unsigned u) { return __uint_as_float(u << 16); }
DI float bf_hi(unsigned u) { return __uint_as_float(u & 0xFFFF0000u); }
DI float rbf(float f) { return bf_lo(pk_bf16(f, 0.f) & 0xFFFFu); }
DI __amdgpu_buffer_rsrc_t mkrsrc(const void* p, unsigned n) { return __builtin_amdgcn_make_buffer_rsrc((void*)p, 0, n, 0x00020000); }
DI u4 ld16(__amdgpu_buffer_rsrc_t r, unsigned vo, unsigned so) { return __builtin_amdgcn_raw_buffer_load_b128(r, vo, so, 16); }
DI u2 ld8(__amdgpu_buffer_rsrc_t r, unsigned vo, unsigned so) { return __builtin_amdgcn_raw_buffer_load_b64(r, vo, so, 16); }
DI void st16(u4 v, __amdgpu_buffer_rsrc_t r, unsigned vo, unsigned so) { unsigned o = vo + so; asm volatile("" : "+v"(o)); __builtin_amdgcn_raw_buffer_store_b128(v, r, o, 0, 16); }
DI f4 mfma32(u4 a, u4 b, f4 c) { return __builtin_amdgcn_mfma_f32_16x16x32_bf16(__builtin_bit_cast(bf8, a), __builtin_bit_cast(bf8, b), c, 0, 0, 0); }
DI f4 mfma16(u2 a, u2 b, f4 c) { return __builtin_amdgcn_mfma_f32_16x16x16bf16_1k(__builtin_bit_cast(s4, a), __builtin_bit_cast(s4, b), c, 0, 0, 0); }
DI u2 trd(LAS unsigned char* p) { return __builtin_bit_cast(u2, __builtin_amdgcn_ds_read_tr16_b64_v4i16((LAS s4*)p)); }
DI u4 pack8(f4 a, f4 b) { return (u4){pk_bf16(a[0], a[1]), pk_bf16(a[2], a[3]), pk_bf16(b[0], b[1]), pk_bf16(b[2], b[3])}; }
DI void dma16(const void* src, unsigned ldsoff) {
  unsigned keep;
  asm volatile("s_mov_b32 %0, m0\n\ts_mov_b32 m0, %2\n\ts_nop 0\n\tglobal_load_lds_dwordx4 %1, off\n\ts_mov_b32 m0, %0" : "=&s"(keep) : "v"(src), "s"(__builtin_amdgcn_readfirstlane(ldsoff)) : "memory");
}
#define BARRIER() asm volatile("s_waitcnt lgkmcnt(0)\n\ts_barrier" ::: "memory")
DI float allmax_g(float x) {
  auto r = __builtin_amdgcn_permlane32_swap(__float_as_uint(x), __float_as_uint(x), false, false);
  x = fmaxf(__uint_as_float(r[0]), __uint_as_float(r[1]));
  auto q = __builtin_amdgcn_permlane16_swap(__float_as_uint(x), __float_as_uint(x), false, false);
  return fmaxf(__uint_as_float(q[0]), __uint_as_float(q[1]));
}
DI float allsum_g(float x) {
  auto r = __builtin_amdgcn_permlane32_swap(__float_as_uint(x), __float_as_uint(x), false, false);
  x = __uint_as_float(r[0]) + __uint_as_float(r[1]);
  auto q = __builtin_amdgcn_permlane16_swap(__float_as_uint(x), __float_as_uint(x), false, false);
  return __uint_as_float(q[0]) + __uint_as_float(q[1]);
}
DI unsigned fmap(unsigned tbl, unsigned L) {
  const unsigned R = ((L >> 13) << 14) | (L & 8191u);
  return W_FAR + (R | ((((tbl >> (R >> 24)) ^ (R >> 21)) & 1u) << 13));
}
DI void wait_ge(const unsigned* w, unsigned mask, unsigned want) {
  for (unsigned n = 0; n < (1u << 17); ++n) {
    unsigned v = __builtin_amdgcn_readfirstlane(__hip_atomic_load((const GAS unsigned*)w, RLX_AGENT));
    if ((v & mask) >= want) break;
    __builtin_amdgcn_s_sleep(2);
  }
  asm volatile("" ::: "memory");
}

typedef unsigned long long u64;
DI unsigned get_tag(unsigned char* ws) { return __builtin_amdgcn_readfirstlane(__hip_atomic_load((const GAS unsigned*)(ws + W_CTL) + C_EPOCH, RLX_AGENT)) + 1u; }
DI unsigned get_tbl(unsigned char* ws, unsigned tag) {
  u64 v = 0;
  for (unsigned n = 0; n < (1u << 17); ++n) {
    v = __hip_atomic_load((const GAS u64*)(ws + W_CTL), RLX_AGENT);
    if (__builtin_amdgcn_readfirstlane((unsigned)(v >> 32)) == tag) break;
    __builtin_amdgcn_s_sleep(2);
  }
  asm volatile("" ::: "memory");
  return __builtin_amdgcn_readfirstlane((unsigned)v) >> 1;
}
DI unsigned wait_done(unsigned char* ws, int first, int n, unsigned tag) {
  const int lane = lane_id();
  u64 v = 0;
  for (unsigned it = 0; it < (1u << 17); ++it) {
    v = __hip_atomic_load((const GAS u64*)(ws + W_CTL) + C_DONE / 2 + first + (lane < n ? lane : 0), RLX_AGENT);
    if (__all((unsigned)(v >> 32) == tag)) break;
    __builtin_amdgcn_s_sleep(1);
  }
  asm volatile("" ::: "memory");
  return (unsigned)v;
}
DI void set_done(unsigned char* ws, int idx, unsigned tag, unsigned val) { __hip_atomic_store((GAS u64*)(ws + W_CTL) + C_DONE / 2 + idx, ((u64)tag << 32) | val, RLX_AGENT); }
DI float geglu(float x, float u) {
  constexpr float c1 = -2.0f * 0.7978845608028654f * 1.4426950408889634f, c2 = c1 * 0.044715f;
  const float e = __builtin_amdgcn_exp2f(x * __builtin_fmaf(x * x, c2, c1));
  return (x * u) * __builtin_amdgcn_rcpf(1.0f + e);
}

#define CL10(p, a) p #a "0", p #a "1", p #a "2", p #a "3", p #a "4", p #a "5", p #a "6", p #a "7", p #a "8", p #a "9"
#define CLOB_A "a0", "a1", "a2", "a3", "a4", "a5", "a6", "a7", "a8", "a9", CL10("a",1), CL10("a",2), CL10("a",3), CL10("a",4), CL10("a",5), CL10("a",6), CL10("a",7), CL10("a",8), CL10("a",9), \
  CL10("a",10), CL10("a",11), CL10("a",12), CL10("a",13), CL10("a",14), CL10("a",15), CL10("a",16), CL10("a",17), CL10("a",18), CL10("a",19), CL10("a",20), CL10("a",21), CL10("a",22), CL10("a",23), CL10("a",24), \
  "a250", "a251", "a252", "a253", "a254", "a255"
#define CLOB_B "v224", "v225", "v226", "v227", "v228", "v229", CL10("v",23), CL10("v",24), "v250", "v251", "v252", "v253", "v254", "v255"
template <int KIND>
DI void gemm_chunk(f4 (&acc)[4][4], const int (&ta)[4], __amdgpu_buffer_rsrc_t rs, unsigned vo, unsigned so, GAS unsigned* flag, unsigned add) {
  unsigned st;
#define GEMM_OPS : "=&v"(acc[0][0]), "=&v"(acc[0][1]), "=&v"(acc[0][2]), "=&v"(acc[0][3]), "=&v"(acc[1][0]), "=&v"(acc[1][1]), "=&v"(acc[1][2]), "=&v"(acc[1][3]), \
    "=&v"(acc[2][0]), "=&v"(acc[2][1]), "=&v"(acc[2][2]), "=&v"(acc[2][3]), "=&v"(acc[3][0]), "=&v"(acc[3][1]), "=&v"(acc[3][2]), "=&v"(acc[3][3]), [st] "=&s"(st) \
    : [ta0] "v"(ta[0]), [ta1] "v"(ta[1]), [ta2] "v"(ta[2]), [ta3] "v"(ta[3]), [rs] "s"(rs), [vo] "v"(vo), [so] "s"(so), [flag] "s"(flag), [add] "v"(add), [z] "v"(0u) \
    : "memory", "scc", CLOB_A, CLOB_B
  if constexpr (KIND == 0) asm volatile(
"s_waitcnt lgkmcnt(0)\n\t"
"ds_read_b64_tr_b16 v[224:225], %[ta0] offset:0\n\t"
"ds_read_b64_tr_b16 v[226:227], %[ta0] offset:2048\n\t"
"ds_read_b64_tr_b16 v[228:229], %[ta1] offset:0\n\t"
"ds_read_b64_tr_b16 v[230:231], %[ta1] offset:2048\n\t"
"ds_read_b64_tr_b16 v[232:233], %[ta2] offset:0\n\t"
"ds_read_b64_tr_b16 v[234:235], %[ta2] offset:2048\n\t"
"ds_read_b64_tr_b16 v[236:237], %[ta3] offset:0\n\t"
"ds_read_b64_tr_b16 v[238:239], %[ta3] offset:2048\n\t"
"s_waitcnt vmcnt(60)\n\t"
"s_waitcnt lgkmcnt(6)\n\t"
"v_mfma_f32_16x16x32_bf16 %0, a[0:3], v[224:227], 0\n\t"
"ds_read_b64_tr_b16 v[240:241], %[ta0] offset:4096\n\t"
"v_mfma_f32_16x16x32_bf16 %4, a[64:67], v[224:227], 0\n\t"
"ds_read_b64_tr_b16 v[242:243], %[ta0] offset:6144\n\t"
"v_mfma_f32_16x16x32_bf16 %8, a[128:131], v[224:227], 0\n\t"
"ds_read_b64_tr_b16 v[244:245], %[ta1] offset:4096\n\t"
"v_mfma_f32_16x16x32_bf16 %12, a[192:195], v[224:227], 0\n\t"
"ds_read_b64_tr_b16 v[246:247], %[ta1] offset:6144\n\t"
"s_waitcnt lgkmcnt(8)\n\t"
"v_mfma_f32_16x16x32_bf16 %1, a[0:3], v[228:231], 0\n\t"
"ds_read_b64_tr_b16 v[248:249], %[ta2] offset:4096\n\t"
"v_mfma_f32_16x16x32_bf16 %5, a[64:67], v[228:231], 0\n\t"
"ds_read_b64_tr_b16 v[250:251], %[ta2] offset:6144\n\t"
"v_mfma_f32_16x16x32_bf16 %9, a[128:131], v[228:231], 0\n\t"
"ds_read_b64_tr_b16 v[252:253], %[ta3] offset:4096\n\t"
"v_mfma_f32_16x16x32_bf16 %13, a[192:195], v[228:231], 0\n\t"
"ds_read_b64_tr_b16 v[254:255], %[ta3] offset:6144\n\t"
"s_waitcnt lgkmcnt(10)\n\t"
"v_mfma_f32_16x16x32_bf16 %2, a[0:3], v[232:235], 0\n\t"
"v_mfma_f32_16x16x32_bf16 %6, a[64:67], v[232:235], 0\n\t"
"v_mfma_f32_16x16x32_bf16 %10, a[128:131], v[232:235], 0\n\t"
"v_mfma_f32_16x16x32_bf16 %14, a[192:195], v[232:235], 0\n\t"
"s_waitcnt lgkmcnt(8)\n\t"
"v_mfma_f32_16x16x32_bf16 %3, a[0:3], v[236:239], 0\n\t"
"v_mfma_f32_16x16x32_bf16 %7, a[64:67], v[236:239], 0\n\t"
"v_mfma_f32_16x16x32_bf16 %11, a[128:131], v[236:239], 0\n\t"
"v_mfma_f32_16x16x32_bf16 %15, a[192:195], v[236:239], 0\n\t"
"s_waitcnt vmcnt(56)\n\t"
"s_waitcnt lgkmcnt(6)\n\t"
"v_mfma_f32_16x16x32_bf16 %0, a[4:7], v[240:243], %0\n\t"
"ds_read_b64_tr_b16 v[224:225], %[ta0] offset:8192\n\t"
"v_mfma_f32_16x16x32_bf16 %4, a[68:71], v[240:243], %4\n\t"
"ds_read_b64_tr_b16 v[226:227], %[ta0] offset:10240\n\t"
"v_mfma_f32_16x16x32_bf16 %8, a[132:135], v[240:243], %8\n\t"
"ds_read_b64_tr_b16 v[228:229], %[ta1] offset:8192\n\t"
"v_mfma_f32_16x16x32_bf16 %12, a[196:199], v[240:243], %12\n\t"
"ds_read_b64_tr_b16 v[230:231], %[ta1] offset:10240\n\t"
"s_waitcnt lgkmcnt(8)\n\t"
"v_mfma_f32_16x16x32_bf16 %1, a[4:7], v[244:247], %1\n\t"
"ds_read_b64_tr_b16 v[232:233], %[ta2] offset:8192\n\t"
"v_mfma_f32_16x16x32_bf16 %5, a[68:71], v[244:247], %5\n\t"
"ds_read_b64_tr_b16 v[234:235], %[ta2] offset:10240\n\t"
"v_mfma_f32_16x16x32_bf16 %9, a[132:135], v[244:247], %9\n\t"
"ds_read_b64_tr_b16 v[236:237], %[ta3] offset:8192\n\t"
"v_mfma_f32_16x16x32_bf16 %13, a[196:199], v[244:247], %13\n\t"
"ds_read_b64_tr_b16 v[238:239], %[ta3] offset:10240\n\t"
"s_waitcnt lgkmcnt(10)\n\t"
"v_mfma_f32_16x16x32_bf16 %2, a[4:7], v[248:251], %2\n\t"
"v_mfma_f32_16x16x32_bf16 %6, a[68:71], v[248:251], %6\n\t"
"v_mfma_f32_16x16x32_bf16 %10, a[132:135], v[248:251], %10\n\t"
"v_mfma_f32_16x16x32_bf16 %14, a[196:199], v[248:251], %14\n\t"
"s_waitcnt lgkmcnt(8)\n\t"
"v_mfma_f32_16x16x32_bf16 %3, a[4:7], v[252:255], %3\n\t"
"v_mfma_f32_16x16x32_bf16 %7, a[68:71], v[252:255], %7\n\t"
"v_mfma_f32_16x16x32_bf16 %11, a[132:135], v[252:255], %11\n\t"
"v_mfma_f32_16x16x32_bf16 %15, a[196:199], v[252:255], %15\n\t"
"s_waitcnt vmcnt(52)\n\t"
"s_waitcnt lgkmcnt(6)\n\t"
"v_mfma_f32_16x16x32_bf16 %0, a[8:11], v[224:227], %0\n\t"
"ds_read_b64_tr_b16 v[240:241], %[ta0] offset:12288\n\t"
"v_mfma_f32_16x16x32_bf16 %4, a[72:75], v[224:227], %4\n\t"
"ds_read_b64_tr_b16 v[242:243], %[ta0] offset:14336\n\t"
"v_mfma_f32_16x16x32_bf16 %8, a[136:139], v[224:227], %8\n\t"
"ds_read_b64_tr_b16 v[244:245], %[ta1] offset:12288\n\t"
"v_mfma_f32_16x16x32_bf16 %12, a[200:203], v[224:227], %12\n\t"
"ds_read_b64_tr_b16 v[246:247], %[ta1] offset:14336\n\t"
"s_waitcnt lgkmcnt(8)\n\t"
"v_mfma_f32_16x16x32_bf16 %1, a[8:11], v[228:231], %1\n\t"
"ds_read_b64_tr_b16 v[248:249], %[ta2] offset:12288\n\t"
"v_mfma_f32_16x16x32_bf16 %5, a[72:75], v[228:231], %5\n\t"
"ds_read_b64_tr_b16 v[250:251], %[ta2] offset:14336\n\t"
"v_mfma_f32_16x16x32_bf16 %9, a[136:139], v[228:231], %9\n\t"
"ds_read_b64_tr_b16 v[252:253], %[ta3] offset:12288\n\t"
"v_mfma_f32_16x16x32_bf16 %13, a[200:203], v[228:231], %13\n\t"
"ds_read_b64_tr_b16 v[254:255], %[ta3] offset:14336\n\t"
"s_waitcnt lgkmcnt(10)\n\t"
"v_mfma_f32_16x16x32_bf16 %2, a[8:11], v[232:235], %2\n\t"
"v_mfma_f32_16x16x32_bf16 %6, a[72:75], v[232:235], %6\n\t"
"v_mfma_f32_16x16x32_bf16 %10, a[136:139], v[232:235], %10\n\t"
"v_mfma_f32_16x16x32_bf16 %14, a[200:203], v[232:235], %14\n\t"
"s_waitcnt lgkmcnt(8)\n\t"
"v_mfma_f32_16x16x32_bf16 %3, a[8:11], v[236:239], %3\n\t"
"v_mfma_f32_16x16x32_bf16 %7, a[72:75], v[236:239], %7\n\t"
"v_mfma_f32_16x16x32_bf16 %11, a[136:139], v[236:239], %11\n\t"
"v_mfma_f32_16x16x32_bf16 %15, a[200:203], v[236:239], %15\n\t"
"s_waitcnt vmcnt(48)\n\t"
"s_waitcnt lgkmcnt(6)\n\t"
"v_mfma_f32_16x16x32_bf16 %0, a[12:15], v[240:243], %0\n\t"
"ds_read_b64_tr_b16 v[224:225], %[ta0] offset:16384\n\t"
"v_mfma_f32_16x16x32_bf16 %4, a[76:79], v[240:243], %4\n\t"
"ds_read_b64_tr_b16 v[226:227], %[ta0] offset:18432\n\t"
"v_mfma_f32_16x16x32_bf16 %8, a[140:143], v[240:243], %8\n\t"
"ds_read_b64_tr_b16 v[228:229], %[ta1] offset:16384\n\t"
"v_mfma_f32_16x16x32_bf16 %12, a[204:207], v[240:243], %12\n\t"
"ds_read_b64_tr_b16 v[230:231], %[ta1] offset:18432\n\t"
"s_waitcnt lgkmcnt(8)\n\t"
"v_mfma_f32_16x16x32_bf16 %1, a[12:15], v[244:247], %1\n\t"
"ds_read_b64_tr_b16 v[232:233], %[ta2] offset:16384\n\t"
"v_mfma_f32_16x16x32_bf16 %5, a[76:79], v[244:247], %5\n\t"
"ds_read_b64_tr_b16 v[234:235], %[ta2] offset:18432\n\t"
"v_mfma_f32_16x16x32_bf16 %9, a[140:143], v[244:247], %9\n\t"
"ds_read_b64_tr_b16 v[236:237], %[ta3] offset:16384\n\t"
"v_mfma_f32_16x16x32_bf16 %13, a[204:207], v[244:247], %13\n\t"
"ds_read_b64_tr_b16 v[238:239], %[ta3] offset:18432\n\t"
"s_waitcnt lgkmcnt(10)\n\t"
"v_mfma_f32_16x16x32_bf16 %2, a[12:15], v[248:251], %2\n\t"
"v_mfma_f32_16x16x32_bf16 %6, a[76:79], v[248:251], %6\n\t"
"v_mfma_f32_16x16x32_bf16 %10, a[140:143], v[248:251], %10\n\t"
"v_mfma_f32_16x16x32_bf16 %14, a[204:207], v[248:251], %14\n\t"
"s_waitcnt lgkmcnt(8)\n\t"
"v_mfma_f32_16x16x32_bf16 %3, a[12:15], v[252:255], %3\n\t"
"v_mfma_f32_16x16x32_bf16 %7, a[76:79], v[252:255], %7\n\t"
"v_mfma_f32_16x16x32_bf16 %11, a[140:143], v[252:255], %11\n\t"
"v_mfma_f32_16x16x32_bf16 %15, a[204:207], v[252:255], %15\n\t"
"s_waitcnt vmcnt(44)\n\t"
"s_waitcnt lgkmcnt(6)\n\t"
"v_mfma_f32_16x16x32_bf16 %0, a[16:19], v[224:227], %0\n\t"
"ds_read_b64_tr_b16 v[240:241], %[ta0] offset:20480\n\t"
"v_mfma_f32_16x16x32_bf16 %4, a[80:83], v[224:227], %4\n\t"
"ds_read_b64_tr_b16 v[242:243], %[ta0] offset:22528\n\t"
"v_mfma_f32_16x16x32_bf16 %8, a[144:147], v[224:227], %8\n\t"
"ds_read_b64_tr_b16 v[244:245], %[ta1] offset:20480\n\t"
"v_mfma_f32_16x16x32_bf16 %12, a[208:211], v[224:227], %12\n\t"
"ds_read_b64_tr_b16 v[246:247], %[ta1] offset:22528\n\t"
"s_waitcnt lgkmcnt(8)\n\t"
"v_mfma_f32_16x16x32_bf16 %1, a[16:19], v[228:231], %1\n\t"
"ds_read_b64_tr_b16 v[248:249], %[ta2] offset:20480\n\t"
"v_mfma_f32_16x16x32_bf16 %5, a[80:83], v[228:231], %5\n\t"
"ds_read_b64_tr_b16 v[250:251], %[ta2] offset:22528\n\t"
"v_mfma_f32_16x16x32_bf16 %9, a[144:147], v[228:231], %9\n\t"
"ds_read_b64_tr_b16 v[252:253], %[ta3] offset:20480\n\t"
"v_mfma_f32_16x16x32_bf16 %13, a[208:211], v[228:231], %13\n\t"
"ds_read_b64_tr_b16 v[254:255], %[ta3] offset:22528\n\t"
"s_waitcnt lgkmcnt(10)\n\t"
"v_mfma_f32_16x16x32_bf16 %2, a[16:19], v[232:235], %2\n\t"
"v_mfma_f32_16x16x32_bf16 %6, a[80:83], v[232:235], %6\n\t"
"v_mfma_f32_16x16x32_bf16 %10, a[144:147], v[232:235], %10\n\t"
"v_mfma_f32_16x16x32_bf16 %14, a[208:211], v[232:235], %14\n\t"
"s_waitcnt lgkmcnt(8)\n\t"
"v_mfma_f32_16x16x32_bf16 %3, a[16:19], v[236:239], %3\n\t"
"v_mfma_f32_16x16x32_bf16 %7, a[80:83], v[236:239], %7\n\t"
"v_mfma_f32_16x16x32_bf16 %11, a[144:147], v[236:239], %11\n\t"
"v_mfma_f32_16x16x32_bf16 %15, a[208:211], v[236:239], %15\n\t"
"s_waitcnt vmcnt(40)\n\t"
"s_waitcnt lgkmcnt(6)\n\t"
"v_mfma_f32_16x16x32_bf16 %0, a[20:23], v[240:243], %0\n\t"
"ds_read_b64_tr_b16 v[224:225], %[ta0] offset:24576\n\t"
"v_mfma_f32_16x16x32_bf16 %4, a[84:87], v[240:243], %4\n\t"
"ds_read_b64_tr_b16 v[226:227], %[ta0] offset:26624\n\t"
"v_mfma_f32_16x16x32_bf16 %8, a[148:151], v[240:243], %8\n\t"
"ds_read_b64_tr_b16 v[228:229], %[ta1] offset:24576\n\t"
"v_mfma_f32_16x16x32_bf16 %12, a[212:215], v[240:243], %12\n\t"
"ds_read_b64_tr_b16 v[230:231], %[ta1] offset:26624\n\t"
"s_waitcnt lgkmcnt(8)\n\t"
"v_mfma_f32_16x16x32_bf16 %1, a[20:23], v[244:247], %1\n\t"
"ds_read_b64_tr_b16 v[232:233], %[ta2] offset:24576\n\t"
"v_mfma_f32_16x16x32_bf16 %5, a[84:87], v[244:247], %5\n\t"
"ds_read_b64_tr_b16 v[234:235], %[ta2] offset:26624\n\t"
"v_mfma_f32_16x16x32_bf16 %9, a[148:151], v[244:247], %9\n\t"
"ds_read_b64_tr_b16 v[236:237], %[ta3] offset:24576\n\t"
"v_mfma_f32_16x16x32_bf16 %13, a[212:215], v[244:247], %13\n\t"
"ds_read_b64_tr_b16 v[238:239], %[ta3] offset:26624\n\t"
"s_waitcnt lgkmcnt(10)\n\t"
"v_mfma_f32_16x16x32_bf16 %2, a[20:23], v[248:251], %2\n\t"
"v_mfma_f32_16x16x32_bf16 %6, a[84:87], v[248:251], %6\n\t"
"v_mfma_f32_16x16x32_bf16 %10, a[148:151], v[248:251], %10\n\t"
"v_mfma_f32_16x16x32_bf16 %14, a[212:215], v[248:251], %14\n\t"
"s_waitcnt lgkmcnt(8)\n\t"
"v_mfma_f32_16x16x32_bf16 %3, a[20:23], v[252:255], %3\n\t"
"v_mfma_f32_16x16x32_bf16 %7, a[84:87], v[252:255], %7\n\t"
"v_mfma_f32_16x16x32_bf16 %11, a[148:151], v[252:255], %11\n\t"
"v_mfma_f32_16x16x32_bf16 %15, a[212:215], v[252:255], %15\n\t"
"s_waitcnt vmcnt(36)\n\t"
"s_waitcnt lgkmcnt(6)\n\t"
"v_mfma_f32_16x16x32_bf16 %0, a[24:27], v[224:227], %0\n\t"
"ds_read_b64_tr_b16 v[240:241], %[ta0] offset:28672\n\t"
"v_mfma_f32_16x16x32_bf16 %4, a[88:91], v[224:227], %4\n\t"
"ds_read_b64_tr_b16 v[242:243], %[ta0] offset:30720\n\t"
"v_mfma_f32_16x16x32_bf16 %8, a[152:155], v[224:227], %8\n\t"
"ds_read_b64_tr_b16 v[244:245], %[ta1] offset:28672\n\t"
"v_mfma_f32_16x16x32_bf16 %12, a[216:219], v[224:227], %12\n\t"
"ds_read_b64_tr_b16 v[246:247], %[ta1] offset:30720\n\t"
"s_waitcnt lgkmcnt(8)\n\t"
"v_mfma_f32_16x16x32_bf16 %1, a[24:27], v[228:231], %1\n\t"
"ds_read_b64_tr_b16 v[248:249], %[ta2] offset:28672\n\t"
"v_mfma_f32_16x16x32_bf16 %5, a[88:91], v[228:231], %5\n\t"
"ds_read_b64_tr_b16 v[250:251], %[ta2] offset:30720\n\t"
"v_mfma_f32_16x16x32_bf16 %9, a[152:155], v[228:231], %9\n\t"
"ds_read_b64_tr_b16 v[252:253], %[ta3] offset:28672\n\t"
"v_mfma_f32_16x16x32_bf16 %13, a[216:219], v[228:231], %13\n\t"
"ds_read_b64_tr_b16 v[254:255], %[ta3] offset:30720\n\t"
"s_waitcnt lgkmcnt(10)\n\t"
"v_mfma_f32_16x16x32_bf16 %2, a[24:27], v[232:235], %2\n\t"
"v_mfma_f32_16x16x32_bf16 %6, a[88:91], v[232:235], %6\n\t"
"v_mfma_f32_16x16x32_bf16 %10, a[152:155], v[232:235], %10\n\t"
"v_mfma_f32_16x16x32_bf16 %14, a[216:219], v[232:235], %14\n\t"
"s_waitcnt lgkmcnt(8)\n\t"
"v_mfma_f32_16x16x32_bf16 %3, a[24:27], v[236:239], %3\n\t"
"v_mfma_f32_16x16x32_bf16 %7, a[88:91], v[236:239], %7\n\t"
"v_mfma_f32_16x16x32_bf16 %11, a[152:155], v[236:239], %11\n\t"
"v_mfma_f32_16x16x32_bf16 %15, a[216:219], v[236:239], %15\n\t"
"s_waitcnt vmcnt(32)\n\t"
"s_waitcnt lgkmcnt(6)\n\t"
"v_mfma_f32_16x16x32_bf16 %0, a[28:31], v[240:243], %0\n\t"
"ds_read_b64_tr_b16 v[224:225], %[ta0] offset:32768\n\t"
"v_mfma_f32_16x16x32_bf16 %4, a[92:95], v[240:243], %4\n\t"
"ds_read_b64_tr_b16 v[226:227], %[ta0] offset:34816\n\t"
"v_mfma_f32_16x16x32_bf16 %8, a[156:159], v[240:243], %8\n\t"
"ds_read_b64_tr_b16 v[228:229], %[ta1] offset:32768\n\t"
"v_mfma_f32_16x16x32_bf16 %12, a[220:223], v[240:243], %12\n\t"
"ds_read_b64_tr_b16 v[230:231], %[ta1] offset:34816\n\t"
"s_waitcnt lgkmcnt(8)\n\t"
"v_mfma_f32_16x16x32_bf16 %1, a[28:31], v[244:247], %1\n\t"
"ds_read_b64_tr_b16 v[232:233], %[ta2] offset:32768\n\t"
"v_mfma_f32_16x16x32_bf16 %5, a[92:95], v[244:247], %5\n\t"
"ds_read_b64_tr_b16 v[234:235], %[ta2] offset:34816\n\t"
"v_mfma_f32_16x16x32_bf16 %9, a[156:159], v[244:247], %9\n\t"
"ds_read_b64_tr_b16 v[236:237], %[ta3] offset:32768\n\t"
"v_mfma_f32_16x16x32_bf16 %13, a[220:223], v[244:247], %13\n\t"
"ds_read_b64_tr_b16 v[238:239], %[ta3] offset:34816\n\t"
"s_waitcnt lgkmcnt(10)\n\t"
"v_mfma_f32_16x16x32_bf16 %2, a[28:31], v[248:251], %2\n\t"
"v_mfma_f32_16x16x32_bf16 %6, a[92:95], v[248:251], %6\n\t"
"v_mfma_f32_16x16x32_bf16 %10, a[156:159], v[248:251], %10\n\t"
"v_mfma_f32_16x16x32_bf16 %14, a[220:223], v[248:251], %14\n\t"
"s_waitcnt lgkmcnt(8)\n\t"
"v_mfma_f32_16x16x32_bf16 %3, a[28:31], v[252:255], %3\n\t"
"v_mfma_f32_16x16x32_bf16 %7, a[92:95], v[252:255], %7\n\t"
"v_mfma_f32_16x16x32_bf16 %11, a[156:159], v[252:255], %11\n\t"
"v_mfma_f32_16x16x32_bf16 %15, a[220:223], v[252:255], %15\n\t"
"s_waitcnt vmcnt(28)\n\t"
"s_waitcnt lgkmcnt(6)\n\t"
"v_mfma_f32_16x16x32_bf16 %0, a[32:35], v[224:227], %0\n\t"
"ds_read_b64_tr_b16 v[240:241], %[ta0] offset:36864\n\t"
"v_mfma_f32_16x16x32_bf16 %4, a[96:99], v[224:227], %4\n\t"
"ds_read_b64_tr_b16 v[242:243], %[ta0] offset:38912\n\t"
"v_mfma_f32_16x16x32_bf16 %8, a[160:163], v[224:227], %8\n\t"
"ds_read_b64_tr_b16 v[244:245], %[ta1] offset:36864\n\t"
"v_mfma_f32_16x16x32_bf16 %12, a[224:227], v[224:227], %12\n\t"
"ds_read_b64_tr_b16 v[246:247], %[ta1] offset:38912\n\t"
"s_waitcnt lgkmcnt(8)\n\t"
"v_mfma_f32_16x16x32_bf16 %1, a[32:35], v[228:231], %1\n\t"
"ds_read_b64_tr_b16 v[248:249], %[ta2] offset:36864\n\t"
"v_mfma_f32_16x16x32_bf16 %5, a[96:99], v[228:231], %5\n\t"
"ds_read_b64_tr_b16 v[250:251], %[ta2] offset:38912\n\t"
"v_mfma_f32_16x16x32_bf16 %9, a[160:163], v[228:231], %9\n\t"
"ds_read_b64_tr_b16 v[252:253], %[ta3] offset:36864\n\t"
"v_mfma_f32_16x16x32_bf16 %13, a[224:227], v[228:231], %13\n\t"
"ds_read_b64_tr_b16 v[254:255], %[ta3] offset:38912\n\t"
"s_waitcnt lgkmcnt(10)\n\t"
"v_mfma_f32_16x16x32_bf16 %2, a[32:35], v[232:235], %2\n\t"
"v_mfma_f32_16x16x32_bf16 %6, a[96:99], v[232:235], %6\n\t"
"v_mfma_f32_16x16x32_bf16 %10, a[160:163], v[232:235], %10\n\t"
"v_mfma_f32_16x16x32_bf16 %14, a[224:227], v[232:235], %14\n\t"
"s_waitcnt lgkmcnt(8)\n\t"
"v_mfma_f32_16x16x32_bf16 %3, a[32:35], v[236:239], %3\n\t"
"v_mfma_f32_16x16x32_bf16 %7, a[96:99], v[236:239], %7\n\t"
"v_mfma_f32_16x16x32_bf16 %11, a[160:163], v[236:239], %11\n\t"
"v_mfma_f32_16x16x32_bf16 %15, a[224:227], v[236:239], %15\n\t"
"s_waitcnt vmcnt(24)\n\t"
"s_waitcnt lgkmcnt(6)\n\t"
"v_mfma_f32_16x16x32_bf16 %0, a[36:39], v[240:243], %0\n\t"
"ds_read_b64_tr_b16 v[224:225], %[ta0] offset:40960\n\t"
"v_mfma_f32_16x16x32_bf16 %4, a[100:103], v[240:243], %4\n\t"
"ds_read_b64_tr_b16 v[226:227], %[ta0] offset:43008\n\t"
"v_mfma_f32_16x16x32_bf16 %8, a[164:167], v[240:243], %8\n\t"
"ds_read_b64_tr_b16 v[228:229], %[ta1] offset:40960\n\t"
"v_mfma_f32_16x16x32_bf16 %12, a[228:231], v[240:243], %12\n\t"
"ds_read_b64_tr_b16 v[230:231], %[ta1] offset:43008\n\t"
"s_waitcnt lgkmcnt(8)\n\t"
"v_mfma_f32_16x16x32_bf16 %1, a[36:39], v[244:247], %1\n\t"
"ds_read_b64_tr_b16 v[232:233], %[ta2] offset:40960\n\t"
"v_mfma_f32_16x16x32_bf16 %5, a[100:103], v[244:247], %5\n\t"
"ds_read_b64_tr_b16 v[234:235], %[ta2] offset:43008\n\t"
"v_mfma_f32_16x16x32_bf16 %9, a[164:167], v[244:247], %9\n\t"
"ds_read_b64_tr_b16 v[236:237], %[ta3] offset:40960\n\t"
"v_mfma_f32_16x16x32_bf16 %13, a[228:231], v[244:247], %13\n\t"
"ds_read_b64_tr_b16 v[238:239], %[ta3] offset:43008\n\t"
"s_waitcnt lgkmcnt(10)\n\t"
"v_mfma_f32_16x16x32_bf16 %2, a[36:39], v[248:251], %2\n\t"
"v_mfma_f32_16x16x32_bf16 %6, a[100:103], v[248:251], %6\n\t"
"v_mfma_f32_16x16x32_bf16 %10, a[164:167], v[248:251], %10\n\t"
"v_mfma_f32_16x16x32_bf16 %14, a[228:231], v[248:251], %14\n\t"
"s_waitcnt lgkmcnt(8)\n\t"
"v_mfma_f32_16x16x32_bf16 %3, a[36:39], v[252:255], %3\n\t"
"v_mfma_f32_16x16x32_bf16 %7, a[100:103], v[252:255], %7\n\t"
"v_mfma_f32_16x16x32_bf16 %11, a[164:167], v[252:255], %11\n\t"
"v_mfma_f32_16x16x32_bf16 %15, a[228:231], v[252:255], %15\n\t"
"s_waitcnt vmcnt(20)\n\t"
"s_waitcnt lgkmcnt(6)\n\t"
"v_mfma_f32_16x16x32_bf16 %0, a[40:43], v[224:227], %0\n\t"
"ds_read_b64_tr_b16 v[240:241], %[ta0] offset:45056\n\t"
"v_mfma_f32_16x16x32_bf16 %4, a[104:107], v[224:227], %4\n\t"
"ds_read_b64_tr_b16 v[242:243], %[ta0] offset:47104\n\t"
"v_mfma_f32_16x16x32_bf16 %8, a[168:171], v[224:227], %8\n\t"
"ds_read_b64_tr_b16 v[244:245], %[ta1] offset:45056\n\t"
"v_mfma_f32_16x16x32_bf16 %12, a[232:235], v[224:227], %12\n\t"
"ds_read_b64_tr_b16 v[246:247], %[ta1] offset:47104\n\t"
"s_waitcnt lgkmcnt(8)\n\t"
"v_mfma_f32_16x16x32_bf16 %1, a[40:43], v[228:231], %1\n\t"
"ds_read_b64_tr_b16 v[248:249], %[ta2] offset:45056\n\t"
"v_mfma_f32_16x16x32_bf16 %5, a[104:107], v[228:231], %5\n\t"
"ds_read_b64_tr_b16 v[250:251], %[ta2] offset:47104\n\t"
"v_mfma_f32_16x16x32_bf16 %9, a[168:171], v[228:231], %9\n\t"
"ds_read_b64_tr_b16 v[252:253], %[ta3] offset:45056\n\t"
"v_mfma_f32_16x16x32_bf16 %13, a[232:235], v[228:231], %13\n\t"
"ds_read_b64_tr_b16 v[254:255], %[ta3] offset:47104\n\t"
"s_waitcnt lgkmcnt(10)\n\t"
"v_mfma_f32_16x16x32_bf16 %2, a[40:43], v[232:235], %2\n\t"
"v_mfma_f32_16x16x32_bf16 %6, a[104:107], v[232:235], %6\n\t"
"v_mfma_f32_16x16x32_bf16 %10, a[168:171], v[232:235], %10\n\t"
"v_mfma_f32_16x16x32_bf16 %14, a[232:235], v[232:235], %14\n\t"
"s_waitcnt lgkmcnt(8)\n\t"
"v_mfma_f32_16x16x32_bf16 %3, a[40:43], v[236:239], %3\n\t"
"v_mfma_f32_16x16x32_bf16 %7, a[104:107], v[236:239], %7\n\t"
"v_mfma_f32_16x16x32_bf16 %11, a[168:171], v[236:239], %11\n\t"
"v_mfma_f32_16x16x32_bf16 %15, a[232:235], v[236:239], %15\n\t"
"s_waitcnt vmcnt(16)\n\t"
"s_waitcnt lgkmcnt(6)\n\t"
"v_mfma_f32_16x16x32_bf16 %0, a[44:47], v[240:243], %0\n\t"
"ds_read_b64_tr_b16 v[224:225], %[ta0] offset:49152\n\t"
"v_mfma_f32_16x16x32_bf16 %4, a[108:111], v[240:243], %4\n\t"
"ds_read_b64_tr_b16 v[226:227], %[ta0] offset:51200\n\t"
"v_mfma_f32_16x16x32_bf16 %8, a[172:175], v[240:243], %8\n\t"
"ds_read_b64_tr_b16 v[228:229], %[ta1] offset:49152\n\t"
"v_mfma_f32_16x16x32_bf16 %12, a[236:239], v[240:243], %12\n\t"
"ds_read_b64_tr_b16 v[230:231], %[ta1] offset:51200\n\t"
"s_waitcnt lgkmcnt(8)\n\t"
"v_mfma_f32_16x16x32_bf16 %1, a[44:47], v[244:247], %1\n\t"
"ds_read_b64_tr_b16 v[232:233], %[ta2] offset:49152\n\t"
"v_mfma_f32_16x16x32_bf16 %5, a[108:111], v[244:247], %5\n\t"
"ds_read_b64_tr_b16 v[234:235], %[ta2] offset:51200\n\t"
"v_mfma_f32_16x16x32_bf16 %9, a[172:175], v[244:247], %9\n\t"
"ds_read_b64_tr_b16 v[236:237], %[ta3] offset:49152\n\t"
"v_mfma_f32_16x16x32_bf16 %13, a[236:239], v[244:247], %13\n\t"
"ds_read_b64_tr_b16 v[238:239], %[ta3] offset:51200\n\t"
"s_waitcnt lgkmcnt(10)\n\t"
"v_mfma_f32_16x16x32_bf16 %2, a[44:47], v[248:251], %2\n\t"
"v_mfma_f32_16x16x32_bf16 %6, a[108:111], v[248:251], %6\n\t"
"v_mfma_f32_16x16x32_bf16 %10, a[172:175], v[248:251], %10\n\t"
"v_mfma_f32_16x16x32_bf16 %14, a[236:239], v[248:251], %14\n\t"
"s_waitcnt lgkmcnt(8)\n\t"
"v_mfma_f32_16x16x32_bf16 %3, a[44:47], v[252:255], %3\n\t"
"v_mfma_f32_16x16x32_bf16 %7, a[108:111], v[252:255], %7\n\t"
"v_mfma_f32_16x16x32_bf16 %11, a[172:175], v[252:255], %11\n\t"
"v_mfma_f32_16x16x32_bf16 %15, a[236:239], v[252:255], %15\n\t"
"s_waitcnt vmcnt(12)\n\t"
"s_waitcnt lgkmcnt(6)\n\t"
"v_mfma_f32_16x16x32_bf16 %0, a[48:51], v[224:227], %0\n\t"
"ds_read_b64_tr_b16 v[240:241], %[ta0] offset:53248\n\t"
"v_mfma_f32_16x16x32_bf16 %4, a[112:115], v[224:227], %4\n\t"
"ds_read_b64_tr_b16 v[242:243], %[ta0] offset:55296\n\t"
"v_mfma_f32_16x16x32_bf16 %8, a[176:179], v[224:227], %8\n\t"
"ds_read_b64_tr_b16 v[244:245], %[ta1] offset:53248\n\t"
"v_mfma_f32_16x16x32_bf16 %12, a[240:243], v[224:227], %12\n\t"
"ds_read_b64_tr_b16 v[246:247], %[ta1] offset:55296\n\t"
"s_waitcnt lgkmcnt(8)\n\t"
"v_mfma_f32_16x16x32_bf16 %1, a[48:51], v[228:231], %1\n\t"
"ds_read_b64_tr_b16 v[248:249], %[ta2] offset:53248\n\t"
"v_mfma_f32_16x16x32_bf16 %5, a[112:115], v[228:231], %5\n\t"
"ds_read_b64_tr_b16 v[250:251], %[ta2] offset:55296\n\t"
"v_mfma_f32_16x16x32_bf16 %9, a[176:179], v[228:231], %9\n\t"
"ds_read_b64_tr_b16 v[252:253], %[ta3] offset:53248\n\t"
"v_mfma_f32_16x16x32_bf16 %13, a[240:243], v[228:231], %13\n\t"
"ds_read_b64_tr_b16 v[254:255], %[ta3] offset:55296\n\t"
"s_waitcnt lgkmcnt(10)\n\t"
"v_mfma_f32_16x16x32_bf16 %2, a[48:51], v[232:235], %2\n\t"
"v_mfma_f32_16x16x32_bf16 %6, a[112:115], v[232:235], %6\n\t"
"v_mfma_f32_16x16x32_bf16 %10, a[176:179], v[232:235], %10\n\t"
"v_mfma_f32_16x16x32_bf16 %14, a[240:243], v[232:235], %14\n\t"
"s_waitcnt lgkmcnt(8)\n\t"
"v_mfma_f32_16x16x32_bf16 %3, a[48:51], v[236:239], %3\n\t"
"v_mfma_f32_16x16x32_bf16 %7, a[112:115], v[236:239], %7\n\t"
"v_mfma_f32_16x16x32_bf16 %11, a[176:179], v[236:239], %11\n\t"
"v_mfma_f32_16x16x32_bf16 %15, a[240:243], v[236:239], %15\n\t"
"s_waitcnt vmcnt(8)\n\t"
"s_waitcnt lgkmcnt(6)\n\t"
"v_mfma_f32_16x16x32_bf16 %0, a[52:55], v[240:243], %0\n\t"
"ds_read_b64_tr_b16 v[224:225], %[ta0] offset:57344\n\t"
"v_mfma_f32_16x16x32_bf16 %4, a[116:119], v[240:243], %4\n\t"
"ds_read_b64_tr_b16 v[226:227], %[ta0] offset:59392\n\t"
"v_mfma_f32_16x16x32_bf16 %8, a[180:183], v[240:243], %8\n\t"
"ds_read_b64_tr_b16 v[228:229], %[ta1] offset:57344\n\t"
"v_mfma_f32_16x16x32_bf16 %12, a[244:247], v[240:243], %12\n\t"
"ds_read_b64_tr_b16 v[230:231], %[ta1] offset:59392\n\t"
"s_waitcnt lgkmcnt(8)\n\t"
"v_mfma_f32_16x16x32_bf16 %1, a[52:55], v[244:247], %1\n\t"
"ds_read_b64_tr_b16 v[232:233], %[ta2] offset:57344\n\t"
"v_mfma_f32_16x16x32_bf16 %5, a[116:119], v[244:247], %5\n\t"
"ds_read_b64_tr_b16 v[234:235], %[ta2] offset:59392\n\t"
"v_mfma_f32_16x16x32_bf16 %9, a[180:183], v[244:247], %9\n\t"
"ds_read_b64_tr_b16 v[236:237], %[ta3] offset:57344\n\t"
"v_mfma_f32_16x16x32_bf16 %13, a[244:247], v[244:247], %13\n\t"
"ds_read_b64_tr_b16 v[238:239], %[ta3] offset:59392\n\t"
"s_waitcnt lgkmcnt(10)\n\t"
"v_mfma_f32_16x16x32_bf16 %2, a[52:55], v[248:251], %2\n\t"
"v_mfma_f32_16x16x32_bf16 %6, a[116:119], v[248:251], %6\n\t"
"v_mfma_f32_16x16x32_bf16 %10, a[180:183], v[248:251], %10\n\t"
"v_mfma_f32_16x16x32_bf16 %14, a[244:247], v[248:251], %14\n\t"
"s_waitcnt lgkmcnt(8)\n\t"
"v_mfma_f32_16x16x32_bf16 %3, a[52:55], v[252:255], %3\n\t"
"v_mfma_f32_16x16x32_bf16 %7, a[116:119], v[252:255], %7\n\t"
"v_mfma_f32_16x16x32_bf16 %11, a[180:183], v[252:255], %11\n\t"
"v_mfma_f32_16x16x32_bf16 %15, a[244:247], v[252:255], %15\n\t"
"s_waitcnt vmcnt(4)\n\t"
"s_waitcnt lgkmcnt(6)\n\t"
"v_mfma_f32_16x16x32_bf16 %0, a[56:59], v[224:227], %0\n\t"
"ds_read_b64_tr_b16 v[240:241], %[ta0] offset:61440\n\t"
"v_mfma_f32_16x16x32_bf16 %4, a[120:123], v[224:227], %4\n\t"
"ds_read_b64_tr_b16 v[242:243], %[ta0] offset:63488\n\t"
"v_mfma_f32_16x16x32_bf16 %8, a[184:187], v[224:227], %8\n\t"
"ds_read_b64_tr_b16 v[244:245], %[ta1] offset:61440\n\t"
"v_mfma_f32_16x16x32_bf16 %12, a[248:251], v[224:227], %12\n\t"
"ds_read_b64_tr_b16 v[246:247], %[ta1] offset:63488\n\t"
"s_waitcnt lgkmcnt(8)\n\t"
"v_mfma_f32_16x16x32_bf16 %1, a[56:59], v[228:231], %1\n\t"
"ds_read_b64_tr_b16 v[248:249], %[ta2] offset:61440\n\t"
"v_mfma_f32_16x16x32_bf16 %5, a[120:123], v[228:231], %5\n\t"
"ds_read_b64_tr_b16 v[250:251], %[ta2] offset:63488\n\t"
"v_mfma_f32_16x16x32_bf16 %9, a[184:187], v[228:231], %9\n\t"
"ds_read_b64_tr_b16 v[252:253], %[ta3] offset:61440\n\t"
"v_mfma_f32_16x16x32_bf16 %13, a[248:251], v[228:231], %13\n\t"
"ds_read_b64_tr_b16 v[254:255], %[ta3] offset:63488\n\t"
"s_waitcnt lgkmcnt(10)\n\t"
"v_mfma_f32_16x16x32_bf16 %2, a[56:59], v[232:235], %2\n\t"
"v_mfma_f32_16x16x32_bf16 %6, a[120:123], v[232:235], %6\n\t"
"v_mfma_f32_16x16x32_bf16 %10, a[184:187], v[232:235], %10\n\t"
"v_mfma_f32_16x16x32_bf16 %14, a[248:251], v[232:235], %14\n\t"
"s_waitcnt lgkmcnt(8)\n\t"
"v_mfma_f32_16x16x32_bf16 %3, a[56:59], v[236:239], %3\n\t"
"v_mfma_f32_16x16x32_bf16 %7, a[120:123], v[236:239], %7\n\t"
"v_mfma_f32_16x16x32_bf16 %11, a[184:187], v[236:239], %11\n\t"
"v_mfma_f32_16x16x32_bf16 %15, a[248:251], v[236:239], %15\n\t"
"s_waitcnt vmcnt(0)\n\t"
"s_waitcnt lgkmcnt(6)\n\t"
"v_mfma_f32_16x16x32_bf16 %0, a[60:63], v[240:243], %0\n\t"
"v_mfma_f32_16x16x32_bf16 %4, a[124:127], v[240:243], %4\n\t"
"v_mfma_f32_16x16x32_bf16 %8, a[188:191], v[240:243], %8\n\t"
"v_mfma_f32_16x16x32_bf16 %12, a[252:255], v[240:243], %12\n\t"
"s_waitcnt lgkmcnt(4)\n\t"
"v_mfma_f32_16x16x32_bf16 %1, a[60:63], v[244:247], %1\n\t"
"v_mfma_f32_16x16x32_bf16 %5, a[124:127], v[244:247], %5\n\t"
"v_mfma_f32_16x16x32_bf16 %9, a[188:191], v[244:247], %9\n\t"
"v_mfma_f32_16x16x32_bf16 %13, a[252:255], v[244:247], %13\n\t"
"s_waitcnt lgkmcnt(2)\n\t"
"v_mfma_f32_16x16x32_bf16 %2, a[60:63], v[248:251], %2\n\t"
"v_mfma_f32_16x16x32_bf16 %6, a[124:127], v[248:251], %6\n\t"
"s_mov_b64 exec, 1\n\t"
"global_atomic_add %[z], %[add], %[flag]\n\t"
"s_mov_b64 exec, -1\n\t"
"v_mfma_f32_16x16x32_bf16 %10, a[188:191], v[248:251], %10\n\t"
"v_mfma_f32_16x16x32_bf16 %14, a[252:255], v[248:251], %14\n\t"
"s_waitcnt lgkmcnt(0)\n\t"
"v_mfma_f32_16x16x32_bf16 %3, a[60:63], v[252:255], %3\n\t"
"v_mfma_f32_16x16x32_bf16 %7, a[124:127], v[252:255], %7\n\t"
"v_mfma_f32_16x16x32_bf16 %11, a[188:191], v[252:255], %11\n\t"
"v_mfma_f32_16x16x32_bf16 %15, a[252:255], v[252:255], %15\n\t"
"s_nop 7\n\t"
    GEMM_OPS);
  else if constexpr (KIND == 1) asm volatile(
"s_waitcnt lgkmcnt(0)\n\t"
"ds_read_b64_tr_b16 v[224:225], %[ta0] offset:0\n\t"
"ds_read_b64_tr_b16 v[226:227], %[ta0] offset:2048\n\t"
"ds_read_b64_tr_b16 v[228:229], %[ta1] offset:0\n\t"
"ds_read_b64_tr_b16 v[230:231], %[ta1] offset:2048\n\t"
"ds_read_b64_tr_b16 v[232:233], %[ta2] offset:0\n\t"
"ds_read_b64_tr_b16 v[234:235], %[ta2] offset:2048\n\t"
"ds_read_b64_tr_b16 v[236:237], %[ta3] offset:0\n\t"
"ds_read_b64_tr_b16 v[238:239], %[ta3] offset:2048\n\t"
"s_waitcnt vmcnt(60)\n\t"
"s_waitcnt lgkmcnt(6)\n\t"
"v_mfma_f32_16x16x32_bf16 %0, v[224:227], a[0:3], 0\n\t"
"ds_read_b64_tr_b16 v[240:241], %[ta0] offset:4096\n\t"
"v_mfma_f32_16x16x32_bf16 %4, v[224:227], a[64:67], 0\n\t"
"ds_read_b64_tr_b16 v[242:243], %[ta0] offset:6144\n\t"
"v_mfma_f32_16x16x32_bf16 %8, v[224:227], a[128:131], 0\n\t"
"ds_read_b64_tr_b16 v[244:245], %[ta1] offset:4096\n\t"
"v_mfma_f32_16x16x32_bf16 %12, v[224:227], a[192:195], 0\n\t"
"ds_read_b64_tr_b16 v[246:247], %[ta1] offset:6144\n\t"
"s_waitcnt lgkmcnt(8)\n\t"
"v_mfma_f32_16x16x32_bf16 %1, v[228:231], a[0:3], 0\n\t"
"ds_read_b64_tr_b16 v[248:249], %[ta2] offset:4096\n\t"
"v_mfma_f32_16x16x32_bf16 %5, v[228:231], a[64:67], 0\n\t"
"ds_read_b64_tr_b16 v[250:251], %[ta2] offset:6144\n\t"
"v_mfma_f32_16x16x32_bf16 %9, v[228:231], a[128:131], 0\n\t"
"ds_read_b64_tr_b16 v[252:253], %[ta3] offset:4096\n\t"
"v_mfma_f32_16x16x32_bf16 %13, v[228:231], a[192:195], 0\n\t"
"ds_read_b64_tr_b16 v[254:255], %[ta3] offset:6144\n\t"
"s_waitcnt lgkmcnt(10)\n\t"
"v_mfma_f32_16x16x32_bf16 %2, v[232:235], a[0:3], 0\n\t"
"v_mfma_f32_16x16x32_bf16 %6, v[232:235], a[64:67], 0\n\t"
"v_mfma_f32_16x16x32_bf16 %10, v[232:235], a[128:131], 0\n\t"
"v_mfma_f32_16x16x32_bf16 %14, v[232:235], a[192:195], 0\n\t"
"s_waitcnt lgkmcnt(8)\n\t"
"v_mfma_f32_16x16x32_bf16 %3, v[236:239], a[0:3], 0\n\t"
"v_mfma_f32_16x16x32_bf16 %7, v[236:239], a[64:67], 0\n\t"
"v_mfma_f32_16x16x32_bf16 %11, v[236:239], a[128:131], 0\n\t"
"v_mfma_f32_16x16x32_bf16 %15, v[236:239], a[192:195], 0\n\t"
"s_waitcnt vmcnt(56)\n\t"
"s_waitcnt lgkmcnt(6)\n\t"
"v_mfma_f32_16x16x32_bf16 %0, v[240:243], a[4:7], %0\n\t"
"ds_read_b64_tr_b16 v[224:225], %[ta0] offset:8192\n\t"
"v_mfma_f32_16x16x32_bf16 %4, v[240:243], a[68:71], %4\n\t"
"ds_read_b64_tr_b16 v[226:227], %[ta0] offset:10240\n\t"
"v_mfma_f32_16x16x32_bf16 %8, v[240:243], a[132:135], %8\n\t"
"ds_read_b64_tr_b16 v[228:229], %[ta1] offset:8192\n\t"
"v_mfma_f32_16x16x32_bf16 %12, v[240:243], a[196:199], %12\n\t"
"ds_read_b64_tr_b16 v[230:231], %[ta1] offset:10240\n\t"
"s_waitcnt lgkmcnt(8)\n\t"
"v_mfma_f32_16x16x32_bf16 %1, v[244:247], a[4:7], %1\n\t"
"ds_read_b64_tr_b16 v[232:233], %[ta2] offset:8192\n\t"
"v_mfma_f32_16x16x32_bf16 %5, v[244:247], a[68:71], %5\n\t"
"ds_read_b64_tr_b16 v[234:235], %[ta2] offset:10240\n\t"
"v_mfma_f32_16x16x32_bf16 %9, v[244:247], a[132:135], %9\n\t"
"ds_read_b64_tr_b16 v[236:237], %[ta3] offset:8192\n\t"
"v_mfma_f32_16x16x32_bf16 %13, v[244:247], a[196:199], %13\n\t"
"ds_read_b64_tr_b16 v[238:239], %[ta3] offset:10240\n\t"
"s_waitcnt lgkmcnt(10)\n\t"
"v_mfma_f32_16x16x32_bf16 %2, v[248:251], a[4:7], %2\n\t"
"v_mfma_f32_16x16x32_bf16 %6, v[248:251], a[68:71], %6\n\t"
"v_mfma_f32_16x16x32_bf16 %10, v[248:251], a[132:135], %10\n\t"
"v_mfma_f32_16x16x32_bf16 %14, v[248:251], a[196:199], %14\n\t"
"s_waitcnt lgkmcnt(8)\n\t"
"v_mfma_f32_16x16x32_bf16 %3, v[252:255], a[4:7], %3\n\t"
"v_mfma_f32_16x16x32_bf16 %7, v[252:255], a[68:71], %7\n\t"
"v_mfma_f32_16x16x32_bf16 %11, v[252:255], a[132:135], %11\n\t"
"v_mfma_f32_16x16x32_bf16 %15, v[252:255], a[196:199], %15\n\t"
"s_waitcnt vmcnt(52)\n\t"
"s_waitcnt lgkmcnt(6)\n\t"
"v_mfma_f32_16x16x32_bf16 %0, v[224:227], a[8:11], %0\n\t"
"ds_read_b64_tr_b16 v[240:241], %[ta0] offset:12288\n\t"
"v_mfma_f32_16x16x32_bf16 %4, v[224:227], a[72:75], %4\n\t"
"ds_read_b64_tr_b16 v[242:243], %[ta0] offset:14336\n\t"
"v_mfma_f32_16x16x32_bf16 %8, v[224:227], a[136:139], %8\n\t"
"ds_read_b64_tr_b16 v[244:245], %[ta1] offset:12288\n\t"
"v_mfma_f32_16x16x32_bf16 %12, v[224:227], a[200:203], %12\n\t"
"ds_read_b64_tr_b16 v[246:247], %[ta1] offset:14336\n\t"
"s_waitcnt lgkmcnt(8)\n\t"
"v_mfma_f32_16x16x32_bf16 %1, v[228:231], a[8:11], %1\n\t"
"ds_read_b64_tr_b16 v[248:249], %[ta2] offset:12288\n\t"
"v_mfma_f32_16x16x32_bf16 %5, v[228:231], a[72:75], %5\n\t"
"ds_read_b64_tr_b16 v[250:251], %[ta2] offset:14336\n\t"
"v_mfma_f32_16x16x32_bf16 %9, v[228:231], a[136:139], %9\n\t"
"ds_read_b64_tr_b16 v[252:253], %[ta3] offset:12288\n\t"
"v_mfma_f32_16x16x32_bf16 %13, v[228:231], a[200:203], %13\n\t"
"ds_read_b64_tr_b16 v[254:255], %[ta3] offset:14336\n\t"
"s_waitcnt lgkmcnt(10)\n\t"
"v_mfma_f32_16x16x32_bf16 %2, v[232:235], a[8:11], %2\n\t"
"v_mfma_f32_16x16x32_bf16 %6, v[232:235], a[72:75], %6\n\t"
"v_mfma_f32_16x16x32_bf16 %10, v[232:235], a[136:139], %10\n\t"
"v_mfma_f32_16x16x32_bf16 %14, v[232:235], a[200:203], %14\n\t"
"s_waitcnt lgkmcnt(8)\n\t"
"v_mfma_f32_16x16x32_bf16 %3, v[236:239], a[8:11], %3\n\t"
"v_mfma_f32_16x16x32_bf16 %7, v[236:239], a[72:75], %7\n\t"
"v_mfma_f32_16x16x32_bf16 %11, v[236:239], a[136:139], %11\n\t"
"v_mfma_f32_16x16x32_bf16 %15, v[236:239], a[200:203], %15\n\t"
"s_waitcnt vmcnt(48)\n\t"
"s_waitcnt lgkmcnt(6)\n\t"
"v_mfma_f32_16x16x32_bf16 %0, v[240:243], a[12:15], %0\n\t"
"ds_read_b64_tr_b16 v[224:225], %[ta0] offset:16384\n\t"
"v_mfma_f32_16x16x32_bf16 %4, v[240:243], a[76:79], %4\n\t"
"ds_read_b64_tr_b16 v[226:227], %[ta0] offset:18432\n\t"
"v_mfma_f32_16x16x32_bf16 %8, v[240:243], a[140:143], %8\n\t"
"ds_read_b64_tr_b16 v[228:229], %[ta1] offset:16384\n\t"
"v_mfma_f32_16x16x32_bf16 %12, v[240:243], a[204:207], %12\n\t"
"ds_read_b64_tr_b16 v[230:231], %[ta1] offset:18432\n\t"
"s_waitcnt lgkmcnt(8)\n\t"
"v_mfma_f32_16x16x32_bf16 %1, v[244:247], a[12:15], %1\n\t"
"ds_read_b64_tr_b16 v[232:233], %[ta2] offset:16384\n\t"
"v_mfma_f32_16x16x32_bf16 %5, v[244:247], a[76:79], %5\n\t"
"ds_read_b64_tr_b16 v[234:235], %[ta2] offset:18432\n\t"
"v_mfma_f32_16x16x32_bf16 %9, v[244:247], a[140:143], %9\n\t"
"ds_read_b64_tr_b16 v[236:237], %[ta3] offset:16384\n\t"
"v_mfma_f32_16x16x32_bf16 %13, v[244:247], a[204:207], %13\n\t"
"ds_read_b64_tr_b16 v[238:239], %[ta3] offset:18432\n\t"
"s_waitcnt lgkmcnt(10)\n\t"
"v_mfma_f32_16x16x32_bf16 %2, v[248:251], a[12:15], %2\n\t"
"v_mfma_f32_16x16x32_bf16 %6, v[248:251], a[76:79], %6\n\t"
"v_mfma_f32_16x16x32_bf16 %10, v[248:251], a[140:143], %10\n\t"
"v_mfma_f32_16x16x32_bf16 %14, v[248:251], a[204:207], %14\n\t"
"s_waitcnt lgkmcnt(8)\n\t"
"v_mfma_f32_16x16x32_bf16 %3, v[252:255], a[12:15], %3\n\t"
"v_mfma_f32_16x16x32_bf16 %7, v[252:255], a[76:79], %7\n\t"
"v_mfma_f32_16x16x32_bf16 %11, v[252:255], a[140:143], %11\n\t"
"v_mfma_f32_16x16x32_bf16 %15, v[252:255], a[204:207], %15\n\t"
"s_waitcnt vmcnt(44)\n\t"
"s_waitcnt lgkmcnt(6)\n\t"
"v_mfma_f32_16x16x32_bf16 %0, v[224:227], a[16:19], %0\n\t"
"ds_read_b64_tr_b16 v[240:241], %[ta0] offset:20480\n\t"
"v_mfma_f32_16x16x32_bf16 %4, v[224:227], a[80:83], %4\n\t"
"ds_read_b64_tr_b16 v[242:243], %[ta0] offset:22528\n\t"
"v_mfma_f32_16x16x32_bf16 %8, v[224:227], a[144:147], %8\n\t"
"ds_read_b64_tr_b16 v[244:245], %[ta1] offset:20480\n\t"
"v_mfma_f32_16x16x32_bf16 %12, v[224:227], a[208:211], %12\n\t"
"ds_read_b64_tr_b16 v[246:247], %[ta1] offset:22528\n\t"
"s_waitcnt lgkmcnt(8)\n\t"
"v_mfma_f32_16x16x32_bf16 %1, v[228:231], a[16:19], %1\n\t"
"ds_read_b64_tr_b16 v[248:249], %[ta2] offset:20480\n\t"
"v_mfma_f32_16x16x32_bf16 %5, v[228:231], a[80:83], %5\n\t"
"ds_read_b64_tr_b16 v[250:251], %[ta2] offset:22528\n\t"
"v_mfma_f32_16x16x32_bf16 %9, v[228:231], a[144:147], %9\n\t"
"ds_read_b64_tr_b16 v[252:253], %[ta3] offset:20480\n\t"
"v_mfma_f32_16x16x32_bf16 %13, v[228:231], a[208:211], %13\n\t"
"ds_read_b64_tr_b16 v[254:255], %[ta3] offset:22528\n\t"
"s_waitcnt lgkmcnt(10)\n\t"
"v_mfma_f32_16x16x32_bf16 %2, v[232:235], a[16:19], %2\n\t"
"v_mfma_f32_16x16x32_bf16 %6, v[232:235], a[80:83], %6\n\t"
"v_mfma_f32_16x16x32_bf16 %10, v[232:235], a[144:147], %10\n\t"
"v_mfma_f32_16x16x32_bf16 %14, v[232:235], a[208:211], %14\n\t"
"s_waitcnt lgkmcnt(8)\n\t"
"v_mfma_f32_16x16x32_bf16 %3, v[236:239], a[16:19], %3\n\t"
"v_mfma_f32_16x16x32_bf16 %7, v[236:239], a[80:83], %7\n\t"
"v_mfma_f32_16x16x32_bf16 %11, v[236:239], a[144:147], %11\n\t"
"v_mfma_f32_16x16x32_bf16 %15, v[236:239], a[208:211], %15\n\t"
"s_waitcnt vmcnt(40)\n\t"
"s_waitcnt lgkmcnt(6)\n\t"
"v_mfma_f32_16x16x32_bf16 %0, v[240:243], a[20:23], %0\n\t"
"ds_read_b64_tr_b16 v[224:225], %[ta0] offset:24576\n\t"
"v_mfma_f32_16x16x32_bf16 %4, v[240:243], a[84:87], %4\n\t"
"ds_read_b64_tr_b16 v[226:227], %[ta0] offset:26624\n\t"
"v_mfma_f32_16x16x32_bf16 %8, v[240:243], a[148:151], %8\n\t"
"ds_read_b64_tr_b16 v[228:229], %[ta1] offset:24576\n\t"
"v_mfma_f32_16x16x32_bf16 %12, v[240:243], a[212:215], %12\n\t"
"ds_read_b64_tr_b16 v[230:231], %[ta1] offset:26624\n\t"
"s_waitcnt lgkmcnt(8)\n\t"
"v_mfma_f32_16x16x32_bf16 %1, v[244:247], a[20:23], %1\n\t"
"ds_read_b64_tr_b16 v[232:233], %[ta2] offset:24576\n\t"
"v_mfma_f32_16x16x32_bf16 %5, v[244:247], a[84:87], %5\n\t"
"ds_read_b64_tr_b16 v[234:235], %[ta2] offset:26624\n\t"
"v_mfma_f32_16x16x32_bf16 %9, v[244:247], a[148:151], %9\n\t"
"ds_read_b64_tr_b16 v[236:237], %[ta3] offset:24576\n\t"
"v_mfma_f32_16x16x32_bf16 %13, v[244:247], a[212:215], %13\n\t"
"ds_read_b64_tr_b16 v[238:239], %[ta3] offset:26624\n\t"
"s_waitcnt lgkmcnt(10)\n\t"
"v_mfma_f32_16x16x32_bf16 %2, v[248:251], a[20:23], %2\n\t"
"v_mfma_f32_16x16x32_bf16 %6, v[248:251], a[84:87], %6\n\t"
"v_mfma_f32_16x16x32_bf16 %10, v[248:251], a[148:151], %10\n\t"
"v_mfma_f32_16x16x32_bf16 %14, v[248:251], a[212:215], %14\n\t"
"s_waitcnt lgkmcnt(8)\n\t"
"v_mfma_f32_16x16x32_bf16 %3, v[252:255], a[20:23], %3\n\t"
"v_mfma_f32_16x16x32_bf16 %7, v[252:255], a[84:87], %7\n\t"
"v_mfma_f32_16x16x32_bf16 %11, v[252:255], a[148:151], %11\n\t"
"v_mfma_f32_16x16x32_bf16 %15, v[252:255], a[212:215], %15\n\t"
"s_waitcnt vmcnt(36)\n\t"
"s_waitcnt lgkmcnt(6)\n\t"
"v_mfma_f32_16x16x32_bf16 %0, v[224:227], a[24:27], %0\n\t"
"ds_read_b64_tr_b16 v[240:241], %[ta0] offset:28672\n\t"
"v_mfma_f32_16x16x32_bf16 %4, v[224:227], a[88:91], %4\n\t"
"ds_read_b64_tr_b16 v[242:243], %[ta0] offset:30720\n\t"
"v_mfma_f32_16x16x32_bf16 %8, v[224:227], a[152:155], %8\n\t"
"ds_read_b64_tr_b16 v[244:245], %[ta1] offset:28672\n\t"
"v_mfma_f32_16x16x32_bf16 %12, v[224:227], a[216:219], %12\n\t"
"ds_read_b64_tr_b16 v[246:247], %[ta1] offset:30720\n\t"
"s_waitcnt lgkmcnt(8)\n\t"
"v_mfma_f32_16x16x32_bf16 %1, v[228:231], a[24:27], %1\n\t"
"ds_read_b64_tr_b16 v[248:249], %[ta2] offset:28672\n\t"
"v_mfma_f32_16x16x32_bf16 %5, v[228:231], a[88:91], %5\n\t"
"ds_read_b64_tr_b16 v[250:251], %[ta2] offset:30720\n\t"
"v_mfma_f32_16x16x32_bf16 %9, v[228:231], a[152:155], %9\n\t"
"ds_read_b64_tr_b16 v[252:253], %[ta3] offset:28672\n\t"
"v_mfma_f32_16x16x32_bf16 %13, v[228:231], a[216:219], %13\n\t"
"ds_read_b64_tr_b16 v[254:255], %[ta3] offset:30720\n\t"
"s_waitcnt lgkmcnt(10)\n\t"
"v_mfma_f32_16x16x32_bf16 %2, v[232:235], a[24:27], %2\n\t"
"v_mfma_f32_16x16x32_bf16 %6, v[232:235], a[88:91], %6\n\t"
"v_mfma_f32_16x16x32_bf16 %10, v[232:235], a[152:155], %10\n\t"
"v_mfma_f32_16x16x32_bf16 %14, v[232:235], a[216:219], %14\n\t"
"s_waitcnt lgkmcnt(8)\n\t"
"v_mfma_f32_16x16x32_bf16 %3, v[236:239], a[24:27], %3\n\t"
"v_mfma_f32_16x16x32_bf16 %7, v[236:239], a[88:91], %7\n\t"
"v_mfma_f32_16x16x32_bf16 %11, v[236:239], a[152:155], %11\n\t"
"v_mfma_f32_16x16x32_bf16 %15, v[236:239], a[216:219], %15\n\t"
"s_waitcnt vmcnt(32)\n\t"
"s_waitcnt lgkmcnt(6)\n\t"
"v_mfma_f32_16x16x32_bf16 %0, v[240:243], a[28:31], %0\n\t"
"ds_read_b64_tr_b16 v[224:225], %[ta0] offset:32768\n\t"
"v_mfma_f32_16x16x32_bf16 %4, v[240:243], a[92:95], %4\n\t"
"ds_read_b64_tr_b16 v[226:227], %[ta0] offset:34816\n\t"
"v_mfma_f32_16x16x32_bf16 %8, v[240:243], a[156:159], %8\n\t"
"ds_read_b64_tr_b16 v[228:229], %[ta1] offset:32768\n\t"
"v_mfma_f32_16x16x32_bf16 %12, v[240:243], a[220:223], %12\n\t"
"ds_read_b64_tr_b16 v[230:231], %[ta1] offset:34816\n\t"
"s_waitcnt lgkmcnt(8)\n\t"
"v_mfma_f32_16x16x32_bf16 %1, v[244:247], a[28:31], %1\n\t"
"ds_read_b64_tr_b16 v[232:233], %[ta2] offset:32768\n\t"
"v_mfma_f32_16x16x32_bf16 %5, v[244:247], a[92:95], %5\n\t"
"ds_read_b64_tr_b16 v[234:235], %[ta2] offset:34816\n\t"
"v_mfma_f32_16x16x32_bf16 %9, v[244:247], a[156:159], %9\n\t"
"ds_read_b64_tr_b16 v[236:237], %[ta3] offset:32768\n\t"
"v_mfma_f32_16x16x32_bf16 %13, v[244:247], a[220:223], %13\n\t"
"ds_read_b64_tr_b16 v[238:239], %[ta3] offset:34816\n\t"
"s_waitcnt lgkmcnt(10)\n\t"
"v_mfma_f32_16x16x32_bf16 %2, v[248:251], a[28:31], %2\n\t"
"v_mfma_f32_16x16x32_bf16 %6, v[248:251], a[92:95], %6\n\t"
"v_mfma_f32_16x16x32_bf16 %10, v[248:251], a[156:159], %10\n\t"
"v_mfma_f32_16x16x32_bf16 %14, v[248:251], a[220:223], %14\n\t"
"s_waitcnt lgkmcnt(8)\n\t"
"v_mfma_f32_16x16x32_bf16 %3, v[252:255], a[28:31], %3\n\t"
"v_mfma_f32_16x16x32_bf16 %7, v[252:255], a[92:95], %7\n\t"
"v_mfma_f32_16x16x32_bf16 %11, v[252:255], a[156:159], %11\n\t"
"v_mfma_f32_16x16x32_bf16 %15, v[252:255], a[220:223], %15\n\t"
"s_waitcnt vmcnt(28)\n\t"
"s_waitcnt lgkmcnt(6)\n\t"
"v_mfma_f32_16x16x32_bf16 %0, v[224:227], a[32:35], %0\n\t"
"ds_read_b64_tr_b16 v[240:241], %[ta0] offset:36864\n\t"
"v_mfma_f32_16x16x32_bf16 %4, v[224:227], a[96:99], %4\n\t"
"ds_read_b64_tr_b16 v[242:243], %[ta0] offset:38912\n\t"
"v_mfma_f32_16x16x32_bf16 %8, v[224:227], a[160:163], %8\n\t"
"ds_read_b64_tr_b16 v[244:245], %[ta1] offset:36864\n\t"
"v_mfma_f32_16x16x32_bf16 %12, v[224:227], a[224:227], %12\n\t"
"ds_read_b64_tr_b16 v[246:247], %[ta1] offset:38912\n\t"
"s_waitcnt lgkmcnt(8)\n\t"
"v_mfma_f32_16x16x32_bf16 %1, v[228:231], a[32:35], %1\n\t"
"ds_read_b64_tr_b16 v[248:249], %[ta2] offset:36864\n\t"
"v_mfma_f32_16x16x32_bf16 %5, v[228:231], a[96:99], %5\n\t"
"ds_read_b64_tr_b16 v[250:251], %[ta2] offset:38912\n\t"
"v_mfma_f32_16x16x32_bf16 %9, v[228:231], a[160:163], %9\n\t"
"ds_read_b64_tr_b16 v[252:253], %[ta3] offset:36864\n\t"
"v_mfma_f32_16x16x32_bf16 %13, v[228:231], a[224:227], %13\n\t"
"ds_read_b64_tr_b16 v[254:255], %[ta3] offset:38912\n\t"
"s_waitcnt lgkmcnt(10)\n\t"
"v_mfma_f32_16x16x32_bf16 %2, v[232:235], a[32:35], %2\n\t"
"v_mfma_f32_16x16x32_bf16 %6, v[232:235], a[96:99], %6\n\t"
"v_mfma_f32_16x16x32_bf16 %10, v[232:235], a[160:163], %10\n\t"
"v_mfma_f32_16x16x32_bf16 %14, v[232:235], a[224:227], %14\n\t"
"s_waitcnt lgkmcnt(8)\n\t"
"v_mfma_f32_16x16x32_bf16 %3, v[236:239], a[32:35], %3\n\t"
"v_mfma_f32_16x16x32_bf16 %7, v[236:239], a[96:99], %7\n\t"
"v_mfma_f32_16x16x32_bf16 %11, v[236:239], a[160:163], %11\n\t"
"v_mfma_f32_16x16x32_bf16 %15, v[236:239], a[224:227], %15\n\t"
"s_waitcnt vmcnt(24)\n\t"
"s_waitcnt lgkmcnt(6)\n\t"
"v_mfma_f32_16x16x32_bf16 %0, v[240:243], a[36:39], %0\n\t"
"ds_read_b64_tr_b16 v[224:225], %[ta0] offset:40960\n\t"
"v_mfma_f32_16x16x32_bf16 %4, v[240:243], a[100:103], %4\n\t"
"ds_read_b64_tr_b16 v[226:227], %[ta0] offset:43008\n\t"
"v_mfma_f32_16x16x32_bf16 %8, v[240:243], a[164:167], %8\n\t"
"ds_read_b64_tr_b16 v[228:229], %[ta1] offset:40960\n\t"
"v_mfma_f32_16x16x32_bf16 %12, v[240:243], a[228:231], %12\n\t"
"ds_read_b64_tr_b16 v[230:231], %[ta1] offset:43008\n\t"
"s_waitcnt lgkmcnt(8)\n\t"
"v_mfma_f32_16x16x32_bf16 %1, v[244:247], a[36:39], %1\n\t"
"ds_read_b64_tr_b16 v[232:233], %[ta2] offset:40960\n\t"
"v_mfma_f32_16x16x32_bf16 %5, v[244:247], a[100:103], %5\n\t"
"ds_read_b64_tr_b16 v[234:235], %[ta2] offset:43008\n\t"
"v_mfma_f32_16x16x32_bf16 %9, v[244:247], a[164:167], %9\n\t"
"ds_read_b64_tr_b16 v[236:237], %[ta3] offset:40960\n\t"
"v_mfma_f32_16x16x32_bf16 %13, v[244:247], a[228:231], %13\n\t"
"ds_read_b64_tr_b16 v[238:239], %[ta3] offset:43008\n\t"
"s_waitcnt lgkmcnt(10)\n\t"
"v_mfma_f32_16x16x32_bf16 %2, v[248:251], a[36:39], %2\n\t"
"v_mfma_f32_16x16x32_bf16 %6, v[248:251], a[100:103], %6\n\t"
"v_mfma_f32_16x16x32_bf16 %10, v[248:251], a[164:167], %10\n\t"
"v_mfma_f32_16x16x32_bf16 %14, v[248:251], a[228:231], %14\n\t"
"s_waitcnt lgkmcnt(8)\n\t"
"v_mfma_f32_16x16x32_bf16 %3, v[252:255], a[36:39], %3\n\t"
"v_mfma_f32_16x16x32_bf16 %7, v[252:255], a[100:103], %7\n\t"
"v_mfma_f32_16x16x32_bf16 %11, v[252:255], a[164:167], %11\n\t"
"v_mfma_f32_16x16x32_bf16 %15, v[252:255], a[228:231], %15\n\t"
"s_waitcnt vmcnt(20)\n\t"
"s_waitcnt lgkmcnt(6)\n\t"
"v_mfma_f32_16x16x32_bf16 %0, v[224:227], a[40:43], %0\n\t"
"ds_read_b64_tr_b16 v[240:241], %[ta0] offset:45056\n\t"
"v_mfma_f32_16x16x32_bf16 %4, v[224:227], a[104:107], %4\n\t"
"ds_read_b64_tr_b16 v[242:243], %[ta0] offset:47104\n\t"
"v_mfma_f32_16x16x32_bf16 %8, v[224:227], a[168:171], %8\n\t"
"ds_read_b64_tr_b16 v[244:245], %[ta1] offset:45056\n\t"
"v_mfma_f32_16x16x32_bf16 %12, v[224:227], a[232:235], %12\n\t"
"ds_read_b64_tr_b16 v[246:247], %[ta1] offset:47104\n\t"
"s_waitcnt lgkmcnt(8)\n\t"
"v_mfma_f32_16x16x32_bf16 %1, v[228:231], a[40:43], %1\n\t"
"ds_read_b64_tr_b16 v[248:249], %[ta2] offset:45056\n\t"
"v_mfma_f32_16x16x32_bf16 %5, v[228:231], a[104:107], %5\n\t"
"ds_read_b64_tr_b16 v[250:251], %[ta2] offset:47104\n\t"
"v_mfma_f32_16x16x32_bf16 %9, v[228:231], a[168:171], %9\n\t"
"ds_read_b64_tr_b16 v[252:253], %[ta3] offset:45056\n\t"
"v_mfma_f32_16x16x32_bf16 %13, v[228:231], a[232:235], %13\n\t"
"ds_read_b64_tr_b16 v[254:255], %[ta3] offset:47104\n\t"
"s_waitcnt lgkmcnt(10)\n\t"
"v_mfma_f32_16x16x32_bf16 %2, v[232:235], a[40:43], %2\n\t"
"v_mfma_f32_16x16x32_bf16 %6, v[232:235], a[104:107], %6\n\t"
"v_mfma_f32_16x16x32_bf16 %10, v[232:235], a[168:171], %10\n\t"
"v_mfma_f32_16x16x32_bf16 %14, v[232:235], a[232:235], %14\n\t"
"s_waitcnt lgkmcnt(8)\n\t"
"v_mfma_f32_16x16x32_bf16 %3, v[236:239], a[40:43], %3\n\t"
"v_mfma_f32_16x16x32_bf16 %7, v[236:239], a[104:107], %7\n\t"
"v_mfma_f32_16x16x32_bf16 %11, v[236:239], a[168:171], %11\n\t"
"v_mfma_f32_16x16x32_bf16 %15, v[236:239], a[232:235], %15\n\t"
"s_waitcnt vmcnt(16)\n\t"
"s_waitcnt lgkmcnt(6)\n\t"
"v_mfma_f32_16x16x32_bf16 %0, v[240:243], a[44:47], %0\n\t"
"ds_read_b64_tr_b16 v[224:225], %[ta0] offset:49152\n\t"
"v_mfma_f32_16x16x32_bf16 %4, v[240:243], a[108:111], %4\n\t"
"ds_read_b64_tr_b16 v[226:227], %[ta0] offset:51200\n\t"
"v_mfma_f32_16x16x32_bf16 %8, v[240:243], a[172:175], %8\n\t"
"ds_read_b64_tr_b16 v[228:229], %[ta1] offset:49152\n\t"
"v_mfma_f32_16x16x32_bf16 %12, v[240:243], a[236:239], %12\n\t"
"ds_read_b64_tr_b16 v[230:231], %[ta1] offset:51200\n\t"
"s_waitcnt lgkmcnt(8)\n\t"
"v_mfma_f32_16x16x32_bf16 %1, v[244:247], a[44:47], %1\n\t"
"ds_read_b64_tr_b16 v[232:233], %[ta2] offset:49152\n\t"
"v_mfma_f32_16x16x32_bf16 %5, v[244:247], a[108:111], %5\n\t"
"ds_read_b64_tr_b16 v[234:235], %[ta2] offset:51200\n\t"
"v_mfma_f32_16x16x32_bf16 %9, v[244:247], a[172:175], %9\n\t"
"ds_read_b64_tr_b16 v[236:237], %[ta3] offset:49152\n\t"
"v_mfma_f32_16x16x32_bf16 %13, v[244:247], a[236:239], %13\n\t"
"ds_read_b64_tr_b16 v[238:239], %[ta3] offset:51200\n\t"
"s_waitcnt lgkmcnt(10)\n\t"
"v_mfma_f32_16x16x32_bf16 %2, v[248:251], a[44:47], %2\n\t"
"v_mfma_f32_16x16x32_bf16 %6, v[248:251], a[108:111], %6\n\t"
"v_mfma_f32_16x16x32_bf16 %10, v[248:251], a[172:175], %10\n\t"
"v_mfma_f32_16x16x32_bf16 %14, v[248:251], a[236:239], %14\n\t"
"s_waitcnt lgkmcnt(8)\n\t"
"v_mfma_f32_16x16x32_bf16 %3, v[252:255], a[44:47], %3\n\t"
"v_mfma_f32_16x16x32_bf16 %7, v[252:255], a[108:111], %7\n\t"
"v_mfma_f32_16x16x32_bf16 %11, v[252:255], a[172:175], %11\n\t"
"v_mfma_f32_16x16x32_bf16 %15, v[252:255], a[236:239], %15\n\t"
"s_waitcnt vmcnt(12)\n\t"
"s_waitcnt lgkmcnt(6)\n\t"
"v_mfma_f32_16x16x32_bf16 %0, v[224:227], a[48:51], %0\n\t"
"ds_read_b64_tr_b16 v[240:241], %[ta0] offset:53248\n\t"
"v_mfma_f32_16x16x32_bf16 %4, v[224:227], a[112:115], %4\n\t"
"ds_read_b64_tr_b16 v[242:243], %[ta0] offset:55296\n\t"
"v_mfma_f32_16x16x32_bf16 %8, v[224:227], a[176:179], %8\n\t"
"ds_read_b64_tr_b16 v[244:245], %[ta1] offset:53248\n\t"
"v_mfma_f32_16x16x32_bf16 %12, v[224:227], a[240:243], %12\n\t"
"ds_read_b64_tr_b16 v[246:247], %[ta1] offset:55296\n\t"
"s_waitcnt lgkmcnt(8)\n\t"
"v_mfma_f32_16x16x32_bf16 %1, v[228:231], a[48:51], %1\n\t"
"ds_read_b64_tr_b16 v[248:249], %[ta2] offset:53248\n\t"
"v_mfma_f32_16x16x32_bf16 %5, v[228:231], a[112:115], %5\n\t"
"ds_read_b64_tr_b16 v[250:251], %[ta2] offset:55296\n\t"
"v_mfma_f32_16x16x32_bf16 %9, v[228:231], a[176:179], %9\n\t"
"ds_read_b64_tr_b16 v[252:253], %[ta3] offset:53248\n\t"
"v_mfma_f32_16x16x32_bf16 %13, v[228:231], a[240:243], %13\n\t"
"ds_read_b64_tr_b16 v[254:255], %[ta3] offset:55296\n\t"
"s_waitcnt lgkmcnt(10)\n\t"
"v_mfma_f32_16x16x32_bf16 %2, v[232:235], a[48:51], %2\n\t"
"v_mfma_f32_16x16x32_bf16 %6, v[232:235], a[112:115], %6\n\t"
"v_mfma_f32_16x16x32_bf16 %10, v[232:235], a[176:179], %10\n\t"
"v_mfma_f32_16x16x32_bf16 %14, v[232:235], a[240:243], %14\n\t"
"s_waitcnt lgkmcnt(8)\n\t"
"v_mfma_f32_16x16x32_bf16 %3, v[236:239], a[48:51], %3\n\t"
"v_mfma_f32_16x16x32_bf16 %7, v[236:239], a[112:115], %7\n\t"
"v_mfma_f32_16x16x32_bf16 %11, v[236:239], a[176:179], %11\n\t"
"v_mfma_f32_16x16x32_bf16 %15, v[236:239], a[240:243], %15\n\t"
"s_waitcnt vmcnt(8)\n\t"
"s_waitcnt lgkmcnt(6)\n\t"
"v_mfma_f32_16x16x32_bf16 %0, v[240:243], a[52:55], %0\n\t"
"ds_read_b64_tr_b16 v[224:225], %[ta0] offset:57344\n\t"
"v_mfma_f32_16x16x32_bf16 %4, v[240:243], a[116:119], %4\n\t"
"ds_read_b64_tr_b16 v[226:227], %[ta0] offset:59392\n\t"
"v_mfma_f32_16x16x32_bf16 %8, v[240:243], a[180:183], %8\n\t"
"ds_read_b64_tr_b16 v[228:229], %[ta1] offset:57344\n\t"
"v_mfma_f32_16x16x32_bf16 %12, v[240:243], a[244:247], %12\n\t"
"ds_read_b64_tr_b16 v[230:231], %[ta1] offset:59392\n\t"
"s_waitcnt lgkmcnt(8)\n\t"
"v_mfma_f32_16x16x32_bf16 %1, v[244:247], a[52:55], %1\n\t"
"ds_read_b64_tr_b16 v[232:233], %[ta2] offset:57344\n\t"
"v_mfma_f32_16x16x32_bf16 %5, v[244:247], a[116:119], %5\n\t"
"ds_read_b64_tr_b16 v[234:235], %[ta2] offset:59392\n\t"
"v_mfma_f32_16x16x32_bf16 %9, v[244:247], a[180:183], %9\n\t"
"ds_read_b64_tr_b16 v[236:237], %[ta3] offset:57344\n\t"
"v_mfma_f32_16x16x32_bf16 %13, v[244:247], a[244:247], %13\n\t"
"ds_read_b64_tr_b16 v[238:239], %[ta3] offset:59392\n\t"
"s_waitcnt lgkmcnt(10)\n\t"
"v_mfma_f32_16x16x32_bf16 %2, v[248:251], a[52:55], %2\n\t"
"v_mfma_f32_16x16x32_bf16 %6, v[248:251], a[116:119], %6\n\t"
"v_mfma_f32_16x16x32_bf16 %10, v[248:251], a[180:183], %10\n\t"
"v_mfma_f32_16x16x32_bf16 %14, v[248:251], a[244:247], %14\n\t"
"s_waitcnt lgkmcnt(8)\n\t"
"v_mfma_f32_16x16x32_bf16 %3, v[252:255], a[52:55], %3\n\t"
"v_mfma_f32_16x16x32_bf16 %7, v[252:255], a[116:119], %7\n\t"
"v_mfma_f32_16x16x32_bf16 %11, v[252:255], a[180:183], %11\n\t"
"v_mfma_f32_16x16x32_bf16 %15, v[252:255], a[244:247], %15\n\t"
"s_waitcnt vmcnt(4)\n\t"
"s_waitcnt lgkmcnt(6)\n\t"
"v_mfma_f32_16x16x32_bf16 %0, v[224:227], a[56:59], %0\n\t"
"ds_read_b64_tr_b16 v[240:241], %[ta0] offset:61440\n\t"
"v_mfma_f32_16x16x32_bf16 %4, v[224:227], a[120:123], %4\n\t"
"ds_read_b64_tr_b16 v[242:243], %[ta0] offset:63488\n\t"
"v_mfma_f32_16x16x32_bf16 %8, v[224:227], a[184:187], %8\n\t"
"ds_read_b64_tr_b16 v[244:245], %[ta1] offset:61440\n\t"
"v_mfma_f32_16x16x32_bf16 %12, v[224:227], a[248:251], %12\n\t"
"ds_read_b64_tr_b16 v[246:247], %[ta1] offset:63488\n\t"
"s_waitcnt lgkmcnt(8)\n\t"
"v_mfma_f32_16x16x32_bf16 %1, v[228:231], a[56:59], %1\n\t"
"ds_read_b64_tr_b16 v[248:249], %[ta2] offset:61440\n\t"
"v_mfma_f32_16x16x32_bf16 %5, v[228:231], a[120:123], %5\n\t"
"ds_read_b64_tr_b16 v[250:251], %[ta2] offset:63488\n\t"
"v_mfma_f32_16x16x32_bf16 %9, v[228:231], a[184:187], %9\n\t"
"ds_read_b64_tr_b16 v[252:253], %[ta3] offset:61440\n\t"
"v_mfma_f32_16x16x32_bf16 %13, v[228:231], a[248:251], %13\n\t"
"ds_read_b64_tr_b16 v[254:255], %[ta3] offset:63488\n\t"
"s_waitcnt lgkmcnt(10)\n\t"
"v_mfma_f32_16x16x32_bf16 %2, v[232:235], a[56:59], %2\n\t"
"v_mfma_f32_16x16x32_bf16 %6, v[232:235], a[120:123], %6\n\t"
"v_mfma_f32_16x16x32_bf16 %10, v[232:235], a[184:187], %10\n\t"
"v_mfma_f32_16x16x32_bf16 %14, v[232:235], a[248:251], %14\n\t"
"s_waitcnt lgkmcnt(8)\n\t"
"v_mfma_f32_16x16x32_bf16 %3, v[236:239], a[56:59], %3\n\t"
"v_mfma_f32_16x16x32_bf16 %7, v[236:239], a[120:123], %7\n\t"
"v_mfma_f32_16x16x32_bf16 %11, v[236:239], a[184:187], %11\n\t"
"v_mfma_f32_16x16x32_bf16 %15, v[236:239], a[248:251], %15\n\t"
"s_waitcnt vmcnt(0)\n\t"
"s_waitcnt lgkmcnt(6)\n\t"
"v_mfma_f32_16x16x32_bf16 %0, v[240:243], a[60:63], %0\n\t"
"v_mfma_f32_16x16x32_bf16 %4, v[240:243], a[124:127], %4\n\t"
"v_mfma_f32_16x16x32_bf16 %8, v[240:243], a[188:191], %8\n\t"
"v_mfma_f32_16x16x32_bf16 %12, v[240:243], a[252:255], %12\n\t"
"s_waitcnt lgkmcnt(4)\n\t"
"v_mfma_f32_16x16x32_bf16 %1, v[244:247], a[60:63], %1\n\t"
"v_mfma_f32_16x16x32_bf16 %5, v[244:247], a[124:127], %5\n\t"
"v_mfma_f32_16x16x32_bf16 %9, v[244:247], a[188:191], %9\n\t"
"v_mfma_f32_16x16x32_bf16 %13, v[244:247], a[252:255], %13\n\t"
"s_waitcnt lgkmcnt(2)\n\t"
"v_mfma_f32_16x16x32_bf16 %2, v[248:251], a[60:63], %2\n\t"
"v_mfma_f32_16x16x32_bf16 %6, v[248:251], a[124:127], %6\n\t"
"s_mov_b64 exec, 1\n\t"
"global_atomic_add %[z], %[add], %[flag]\n\t"
"s_mov_b64 exec, -1\n\t"
"v_mfma_f32_16x16x32_bf16 %10, v[248:251], a[188:191], %10\n\t"
"v_mfma_f32_16x16x32_bf16 %14, v[248:251], a[252:255], %14\n\t"
"s_waitcnt lgkmcnt(0)\n\t"
"v_mfma_f32_16x16x32_bf16 %3, v[252:255], a[60:63], %3\n\t"
"v_mfma_f32_16x16x32_bf16 %7, v[252:255], a[124:127], %7\n\t"
"v_mfma_f32_16x16x32_bf16 %11, v[252:255], a[188:191], %11\n\t"
"v_mfma_f32_16x16x32_bf16 %15, v[252:255], a[252:255], %15\n\t"
"s_nop 7\n\t"
    GEMM_OPS);
  else asm volatile(
"s_waitcnt lgkmcnt(0)\n\t"
"ds_read_b64_tr_b16 v[224:225], %[ta0] offset:0\n\t"
"ds_read_b64_tr_b16 v[226:227], %[ta0] offset:2048\n\t"
"ds_read_b64_tr_b16 v[228:229], %[ta1] offset:0\n\t"
"ds_read_b64_tr_b16 v[230:231], %[ta1] offset:2048\n\t"
"ds_read_b64_tr_b16 v[232:233], %[ta2] offset:0\n\t"
"ds_read_b64_tr_b16 v[234:235], %[ta2] offset:2048\n\t"
"ds_read_b64_tr_b16 v[236:237], %[ta3] offset:0\n\t"
"ds_read_b64_tr_b16 v[238:239], %[ta3] offset:2048\n\t"
"s_waitcnt lgkmcnt(6)\n\t"
"v_mfma_f32_16x16x32_bf16 %0, v[224:227], a[0:3], 0\n\t"
"ds_read_b64_tr_b16 v[240:241], %[ta0] offset:4096\n\t"
"v_mfma_f32_16x16x32_bf16 %4, v[224:227], a[64:67], 0\n\t"
"ds_read_b64_tr_b16 v[242:243], %[ta0] offset:6144\n\t"
"v_mfma_f32_16x16x32_bf16 %8, v[224:227], a[128:131], 0\n\t"
"ds_read_b64_tr_b16 v[244:245], %[ta1] offset:4096\n\t"
"v_mfma_f32_16x16x32_bf16 %12, v[224:227], a[192:195], 0\n\t"
"ds_read_b64_tr_b16 v[246:247], %[ta1] offset:6144\n\t"
"s_waitcnt lgkmcnt(8)\n\t"
"v_mfma_f32_16x16x32_bf16 %1, v[228:231], a[0:3], 0\n\t"
"ds_read_b64_tr_b16 v[248:249], %[ta2] offset:4096\n\t"
"v_mfma_f32_16x16x32_bf16 %5, v[228:231], a[64:67], 0\n\t"
"ds_read_b64_tr_b16 v[250:251], %[ta2] offset:6144\n\t"
"v_mfma_f32_16x16x32_bf16 %9, v[228:231], a[128:131], 0\n\t"
"ds_read_b64_tr_b16 v[252:253], %[ta3] offset:4096\n\t"
"v_mfma_f32_16x16x32_bf16 %13, v[228:231], a[192:195], 0\n\t"
"ds_read_b64_tr_b16 v[254:255], %[ta3] offset:6144\n\t"
"s_waitcnt lgkmcnt(10)\n\t"
"v_mfma_f32_16x16x32_bf16 %2, v[232:235], a[0:3], 0\n\t"
"v_mfma_f32_16x16x32_bf16 %6, v[232:235], a[64:67], 0\n\t"
"v_mfma_f32_16x16x32_bf16 %10, v[232:235], a[128:131], 0\n\t"
"v_mfma_f32_16x16x32_bf16 %14, v[232:235], a[192:195], 0\n\t"
"s_waitcnt lgkmcnt(8)\n\t"
"v_mfma_f32_16x16x32_bf16 %3, v[236:239], a[0:3], 0\n\t"
"v_mfma_f32_16x16x32_bf16 %7, v[236:239], a[64:67], 0\n\t"
"v_mfma_f32_16x16x32_bf16 %11, v[236:239], a[128:131], 0\n\t"
"v_mfma_f32_16x16x32_bf16 %15, v[236:239], a[192:195], 0\n\t"
"s_waitcnt lgkmcnt(6)\n\t"
"v_mfma_f32_16x16x32_bf16 %0, v[240:243], a[4:7], %0\n\t"
"ds_read_b64_tr_b16 v[224:225], %[ta0] offset:8192\n\t"
"v_mfma_f32_16x16x32_bf16 %4, v[240:243], a[68:71], %4\n\t"
"ds_read_b64_tr_b16 v[226:227], %[ta0] offset:10240\n\t"
"v_mfma_f32_16x16x32_bf16 %8, v[240:243], a[132:135], %8\n\t"
"ds_read_b64_tr_b16 v[228:229], %[ta1] offset:8192\n\t"
"v_mfma_f32_16x16x32_bf16 %12, v[240:243], a[196:199], %12\n\t"
"ds_read_b64_tr_b16 v[230:231], %[ta1] offset:10240\n\t"
"s_waitcnt lgkmcnt(8)\n\t"
"v_mfma_f32_16x16x32_bf16 %1, v[244:247], a[4:7], %1\n\t"
"ds_read_b64_tr_b16 v[232:233], %[ta2] offset:8192\n\t"
"v_mfma_f32_16x16x32_bf16 %5, v[244:247], a[68:71], %5\n\t"
"ds_read_b64_tr_b16 v[234:235], %[ta2] offset:10240\n\t"
"v_mfma_f32_16x16x32_bf16 %9, v[244:247], a[132:135], %9\n\t"
"ds_read_b64_tr_b16 v[236:237], %[ta3] offset:8192\n\t"
"v_mfma_f32_16x16x32_bf16 %13, v[244:247], a[196:199], %13\n\t"
"ds_read_b64_tr_b16 v[238:239], %[ta3] offset:10240\n\t"
"s_waitcnt lgkmcnt(10)\n\t"
"v_mfma_f32_16x16x32_bf16 %2, v[248:251], a[4:7], %2\n\t"
"s_add_u32 %[st], %[so], 0\n\t"
"buffer_load_dwordx4 a[0:3], %[vo], %[rs], %[st] offen offset:0 sc1\n\t"
"v_mfma_f32_16x16x32_bf16 %6, v[248:251], a[68:71], %6\n\t"
"v_mfma_f32_16x16x32_bf16 %10, v[248:251], a[132:135], %10\n\t"
"s_add_u32 %[st], %[so], 16384\n\t"
"buffer_load_dwordx4 a[64:67], %[vo], %[rs], %[st] offen offset:0 sc1\n\t"
"v_mfma_f32_16x16x32_bf16 %14, v[248:251], a[196:199], %14\n\t"
"s_waitcnt lgkmcnt(8)\n\t"
"v_mfma_f32_16x16x32_bf16 %3, v[252:255], a[4:7], %3\n\t"
"s_add_u32 %[st], %[so], 32768\n\t"
"buffer_load_dwordx4 a[128:131], %[vo], %[rs], %[st] offen offset:0 sc1\n\t"
"v_mfma_f32_16x16x32_bf16 %7, v[252:255], a[68:71], %7\n\t"
"v_mfma_f32_16x16x32_bf16 %11, v[252:255], a[132:135], %11\n\t"
"s_add_u32 %[st], %[so], 49152\n\t"
"buffer_load_dwordx4 a[192:195], %[vo], %[rs], %[st] offen offset:0 sc1\n\t"
"v_mfma_f32_16x16x32_bf16 %15, v[252:255], a[196:199], %15\n\t"
"s_waitcnt lgkmcnt(6)\n\t"
"v_mfma_f32_16x16x32_bf16 %0, v[224:227], a[8:11], %0\n\t"
"ds_read_b64_tr_b16 v[240:241], %[ta0] offset:12288\n\t"
"v_mfma_f32_16x16x32_bf16 %4, v[224:227], a[72:75], %4\n\t"
"ds_read_b64_tr_b16 v[242:243], %[ta0] offset:14336\n\t"
"v_mfma_f32_16x16x32_bf16 %8, v[224:227], a[136:139], %8\n\t"
"ds_read_b64_tr_b16 v[244:245], %[ta1] offset:12288\n\t"
"v_mfma_f32_16x16x32_bf16 %12, v[224:227], a[200:203], %12\n\t"
"ds_read_b64_tr_b16 v[246:247], %[ta1] offset:14336\n\t"
"s_waitcnt lgkmcnt(8)\n\t"
"v_mfma_f32_16x16x32_bf16 %1, v[228:231], a[8:11], %1\n\t"
"ds_read_b64_tr_b16 v[248:249], %[ta2] offset:12288\n\t"
"v_mfma_f32_16x16x32_bf16 %5, v[228:231], a[72:75], %5\n\t"
"ds_read_b64_tr_b16 v[250:251], %[ta2] offset:14336\n\t"
"v_mfma_f32_16x16x32_bf16 %9, v[228:231], a[136:139], %9\n\t"
"ds_read_b64_tr_b16 v[252:253], %[ta3] offset:12288\n\t"
"v_mfma_f32_16x16x32_bf16 %13, v[228:231], a[200:203], %13\n\t"
"ds_read_b64_tr_b16 v[254:255], %[ta3] offset:14336\n\t"
"s_waitcnt lgkmcnt(10)\n\t"
"v_mfma_f32_16x16x32_bf16 %2, v[232:235], a[8:11], %2\n\t"
"s_add_u32 %[st], %[so], 0\n\t"
"buffer_load_dwordx4 a[4:7], %[vo], %[rs], %[st] offen offset:1024 sc1\n\t"
"v_mfma_f32_16x16x32_bf16 %6, v[232:235], a[72:75], %6\n\t"
"v_mfma_f32_16x16x32_bf16 %10, v[232:235], a[136:139], %10\n\t"
"s_add_u32 %[st], %[so], 16384\n\t"
"buffer_load_dwordx4 a[68:71], %[vo], %[rs], %[st] offen offset:1024 sc1\n\t"
"v_mfma_f32_16x16x32_bf16 %14, v[232:235], a[200:203], %14\n\t"
"s_waitcnt lgkmcnt(8)\n\t"
"v_mfma_f32_16x16x32_bf16 %3, v[236:239], a[8:11], %3\n\t"
"s_add_u32 %[st], %[so], 32768\n\t"
"buffer_load_dwordx4 a[132:135], %[vo], %[rs], %[st] offen offset:1024 sc1\n\t"
"v_mfma_f32_16x16x32_bf16 %7, v[236:239], a[72:75], %7\n\t"
"v_mfma_f32_16x16x32_bf16 %11, v[236:239], a[136:139], %11\n\t"
"s_add_u32 %[st], %[so], 49152\n\t"
"buffer_load_dwordx4 a[196:199], %[vo], %[rs], %[st] offen offset:1024 sc1\n\t"
"v_mfma_f32_16x16x32_bf16 %15, v[236:239], a[200:203], %15\n\t"
"s_waitcnt lgkmcnt(6)\n\t"
"v_mfma_f32_16x16x32_bf16 %0, v[240:243], a[12:15], %0\n\t"
"ds_read_b64_tr_b16 v[224:225], %[ta0] offset:16384\n\t"
"v_mfma_f32_16x16x32_bf16 %4, v[240:243], a[76:79], %4\n\t"
"ds_read_b64_tr_b16 v[226:227], %[ta0] offset:18432\n\t"
"v_mfma_f32_16x16x32_bf16 %8, v[240:243], a[140:143], %8\n\t"
"ds_read_b64_tr_b16 v[228:229], %[ta1] offset:16384\n\t"
"v_mfma_f32_16x16x32_bf16 %12, v[240:243], a[204:207], %12\n\t"
"ds_read_b64_tr_b16 v[230:231], %[ta1] offset:18432\n\t"
"s_waitcnt lgkmcnt(8)\n\t"
"v_mfma_f32_16x16x32_bf16 %1, v[244:247], a[12:15], %1\n\t"
"ds_read_b64_tr_b16 v[232:233], %[ta2] offset:16384\n\t"
"v_mfma_f32_16x16x32_bf16 %5, v[244:247], a[76:79], %5\n\t"
"ds_read_b64_tr_b16 v[234:235], %[ta2] offset:18432\n\t"
"v_mfma_f32_16x16x32_bf16 %9, v[244:247], a[140:143], %9\n\t"
"ds_read_b64_tr_b16 v[236:237], %[ta3] offset:16384\n\t"
"v_mfma_f32_16x16x32_bf16 %13, v[244:247], a[204:207], %13\n\t"
"ds_read_b64_tr_b16 v[238:239], %[ta3] offset:18432\n\t"
"s_waitcnt lgkmcnt(10)\n\t"
"v_mfma_f32_16x16x32_bf16 %2, v[248:251], a[12:15], %2\n\t"
"s_add_u32 %[st], %[so], 0\n\t"
"buffer_load_dwordx4 a[8:11], %[vo], %[rs], %[st] offen offset:2048 sc1\n\t"
"v_mfma_f32_16x16x32_bf16 %6, v[248:251], a[76:79], %6\n\t"
"v_mfma_f32_16x16x32_bf16 %10, v[248:251], a[140:143], %10\n\t"
"s_add_u32 %[st], %[so], 16384\n\t"
"buffer_load_dwordx4 a[72:75], %[vo], %[rs], %[st] offen offset:2048 sc1\n\t"
"v_mfma_f32_16x16x32_bf16 %14, v[248:251], a[204:207], %14\n\t"
"s_waitcnt lgkmcnt(8)\n\t"
"v_mfma_f32_16x16x32_bf16 %3, v[252:255], a[12:15], %3\n\t"
"s_add_u32 %[st], %[so], 32768\n\t"
"buffer_load_dwordx4 a[136:139], %[vo], %[rs], %[st] offen offset:2048 sc1\n\t"
"v_mfma_f32_16x16x32_bf16 %7, v[252:255], a[76:79], %7\n\t"
"v_mfma_f32_16x16x32_bf16 %11, v[252:255], a[140:143], %11\n\t"
"s_add_u32 %[st], %[so], 49152\n\t"
"buffer_load_dwordx4 a[200:203], %[vo], %[rs], %[st] offen offset:2048 sc1\n\t"
"v_mfma_f32_16x16x32_bf16 %15, v[252:255], a[204:207], %15\n\t"
"s_waitcnt lgkmcnt(6)\n\t"
"v_mfma_f32_16x16x32_bf16 %0, v[224:227], a[16:19], %0\n\t"
"ds_read_b64_tr_b16 v[240:241], %[ta0] offset:20480\n\t"
"v_mfma_f32_16x16x32_bf16 %4, v[224:227], a[80:83], %4\n\t"
"ds_read_b64_tr_b16 v[242:243], %[ta0] offset:22528\n\t"
"v_mfma_f32_16x16x32_bf16 %8, v[224:227], a[144:147], %8\n\t"
"ds_read_b64_tr_b16 v[244:245], %[ta1] offset:20480\n\t"
"v_mfma_f32_16x16x32_bf16 %12, v[224:227], a[208:211], %12\n\t"
"ds_read_b64_tr_b16 v[246:247], %[ta1] offset:22528\n\t"
"s_waitcnt lgkmcnt(8)\n\t"
"v_mfma_f32_16x16x32_bf16 %1, v[228:231], a[16:19], %1\n\t"
"ds_read_b64_tr_b16 v[248:249], %[ta2] offset:20480\n\t"
"v_mfma_f32_16x16x32_bf16 %5, v[228:231], a[80:83], %5\n\t"
"ds_read_b64_tr_b16 v[250:251], %[ta2] offset:22528\n\t"
"v_mfma_f32_16x16x32_bf16 %9, v[228:231], a[144:147], %9\n\t"
"ds_read_b64_tr_b16 v[252:253], %[ta3] offset:20480\n\t"
"v_mfma_f32_16x16x32_bf16 %13, v[228:231], a[208:211], %13\n\t"
"ds_read_b64_tr_b16 v[254:255], %[ta3] offset:22528\n\t"
"s_waitcnt lgkmcnt(10)\n\t"
"v_mfma_f32_16x16x32_bf16 %2, v[232:235], a[16:19], %2\n\t"
"s_add_u32 %[st], %[so], 0\n\t"
"buffer_load_dwordx4 a[12:15], %[vo], %[rs], %[st] offen offset:3072 sc1\n\t"
"v_mfma_f32_16x16x32_bf16 %6, v[232:235], a[80:83], %6\n\t"
"v_mfma_f32_16x16x32_bf16 %10, v[232:235], a[144:147], %10\n\t"
"s_add_u32 %[st], %[so], 16384\n\t"
"buffer_load_dwordx4 a[76:79], %[vo], %[rs], %[st] offen offset:3072 sc1\n\t"
"v_mfma_f32_16x16x32_bf16 %14, v[232:235], a[208:211], %14\n\t"
"s_waitcnt lgkmcnt(8)\n\t"
"v_mfma_f32_16x16x32_bf16 %3, v[236:239], a[16:19], %3\n\t"
"s_add_u32 %[st], %[so], 32768\n\t"
"buffer_load_dwordx4 a[140:143], %[vo], %[rs], %[st] offen offset:3072 sc1\n\t"
"v_mfma_f32_16x16x32_bf16 %7, v[236:239], a[80:83], %7\n\t"
"v_mfma_f32_16x16x32_bf16 %11, v[236:239], a[144:147], %11\n\t"
"s_add_u32 %[st], %[so], 49152\n\t"
"buffer_load_dwordx4 a[204:207], %[vo], %[rs], %[st] offen offset:3072 sc1\n\t"
"v_mfma_f32_16x16x32_bf16 %15, v[236:239], a[208:211], %15\n\t"
"s_waitcnt lgkmcnt(6)\n\t"
"v_mfma_f32_16x16x32_bf16 %0, v[240:243], a[20:23], %0\n\t"
"ds_read_b64_tr_b16 v[224:225], %[ta0] offset:24576\n\t"
"v_mfma_f32_16x16x32_bf16 %4, v[240:243], a[84:87], %4\n\t"
"ds_read_b64_tr_b16 v[226:227], %[ta0] offset:26624\n\t"
"v_mfma_f32_16x16x32_bf16 %8, v[240:243], a[148:151], %8\n\t"
"ds_read_b64_tr_b16 v[228:229], %[ta1] offset:24576\n\t"
"v_mfma_f32_16x16x32_bf16 %12, v[240:243], a[212:215], %12\n\t"
"ds_read_b64_tr_b16 v[230:231], %[ta1] offset:26624\n\t"
"s_waitcnt lgkmcnt(8)\n\t"
"v_mfma_f32_16x16x32_bf16 %1, v[244:247], a[20:23], %1\n\t"
"ds_read_b64_tr_b16 v[232:233], %[ta2] offset:24576\n\t"
"v_mfma_f32_16x16x32_bf16 %5, v[244:247], a[84:87], %5\n\t"
"ds_read_b64_tr_b16 v[234:235], %[ta2] offset:26624\n\t"
"v_mfma_f32_16x16x32_bf16 %9, v[244:247], a[148:151], %9\n\t"
"ds_read_b64_tr_b16 v[236:237], %[ta3] offset:24576\n\t"
"v_mfma_f32_16x16x32_bf16 %13, v[244:247], a[212:215], %13\n\t"
"ds_read_b64_tr_b16 v[238:239], %[ta3] offset:26624\n\t"
"s_waitcnt lgkmcnt(10)\n\t"
"v_mfma_f32_16x16x32_bf16 %2, v[248:251], a[20:23], %2\n\t"
"s_add_u32 %[st], %[so], 4096\n\t"
"buffer_load_dwordx4 a[16:19], %[vo], %[rs], %[st] offen offset:0 sc1\n\t"
"v_mfma_f32_16x16x32_bf16 %6, v[248:251], a[84:87], %6\n\t"
"v_mfma_f32_16x16x32_bf16 %10, v[248:251], a[148:151], %10\n\t"
"s_add_u32 %[st], %[so], 20480\n\t"
"buffer_load_dwordx4 a[80:83], %[vo], %[rs], %[st] offen offset:0 sc1\n\t"
"v_mfma_f32_16x16x32_bf16 %14, v[248:251], a[212:215], %14\n\t"
"s_waitcnt lgkmcnt(8)\n\t"
"v_mfma_f32_16x16x32_bf16 %3, v[252:255], a[20:23], %3\n\t"
"s_add_u32 %[st], %[so], 36864\n\t"
"buffer_load_dwordx4 a[144:147], %[vo], %[rs], %[st] offen offset:0 sc1\n\t"
"v_mfma_f32_16x16x32_bf16 %7, v[252:255], a[84:87], %7\n\t"
"v_mfma_f32_16x16x32_bf16 %11, v[252:255], a[148:151], %11\n\t"
"s_add_u32 %[st], %[so], 53248\n\t"
"buffer_load_dwordx4 a[208:211], %[vo], %[rs], %[st] offen offset:0 sc1\n\t"
"v_mfma_f32_16x16x32_bf16 %15, v[252:255], a[212:215], %15\n\t"
"s_waitcnt vmcnt(20)\n\t"
"s_waitcnt lgkmcnt(6)\n\t"
"v_mfma_f32_16x16x32_bf16 %0, v[224:227], a[24:27], %0\n\t"
"ds_read_b64_tr_b16 v[240:241], %[ta0] offset:28672\n\t"
"v_mfma_f32_16x16x32_bf16 %4, v[224:227], a[88:91], %4\n\t"
"ds_read_b64_tr_b16 v[242:243], %[ta0] offset:30720\n\t"
"v_mfma_f32_16x16x32_bf16 %8, v[224:227], a[152:155], %8\n\t"
"ds_read_b64_tr_b16 v[244:245], %[ta1] offset:28672\n\t"
"v_mfma_f32_16x16x32_bf16 %12, v[224:227], a[216:219], %12\n\t"
"ds_read_b64_tr_b16 v[246:247], %[ta1] offset:30720\n\t"
"s_waitcnt lgkmcnt(8)\n\t"
"v_mfma_f32_16x16x32_bf16 %1, v[228:231], a[24:27], %1\n\t"
"ds_read_b64_tr_b16 v[248:249], %[ta2] offset:28672\n\t"
"v_mfma_f32_16x16x32_bf16 %5, v[228:231], a[88:91], %5\n\t"
"ds_read_b64_tr_b16 v[250:251], %[ta2] offset:30720\n\t"
"v_mfma_f32_16x16x32_bf16 %9, v[228:231], a[152:155], %9\n\t"
"ds_read_b64_tr_b16 v[252:253], %[ta3] offset:28672\n\t"
"v_mfma_f32_16x16x32_bf16 %13, v[228:231], a[216:219], %13\n\t"
"ds_read_b64_tr_b16 v[254:255], %[ta3] offset:30720\n\t"
"s_waitcnt lgkmcnt(10)\n\t"
"v_mfma_f32_16x16x32_bf16 %2, v[232:235], a[24:27], %2\n\t"
"s_add_u32 %[st], %[so], 4096\n\t"
"buffer_load_dwordx4 a[20:23], %[vo], %[rs], %[st] offen offset:1024 sc1\n\t"
"v_mfma_f32_16x16x32_bf16 %6, v[232:235], a[88:91], %6\n\t"
"s_mov_b64 exec, 1\n\t"
"global_atomic_add %[z], %[add], %[flag]\n\t"
"s_mov_b64 exec, -1\n\t"
"v_mfma_f32_16x16x32_bf16 %10, v[232:235], a[152:155], %10\n\t"
"s_add_u32 %[st], %[so], 20480\n\t"
"buffer_load_dwordx4 a[84:87], %[vo], %[rs], %[st] offen offset:1024 sc1\n\t"
"v_mfma_f32_16x16x32_bf16 %14, v[232:235], a[216:219], %14\n\t"
"s_waitcnt lgkmcnt(8)\n\t"
"v_mfma_f32_16x16x32_bf16 %3, v[236:239], a[24:27], %3\n\t"
"s_add_u32 %[st], %[so], 36864\n\t"
"buffer_load_dwordx4 a[148:151], %[vo], %[rs], %[st] offen offset:1024 sc1\n\t"
"v_mfma_f32_16x16x32_bf16 %7, v[236:239], a[88:91], %7\n\t"
"v_mfma_f32_16x16x32_bf16 %11, v[236:239], a[152:155], %11\n\t"
"s_add_u32 %[st], %[so], 53248\n\t"
"buffer_load_dwordx4 a[212:215], %[vo], %[rs], %[st] offen offset:1024 sc1\n\t"
"v_mfma_f32_16x16x32_bf16 %15, v[236:239], a[216:219], %15\n\t"
"s_waitcnt lgkmcnt(6)\n\t"
"v_mfma_f32_16x16x32_bf16 %0, v[240:243], a[28:31], %0\n\t"
"ds_read_b64_tr_b16 v[224:225], %[ta0] offset:32768\n\t"
"v_mfma_f32_16x16x32_bf16 %4, v[240:243], a[92:95], %4\n\t"
"ds_read_b64_tr_b16 v[226:227], %[ta0] offset:34816\n\t"
"v_mfma_f32_16x16x32_bf16 %8, v[240:243], a[156:159], %8\n\t"
"ds_read_b64_tr_b16 v[228:229], %[ta1] offset:32768\n\t"
"v_mfma_f32_16x16x32_bf16 %12, v[240:243], a[220:223], %12\n\t"
"ds_read_b64_tr_b16 v[230:231], %[ta1] offset:34816\n\t"
"s_waitcnt lgkmcnt(8)\n\t"
"v_mfma_f32_16x16x32_bf16 %1, v[244:247], a[28:31], %1\n\t"
"ds_read_b64_tr_b16 v[232:233], %[ta2] offset:32768\n\t"
"v_mfma_f32_16x16x32_bf16 %5, v[244:247], a[92:95], %5\n\t"
"ds_read_b64_tr_b16 v[234:235], %[ta2] offset:34816\n\t"
"v_mfma_f32_16x16x32_bf16 %9, v[244:247], a[156:159], %9\n\t"
"ds_read_b64_tr_b16 v[236:237], %[ta3] offset:32768\n\t"
"v_mfma_f32_16x16x32_bf16 %13, v[244:247], a[220:223], %13\n\t"
"ds_read_b64_tr_b16 v[238:239], %[ta3] offset:34816\n\t"
"s_waitcnt lgkmcnt(10)\n\t"
"v_mfma_f32_16x16x32_bf16 %2, v[248:251], a[28:31], %2\n\t"
"s_add_u32 %[st], %[so], 4096\n\t"
"buffer_load_dwordx4 a[24:27], %[vo], %[rs], %[st] offen offset:2048 sc1\n\t"
"v_mfma_f32_16x16x32_bf16 %6, v[248:251], a[92:95], %6\n\t"
"v_mfma_f32_16x16x32_bf16 %10, v[248:251], a[156:159], %10\n\t"
"s_add_u32 %[st], %[so], 20480\n\t"
"buffer_load_dwordx4 a[88:91], %[vo], %[rs], %[st] offen offset:2048 sc1\n\t"
"v_mfma_f32_16x16x32_bf16 %14, v[248:251], a[220:223], %14\n\t"
"s_waitcnt lgkmcnt(8)\n\t"
"v_mfma_f32_16x16x32_bf16 %3, v[252:255], a[28:31], %3\n\t"
"s_add_u32 %[st], %[so], 36864\n\t"
"buffer_load_dwordx4 a[152:155], %[vo], %[rs], %[st] offen offset:2048 sc1\n\t"
"v_mfma_f32_16x16x32_bf16 %7, v[252:255], a[92:95], %7\n\t"
"v_mfma_f32_16x16x32_bf16 %11, v[252:255], a[156:159], %11\n\t"
"s_add_u32 %[st], %[so], 53248\n\t"
"buffer_load_dwordx4 a[216:219], %[vo], %[rs], %[st] offen offset:2048 sc1\n\t"
"v_mfma_f32_16x16x32_bf16 %15, v[252:255], a[220:223], %15\n\t"
"s_waitcnt lgkmcnt(6)\n\t"
"v_mfma_f32_16x16x32_bf16 %0, v[224:227], a[32:35], %0\n\t"
"ds_read_b64_tr_b16 v[240:241], %[ta0] offset:36864\n\t"
"v_mfma_f32_16x16x32_bf16 %4, v[224:227], a[96:99], %4\n\t"
"ds_read_b64_tr_b16 v[242:243], %[ta0] offset:38912\n\t"
"v_mfma_f32_16x16x32_bf16 %8, v[224:227], a[160:163], %8\n\t"
"ds_read_b64_tr_b16 v[244:245], %[ta1] offset:36864\n\t"
"v_mfma_f32_16x16x32_bf16 %12, v[224:227], a[224:227], %12\n\t"
"ds_read_b64_tr_b16 v[246:247], %[ta1] offset:38912\n\t"
"s_waitcnt lgkmcnt(8)\n\t"
"v_mfma_f32_16x16x32_bf16 %1, v[228:231], a[32:35], %1\n\t"
"ds_read_b64_tr_b16 v[248:249], %[ta2] offset:36864\n\t"
"v_mfma_f32_16x16x32_bf16 %5, v[228:231], a[96:99], %5\n\t"
"ds_read_b64_tr_b16 v[250:251], %[ta2] offset:38912\n\t"
"v_mfma_f32_16x16x32_bf16 %9, v[228:231], a[160:163], %9\n\t"
"ds_read_b64_tr_b16 v[252:253], %[ta3] offset:36864\n\t"
"v_mfma_f32_16x16x32_bf16 %13, v[228:231], a[224:227], %13\n\t"
"ds_read_b64_tr_b16 v[254:255], %[ta3] offset:38912\n\t"
"s_waitcnt lgkmcnt(10)\n\t"
"v_mfma_f32_16x16x32_bf16 %2, v[232:235], a[32:35], %2\n\t"
"s_add_u32 %[st], %[so], 4096\n\t"
"buffer_load_dwordx4 a[28:31], %[vo], %[rs], %[st] offen offset:3072 sc1\n\t"
"v_mfma_f32_16x16x32_bf16 %6, v[232:235], a[96:99], %6\n\t"
"v_mfma_f32_16x16x32_bf16 %10, v[232:235], a[160:163], %10\n\t"
"s_add_u32 %[st], %[so], 20480\n\t"
"buffer_load_dwordx4 a[92:95], %[vo], %[rs], %[st] offen offset:3072 sc1\n\t"
"v_mfma_f32_16x16x32_bf16 %14, v[232:235], a[224:227], %14\n\t"
"s_waitcnt lgkmcnt(8)\n\t"
"v_mfma_f32_16x16x32_bf16 %3, v[236:239], a[32:35], %3\n\t"
"s_add_u32 %[st], %[so], 36864\n\t"
"buffer_load_dwordx4 a[156:159], %[vo], %[rs], %[st] offen offset:3072 sc1\n\t"
"v_mfma_f32_16x16x32_bf16 %7, v[236:239], a[96:99], %7\n\t"
"v_mfma_f32_16x16x32_bf16 %11, v[236:239], a[160:163], %11\n\t"
"s_add_u32 %[st], %[so], 53248\n\t"
"buffer_load_dwordx4 a[220:223], %[vo], %[rs], %[st] offen offset:3072 sc1\n\t"
"v_mfma_f32_16x16x32_bf16 %15, v[236:239], a[224:227], %15\n\t"
"s_waitcnt lgkmcnt(6)\n\t"
"v_mfma_f32_16x16x32_bf16 %0, v[240:243], a[36:39], %0\n\t"
"ds_read_b64_tr_b16 v[224:225], %[ta0] offset:40960\n\t"
"v_mfma_f32_16x16x32_bf16 %4, v[240:243], a[100:103], %4\n\t"
"ds_read_b64_tr_b16 v[226:227], %[ta0] offset:43008\n\t"
"v_mfma_f32_16x16x32_bf16 %8, v[240:243], a[164:167], %8\n\t"
"ds_read_b64_tr_b16 v[228:229], %[ta1] offset:40960\n\t"
"v_mfma_f32_16x16x32_bf16 %12, v[240:243], a[228:231], %12\n\t"
"ds_read_b64_tr_b16 v[230:231], %[ta1] offset:43008\n\t"
"s_waitcnt lgkmcnt(8)\n\t"
"v_mfma_f32_16x16x32_bf16 %1, v[244:247], a[36:39], %1\n\t"
"ds_read_b64_tr_b16 v[232:233], %[ta2] offset:40960\n\t"
"v_mfma_f32_16x16x32_bf16 %5, v[244:247], a[100:103], %5\n\t"
"ds_read_b64_tr_b16 v[234:235], %[ta2] offset:43008\n\t"
"v_mfma_f32_16x16x32_bf16 %9, v[244:247], a[164:167], %9\n\t"
"ds_read_b64_tr_b16 v[236:237], %[ta3] offset:40960\n\t"
"v_mfma_f32_16x16x32_bf16 %13, v[244:247], a[228:231], %13\n\t"
"ds_read_b64_tr_b16 v[238:239], %[ta3] offset:43008\n\t"
"s_waitcnt lgkmcnt(10)\n\t"
"v_mfma_f32_16x16x32_bf16 %2, v[248:251], a[36:39], %2\n\t"
"s_add_u32 %[st], %[so], 8192\n\t"
"buffer_load_dwordx4 a[32:35], %[vo], %[rs], %[st] offen offset:0 sc1\n\t"
"v_mfma_f32_16x16x32_bf16 %6, v[248:251], a[100:103], %6\n\t"
"v_mfma_f32_16x16x32_bf16 %10, v[248:251], a[164:167], %10\n\t"
"s_add_u32 %[st], %[so], 24576\n\t"
"buffer_load_dwordx4 a[96:99], %[vo], %[rs], %[st] offen offset:0 sc1\n\t"
"v_mfma_f32_16x16x32_bf16 %14, v[248:251], a[228:231], %14\n\t"
"s_waitcnt lgkmcnt(8)\n\t"
"v_mfma_f32_16x16x32_bf16 %3, v[252:255], a[36:39], %3\n\t"
"s_add_u32 %[st], %[so], 40960\n\t"
"buffer_load_dwordx4 a[160:163], %[vo], %[rs], %[st] offen offset:0 sc1\n\t"
"v_mfma_f32_16x16x32_bf16 %7, v[252:255], a[100:103], %7\n\t"
"v_mfma_f32_16x16x32_bf16 %11, v[252:255], a[164:167], %11\n\t"
"s_add_u32 %[st], %[so], 57344\n\t"
"buffer_load_dwordx4 a[224:227], %[vo], %[rs], %[st] offen offset:0 sc1\n\t"
"v_mfma_f32_16x16x32_bf16 %15, v[252:255], a[228:231], %15\n\t"
"s_waitcnt lgkmcnt(6)\n\t"
"v_mfma_f32_16x16x32_bf16 %0, v[224:227], a[40:43], %0\n\t"
"ds_read_b64_tr_b16 v[240:241], %[ta0] offset:45056\n\t"
"v_mfma_f32_16x16x32_bf16 %4, v[224:227], a[104:107], %4\n\t"
"ds_read_b64_tr_b16 v[242:243], %[ta0] offset:47104\n\t"
"v_mfma_f32_16x16x32_bf16 %8, v[224:227], a[168:171], %8\n\t"
"ds_read_b64_tr_b16 v[244:245], %[ta1] offset:45056\n\t"
"v_mfma_f32_16x16x32_bf16 %12, v[224:227], a[232:235], %12\n\t"
"ds_read_b64_tr_b16 v[246:247], %[ta1] offset:47104\n\t"
"s_waitcnt lgkmcnt(8)\n\t"
"v_mfma_f32_16x16x32_bf16 %1, v[228:231], a[40:43], %1\n\t"
"ds_read_b64_tr_b16 v[248:249], %[ta2] offset:45056\n\t"
"v_mfma_f32_16x16x32_bf16 %5, v[228:231], a[104:107], %5\n\t"
"ds_read_b64_tr_b16 v[250:251], %[ta2] offset:47104\n\t"
"v_mfma_f32_16x16x32_bf16 %9, v[228:231], a[168:171], %9\n\t"
"ds_read_b64_tr_b16 v[252:253], %[ta3] offset:45056\n\t"
"v_mfma_f32_16x16x32_bf16 %13, v[228:231], a[232:235], %13\n\t"
"ds_read_b64_tr_b16 v[254:255], %[ta3] offset:47104\n\t"
"s_waitcnt lgkmcnt(10)\n\t"
"v_mfma_f32_16x16x32_bf16 %2, v[232:235], a[40:43], %2\n\t"
"s_add_u32 %[st], %[so], 8192\n\t"
"buffer_load_dwordx4 a[36:39], %[vo], %[rs], %[st] offen offset:1024 sc1\n\t"
"v_mfma_f32_16x16x32_bf16 %6, v[232:235], a[104:107], %6\n\t"
"v_mfma_f32_16x16x32_bf16 %10, v[232:235], a[168:171], %10\n\t"
"s_add_u32 %[st], %[so], 24576\n\t"
"buffer_load_dwordx4 a[100:103], %[vo], %[rs], %[st] offen offset:1024 sc1\n\t"
"v_mfma_f32_16x16x32_bf16 %14, v[232:235], a[232:235], %14\n\t"
"s_waitcnt lgkmcnt(8)\n\t"
"v_mfma_f32_16x16x32_bf16 %3, v[236:239], a[40:43], %3\n\t"
"s_add_u32 %[st], %[so], 40960\n\t"
"buffer_load_dwordx4 a[164:167], %[vo], %[rs], %[st] offen offset:1024 sc1\n\t"
"v_mfma_f32_16x16x32_bf16 %7, v[236:239], a[104:107], %7\n\t"
"v_mfma_f32_16x16x32_bf16 %11, v[236:239], a[168:171], %11\n\t"
"s_add_u32 %[st], %[so], 57344\n\t"
"buffer_load_dwordx4 a[228:231], %[vo], %[rs], %[st] offen offset:1024 sc1\n\t"
"v_mfma_f32_16x16x32_bf16 %15, v[236:239], a[232:235], %15\n\t"
"s_waitcnt lgkmcnt(6)\n\t"
"v_mfma_f32_16x16x32_bf16 %0, v[240:243], a[44:47], %0\n\t"
"ds_read_b64_tr_b16 v[224:225], %[ta0] offset:49152\n\t"
"v_mfma_f32_16x16x32_bf16 %4, v[240:243], a[108:111], %4\n\t"
"ds_read_b64_tr_b16 v[226:227], %[ta0] offset:51200\n\t"
"v_mfma_f32_16x16x32_bf16 %8, v[240:243], a[172:175], %8\n\t"
"ds_read_b64_tr_b16 v[228:229], %[ta1] offset:49152\n\t"
"v_mfma_f32_16x16x32_bf16 %12, v[240:243], a[236:239], %12\n\t"
"ds_read_b64_tr_b16 v[230:231], %[ta1] offset:51200\n\t"
"s_waitcnt lgkmcnt(8)\n\t"
"v_mfma_f32_16x16x32_bf16 %1, v[244:247], a[44:47], %1\n\t"
"ds_read_b64_tr_b16 v[232:233], %[ta2] offset:49152\n\t"
"v_mfma_f32_16x16x32_bf16 %5, v[244:247], a[108:111], %5\n\t"
"ds_read_b64_tr_b16 v[234:235], %[ta2] offset:51200\n\t"
"v_mfma_f32_16x16x32_bf16 %9, v[244:247], a[172:175], %9\n\t"
"ds_read_b64_tr_b16 v[236:237], %[ta3] offset:49152\n\t"
"v_mfma_f32_16x16x32_bf16 %13, v[244:247], a[236:239], %13\n\t"
"ds_read_b64_tr_b16 v[238:239], %[ta3] offset:51200\n\t"
"s_waitcnt lgkmcnt(10)\n\t"
"v_mfma_f32_16x16x32_bf16 %2, v[248:251], a[44:47], %2\n\t"
"s_add_u32 %[st], %[so], 8192\n\t"
"buffer_load_dwordx4 a[40:43], %[vo], %[rs], %[st] offen offset:2048 sc1\n\t"
"v_mfma_f32_16x16x32_bf16 %6, v[248:251], a[108:111], %6\n\t"
"v_mfma_f32_16x16x32_bf16 %10, v[248:251], a[172:175], %10\n\t"
"s_add_u32 %[st], %[so], 24576\n\t"
"buffer_load_dwordx4 a[104:107], %[vo], %[rs], %[st] offen offset:2048 sc1\n\t"
"v_mfma_f32_16x16x32_bf16 %14, v[248:251], a[236:239], %14\n\t"
"s_waitcnt lgkmcnt(8)\n\t"
"v_mfma_f32_16x16x32_bf16 %3, v[252:255], a[44:47], %3\n\t"
"s_add_u32 %[st], %[so], 40960\n\t"
"buffer_load_dwordx4 a[168:171], %[vo], %[rs], %[st] offen offset:2048 sc1\n\t"
"v_mfma_f32_16x16x32_bf16 %7, v[252:255], a[108:111], %7\n\t"
"v_mfma_f32_16x16x32_bf16 %11, v[252:255], a[172:175], %11\n\t"
"s_add_u32 %[st], %[so], 57344\n\t"
"buffer_load_dwordx4 a[232:235], %[vo], %[rs], %[st] offen offset:2048 sc1\n\t"
"v_mfma_f32_16x16x32_bf16 %15, v[252:255], a[236:239], %15\n\t"
"s_waitcnt lgkmcnt(6)\n\t"
"v_mfma_f32_16x16x32_bf16 %0, v[224:227], a[48:51], %0\n\t"
"ds_read_b64_tr_b16 v[240:241], %[ta0] offset:53248\n\t"
"v_mfma_f32_16x16x32_bf16 %4, v[224:227], a[112:115], %4\n\t"
"ds_read_b64_tr_b16 v[242:243], %[ta0] offset:55296\n\t"
"v_mfma_f32_16x16x32_bf16 %8, v[224:227], a[176:179], %8\n\t"
"ds_read_b64_tr_b16 v[244:245], %[ta1] offset:53248\n\t"
"v_mfma_f32_16x16x32_bf16 %12, v[224:227], a[240:243], %12\n\t"
"ds_read_b64_tr_b16 v[246:247], %[ta1] offset:55296\n\t"
"s_waitcnt lgkmcnt(8)\n\t"
"v_mfma_f32_16x16x32_bf16 %1, v[228:231], a[48:51], %1\n\t"
"ds_read_b64_tr_b16 v[248:249], %[ta2] offset:53248\n\t"
"v_mfma_f32_16x16x32_bf16 %5, v[228:231], a[112:115], %5\n\t"
"ds_read_b64_tr_b16 v[250:251], %[ta2] offset:55296\n\t"
"v_mfma_f32_16x16x32_bf16 %9, v[228:231], a[176:179], %9\n\t"
"ds_read_b64_tr_b16 v[252:253], %[ta3] offset:53248\n\t"
"v_mfma_f32_16x16x32_bf16 %13, v[228:231], a[240:243], %13\n\t"
"ds_read_b64_tr_b16 v[254:255], %[ta3] offset:55296\n\t"
"s_waitcnt lgkmcnt(10)\n\t"
"v_mfma_f32_16x16x32_bf16 %2, v[232:235], a[48:51], %2\n\t"
"s_add_u32 %[st], %[so], 8192\n\t"
"buffer_load_dwordx4 a[44:47], %[vo], %[rs], %[st] offen offset:3072 sc1\n\t"
"v_mfma_f32_16x16x32_bf16 %6, v[232:235], a[112:115], %6\n\t"
"v_mfma_f32_16x16x32_bf16 %10, v[232:235], a[176:179], %10\n\t"
"s_add_u32 %[st], %[so], 24576\n\t"
"buffer_load_dwordx4 a[108:111], %[vo], %[rs], %[st] offen offset:3072 sc1\n\t"
"v_mfma_f32_16x16x32_bf16 %14, v[232:235], a[240:243], %14\n\t"
"s_waitcnt lgkmcnt(8)\n\t"
"v_mfma_f32_16x16x32_bf16 %3, v[236:239], a[48:51], %3\n\t"
"s_add_u32 %[st], %[so], 40960\n\t"
"buffer_load_dwordx4 a[172:175], %[vo], %[rs], %[st] offen offset:3072 sc1\n\t"
"v_mfma_f32_16x16x32_bf16 %7, v[236:239], a[112:115], %7\n\t"
"v_mfma_f32_16x16x32_bf16 %11, v[236:239], a[176:179], %11\n\t"
"s_add_u32 %[st], %[so], 57344\n\t"
"buffer_load_dwordx4 a[236:239], %[vo], %[rs], %[st] offen offset:3072 sc1\n\t"
"v_mfma_f32_16x16x32_bf16 %15, v[236:239], a[240:243], %15\n\t"
"s_waitcnt lgkmcnt(6)\n\t"
"v_mfma_f32_16x16x32_bf16 %0, v[240:243], a[52:55], %0\n\t"
"ds_read_b64_tr_b16 v[224:225], %[ta0] offset:57344\n\t"
"v_mfma_f32_16x16x32_bf16 %4, v[240:243], a[116:119], %4\n\t"
"ds_read_b64_tr_b16 v[226:227], %[ta0] offset:59392\n\t"
"v_mfma_f32_16x16x32_bf16 %8, v[240:243], a[180:183], %8\n\t"
"ds_read_b64_tr_b16 v[228:229], %[ta1] offset:57344\n\t"
"v_mfma_f32_16x16x32_bf16 %12, v[240:243], a[244:247], %12\n\t"
"ds_read_b64_tr_b16 v[230:231], %[ta1] offset:59392\n\t"
"s_waitcnt lgkmcnt(8)\n\t"
"v_mfma_f32_16x16x32_bf16 %1, v[244:247], a[52:55], %1\n\t"
"ds_read_b64_tr_b16 v[232:233], %[ta2] offset:57344\n\t"
"v_mfma_f32_16x16x32_bf16 %5, v[244:247], a[116:119], %5\n\t"
"ds_read_b64_tr_b16 v[234:235], %[ta2] offset:59392\n\t"
"v_mfma_f32_16x16x32_bf16 %9, v[244:247], a[180:183], %9\n\t"
"ds_read_b64_tr_b16 v[236:237], %[ta3] offset:57344\n\t"
"v_mfma_f32_16x16x32_bf16 %13, v[244:247], a[244:247], %13\n\t"
"ds_read_b64_tr_b16 v[238:239], %[ta3] offset:59392\n\t"
"s_waitcnt lgkmcnt(10)\n\t"
"v_mfma_f32_16x16x32_bf16 %2, v[248:251], a[52:55], %2\n\t"
"s_add_u32 %[st], %[so], 12288\n\t"
"buffer_load_dwordx4 a[48:51], %[vo], %[rs], %[st] offen offset:0 sc1\n\t"
"v_mfma_f32_16x16x32_bf16 %6, v[248:251], a[116:119], %6\n\t"
"v_mfma_f32_16x16x32_bf16 %10, v[248:251], a[180:183], %10\n\t"
"s_add_u32 %[st], %[so], 28672\n\t"
"buffer_load_dwordx4 a[112:115], %[vo], %[rs], %[st] offen offset:0 sc1\n\t"
"v_mfma_f32_16x16x32_bf16 %14, v[248:251], a[244:247], %14\n\t"
"s_waitcnt lgkmcnt(8)\n\t"
"v_mfma_f32_16x16x32_bf16 %3, v[252:255], a[52:55], %3\n\t"
"s_add_u32 %[st], %[so], 45056\n\t"
"buffer_load_dwordx4 a[176:179], %[vo], %[rs], %[st] offen offset:0 sc1\n\t"
"v_mfma_f32_16x16x32_bf16 %7, v[252:255], a[116:119], %7\n\t"
"v_mfma_f32_16x16x32_bf16 %11, v[252:255], a[180:183], %11\n\t"
"s_add_u32 %[st], %[so], 61440\n\t"
"buffer_load_dwordx4 a[240:243], %[vo], %[rs], %[st] offen offset:0 sc1\n\t"
"v_mfma_f32_16x16x32_bf16 %15, v[252:255], a[244:247], %15\n\t"
"s_waitcnt lgkmcnt(6)\n\t"
"v_mfma_f32_16x16x32_bf16 %0, v[224:227], a[56:59], %0\n\t"
"ds_read_b64_tr_b16 v[240:241], %[ta0] offset:61440\n\t"
"v_mfma_f32_16x16x32_bf16 %4, v[224:227], a[120:123], %4\n\t"
"ds_read_b64_tr_b16 v[242:243], %[ta0] offset:63488\n\t"
"v_mfma_f32_16x16x32_bf16 %8, v[224:227], a[184:187], %8\n\t"
"ds_read_b64_tr_b16 v[244:245], %[ta1] offset:61440\n\t"
"v_mfma_f32_16x16x32_bf16 %12, v[224:227], a[248:251], %12\n\t"
"ds_read_b64_tr_b16 v[246:247], %[ta1] offset:63488\n\t"
"s_waitcnt lgkmcnt(8)\n\t"
"v_mfma_f32_16x16x32_bf16 %1, v[228:231], a[56:59], %1\n\t"
"ds_read_b64_tr_b16 v[248:249], %[ta2] offset:61440\n\t"
"v_mfma_f32_16x16x32_bf16 %5, v[228:231], a[120:123], %5\n\t"
"ds_read_b64_tr_b16 v[250:251], %[ta2] offset:63488\n\t"
"v_mfma_f32_16x16x32_bf16 %9, v[228:231], a[184:187], %9\n\t"
"ds_read_b64_tr_b16 v[252:253], %[ta3] offset:61440\n\t"
"v_mfma_f32_16x16x32_bf16 %13, v[228:231], a[248:251], %13\n\t"
"ds_read_b64_tr_b16 v[254:255], %[ta3] offset:63488\n\t"
"s_waitcnt lgkmcnt(10)\n\t"
"v_mfma_f32_16x16x32_bf16 %2, v[232:235], a[56:59], %2\n\t"
"s_add_u32 %[st], %[so], 12288\n\t"
"buffer_load_dwordx4 a[52:55], %[vo], %[rs], %[st] offen offset:1024 sc1\n\t"
"v_mfma_f32_16x16x32_bf16 %6, v[232:235], a[120:123], %6\n\t"
"v_mfma_f32_16x16x32_bf16 %10, v[232:235], a[184:187], %10\n\t"
"s_add_u32 %[st], %[so], 28672\n\t"
"buffer_load_dwordx4 a[116:119], %[vo], %[rs], %[st] offen offset:1024 sc1\n\t"
"v_mfma_f32_16x16x32_bf16 %14, v[232:235], a[248:251], %14\n\t"
"s_waitcnt lgkmcnt(8)\n\t"
"v_mfma_f32_16x16x32_bf16 %3, v[236:239], a[56:59], %3\n\t"
"s_add_u32 %[st], %[so], 45056\n\t"
"buffer_load_dwordx4 a[180:183], %[vo], %[rs], %[st] offen offset:1024 sc1\n\t"
"v_mfma_f32_16x16x32_bf16 %7, v[236:239], a[120:123], %7\n\t"
"v_mfma_f32_16x16x32_bf16 %11, v[236:239], a[184:187], %11\n\t"
"s_add_u32 %[st], %[so], 61440\n\t"
"buffer_load_dwordx4 a[244:247], %[vo], %[rs], %[st] offen offset:1024 sc1\n\t"
"v_mfma_f32_16x16x32_bf16 %15, v[236:239], a[248:251], %15\n\t"
"s_waitcnt lgkmcnt(6)\n\t"
"v_mfma_f32_16x16x32_bf16 %0, v[240:243], a[60:63], %0\n\t"
"v_mfma_f32_16x16x32_bf16 %4, v[240:243], a[124:127], %4\n\t"
"v_mfma_f32_16x16x32_bf16 %8, v[240:243], a[188:191], %8\n\t"
"v_mfma_f32_16x16x32_bf16 %12, v[240:243], a[252:255], %12\n\t"
"s_waitcnt lgkmcnt(4)\n\t"
"v_mfma_f32_16x16x32_bf16 %1, v[244:247], a[60:63], %1\n\t"
"v_mfma_f32_16x16x32_bf16 %5, v[244:247], a[124:127], %5\n\t"
"v_mfma_f32_16x16x32_bf16 %9, v[244:247], a[188:191], %9\n\t"
"v_mfma_f32_16x16x32_bf16 %13, v[244:247], a[252:255], %13\n\t"
"s_waitcnt lgkmcnt(2)\n\t"
"v_mfma_f32_16x16x32_bf16 %2, v[248:251], a[60:63], %2\n\t"
"s_add_u32 %[st], %[so], 12288\n\t"
"buffer_load_dwordx4 a[56:59], %[vo], %[rs], %[st] offen offset:2048 sc1\n\t"
"v_mfma_f32_16x16x32_bf16 %6, v[248:251], a[124:127], %6\n\t"
"v_mfma_f32_16x16x32_bf16 %10, v[248:251], a[188:191], %10\n\t"
"s_add_u32 %[st], %[so], 28672\n\t"
"buffer_load_dwordx4 a[120:123], %[vo], %[rs], %[st] offen offset:2048 sc1\n\t"
"v_mfma_f32_16x16x32_bf16 %14, v[248:251], a[252:255], %14\n\t"
"s_waitcnt lgkmcnt(0)\n\t"
"v_mfma_f32_16x16x32_bf16 %3, v[252:255], a[60:63], %3\n\t"
"s_add_u32 %[st], %[so], 45056\n\t"
"buffer_load_dwordx4 a[184:187], %[vo], %[rs], %[st] offen offset:2048 sc1\n\t"
"v_mfma_f32_16x16x32_bf16 %7, v[252:255], a[124:127], %7\n\t"
"v_mfma_f32_16x16x32_bf16 %11, v[252:255], a[188:191], %11\n\t"
"s_add_u32 %[st], %[so], 61440\n\t"
"buffer_load_dwordx4 a[248:251], %[vo], %[rs], %[st] offen offset:2048 sc1\n\t"
"v_mfma_f32_16x16x32_bf16 %15, v[252:255], a[252:255], %15\n\t"
"s_add_u32 %[st], %[so], 12288\n\t"
"buffer_load_dwordx4 a[60:63], %[vo], %[rs], %[st] offen offset:3072 sc1\n\t"
"s_add_u32 %[st], %[so], 28672\n\t"
"buffer_load_dwordx4 a[124:127], %[vo], %[rs], %[st] offen offset:3072 sc1\n\t"
"s_add_u32 %[st], %[so], 45056\n\t"
"buffer_load_dwordx4 a[188:191], %[vo], %[rs], %[st] offen offset:3072 sc1\n\t"
"s_add_u32 %[st], %[so], 61440\n\t"
"buffer_load_dwordx4 a[252:255], %[vo], %[rs], %[st] offen offset:3072 sc1\n\t"
"s_nop 7\n\t"
    GEMM_OPS);
}

template <bool ISV>
DI void mini_tile(const u4 (&A)[2][16], f4 (&acc)[2][4], LAS unsigned char* bb, const int (&ta)[4]) {
  u4 B[2][4];
  #pragma unroll
  for (int t = 0; t < 4; ++t) { u2 lo = trd(bb + ta[t]), hi = trd(bb + ta[t] + 2048); B[0][t] = (u4){lo.x, lo.y, hi.x, hi.y}; }
  __builtin_amdgcn_sched_barrier(0);
  #pragma unroll
  for (int k = 0; k < 16; ++k) {
    if (k < 15) {
      #pragma unroll
      for (int t = 0; t < 4; ++t) { u2 lo = trd(bb + ta[t] + (k + 1) * 4096), hi = trd(bb + ta[t] + (k + 1) * 4096 + 2048); B[(k + 1) & 1][t] = (u4){lo.x, lo.y, hi.x, hi.y}; }
    }
    #pragma unroll
    for (int t = 0; t < 4; ++t)
      #pragma unroll
      for (int i = 0; i < 2; ++i) {
        const f4 c = k == 0 ? (f4){0.f, 0.f, 0.f, 0.f} : acc[i][t];
        acc[i][t] = ISV ? mfma32(A[i][k], B[k & 1][t], c) : mfma32(B[k & 1][t], A[i][k], c);
      }
    if (k < 15) {
      #pragma unroll
      for (int j = 0; j < 8; ++j) { __builtin_amdgcn_sched_group_barrier(0x8, 1, 0); __builtin_amdgcn_sched_group_barrier(0x100, 1, 0); }
    }
    __builtin_amdgcn_sched_barrier(0);
  }
}

DI void gemm_role(const Params& p, unsigned char* ws, int f, int w, unsigned tag, LAS unsigned char* lds) {
  const int lane = lane_id(), tid = w * 64 + lane;
  const int g = lane >> 4, q = (lane >> 2) & 3, pp = lane & 3;
  const u16* wgp = p.wg; const u16* wup = p.wu;
  asm volatile("" : "+s"(wgp), "+s"(wup));
  auto stage = [&](int id, int buf, int nw) {
    const int r8 = lane >> 3, cp = lane & 7;
    const int ch = cp ^ (((r8 >> 1) & 3) << 1);
    const u16* src; int ld;
    if (id < 192) {
      const u16* W = id < 128 ? ((id & 1) ? p.Wv : p.Wk) : p.Wq;
      const int cb = id < 128 ? (id >> 1) * 64 : (id - 128) * 64;
      src = W + cb + ch * 8; ld = HD;
    } else {
      const uintptr_t pg = (uintptr_t)wgp, pu = (uintptr_t)wup;
      src = (const u16*)((ch & 4) ? pu : pg) + (id - 192) * 32 + (ch & 3) * 8; ld = DF;
    }
    src += (size_t)r8 * ld;
    for (int i = w; i < 64; i += nw) dma16(src + (size_t)i * 8 * ld, buf * 65536 + i * 1024);
  };
  if (w < 3) stage(f, 0, 3); else wait_done(ws, 96, 32, tag);
  asm volatile("s_barrier" ::: "memory");
  const __amdgpu_buffer_rsrc_t rx = mkrsrc(ws + W_XN, 2u << 20);
  const unsigned vx = w * 65536 + lane * 16;
  auto load_a = [&](unsigned so) {
    unsigned st;
    asm volatile(
"s_add_u32 %[st], %[so], 0\n\t"
"buffer_load_dwordx4 a[0:3], %[vo], %[rs], %[st] offen offset:0 sc1\n\t"
"s_add_u32 %[st], %[so], 16384\n\t"
"buffer_load_dwordx4 a[64:67], %[vo], %[rs], %[st] offen offset:0 sc1\n\t"
"s_add_u32 %[st], %[so], 32768\n\t"
"buffer_load_dwordx4 a[128:131], %[vo], %[rs], %[st] offen offset:0 sc1\n\t"
"s_add_u32 %[st], %[so], 49152\n\t"
"buffer_load_dwordx4 a[192:195], %[vo], %[rs], %[st] offen offset:0 sc1\n\t"
"s_add_u32 %[st], %[so], 0\n\t"
"buffer_load_dwordx4 a[4:7], %[vo], %[rs], %[st] offen offset:1024 sc1\n\t"
"s_add_u32 %[st], %[so], 16384\n\t"
"buffer_load_dwordx4 a[68:71], %[vo], %[rs], %[st] offen offset:1024 sc1\n\t"
"s_add_u32 %[st], %[so], 32768\n\t"
"buffer_load_dwordx4 a[132:135], %[vo], %[rs], %[st] offen offset:1024 sc1\n\t"
"s_add_u32 %[st], %[so], 49152\n\t"
"buffer_load_dwordx4 a[196:199], %[vo], %[rs], %[st] offen offset:1024 sc1\n\t"
"s_add_u32 %[st], %[so], 0\n\t"
"buffer_load_dwordx4 a[8:11], %[vo], %[rs], %[st] offen offset:2048 sc1\n\t"
"s_add_u32 %[st], %[so], 16384\n\t"
"buffer_load_dwordx4 a[72:75], %[vo], %[rs], %[st] offen offset:2048 sc1\n\t"
"s_add_u32 %[st], %[so], 32768\n\t"
"buffer_load_dwordx4 a[136:139], %[vo], %[rs], %[st] offen offset:2048 sc1\n\t"
"s_add_u32 %[st], %[so], 49152\n\t"
"buffer_load_dwordx4 a[200:203], %[vo], %[rs], %[st] offen offset:2048 sc1\n\t"
"s_add_u32 %[st], %[so], 0\n\t"
"buffer_load_dwordx4 a[12:15], %[vo], %[rs], %[st] offen offset:3072 sc1\n\t"
"s_add_u32 %[st], %[so], 16384\n\t"
"buffer_load_dwordx4 a[76:79], %[vo], %[rs], %[st] offen offset:3072 sc1\n\t"
"s_add_u32 %[st], %[so], 32768\n\t"
"buffer_load_dwordx4 a[140:143], %[vo], %[rs], %[st] offen offset:3072 sc1\n\t"
"s_add_u32 %[st], %[so], 49152\n\t"
"buffer_load_dwordx4 a[204:207], %[vo], %[rs], %[st] offen offset:3072 sc1\n\t"
"s_add_u32 %[st], %[so], 4096\n\t"
"buffer_load_dwordx4 a[16:19], %[vo], %[rs], %[st] offen offset:0 sc1\n\t"
"s_add_u32 %[st], %[so], 20480\n\t"
"buffer_load_dwordx4 a[80:83], %[vo], %[rs], %[st] offen offset:0 sc1\n\t"
"s_add_u32 %[st], %[so], 36864\n\t"
"buffer_load_dwordx4 a[144:147], %[vo], %[rs], %[st] offen offset:0 sc1\n\t"
"s_add_u32 %[st], %[so], 53248\n\t"
"buffer_load_dwordx4 a[208:211], %[vo], %[rs], %[st] offen offset:0 sc1\n\t"
"s_add_u32 %[st], %[so], 4096\n\t"
"buffer_load_dwordx4 a[20:23], %[vo], %[rs], %[st] offen offset:1024 sc1\n\t"
"s_add_u32 %[st], %[so], 20480\n\t"
"buffer_load_dwordx4 a[84:87], %[vo], %[rs], %[st] offen offset:1024 sc1\n\t"
"s_add_u32 %[st], %[so], 36864\n\t"
"buffer_load_dwordx4 a[148:151], %[vo], %[rs], %[st] offen offset:1024 sc1\n\t"
"s_add_u32 %[st], %[so], 53248\n\t"
"buffer_load_dwordx4 a[212:215], %[vo], %[rs], %[st] offen offset:1024 sc1\n\t"
"s_add_u32 %[st], %[so], 4096\n\t"
"buffer_load_dwordx4 a[24:27], %[vo], %[rs], %[st] offen offset:2048 sc1\n\t"
"s_add_u32 %[st], %[so], 20480\n\t"
"buffer_load_dwordx4 a[88:91], %[vo], %[rs], %[st] offen offset:2048 sc1\n\t"
"s_add_u32 %[st], %[so], 36864\n\t"
"buffer_load_dwordx4 a[152:155], %[vo], %[rs], %[st] offen offset:2048 sc1\n\t"
"s_add_u32 %[st], %[so], 53248\n\t"
"buffer_load_dwordx4 a[216:219], %[vo], %[rs], %[st] offen offset:2048 sc1\n\t"
"s_add_u32 %[st], %[so], 4096\n\t"
"buffer_load_dwordx4 a[28:31], %[vo], %[rs], %[st] offen offset:3072 sc1\n\t"
"s_add_u32 %[st], %[so], 20480\n\t"
"buffer_load_dwordx4 a[92:95], %[vo], %[rs], %[st] offen offset:3072 sc1\n\t"
"s_add_u32 %[st], %[so], 36864\n\t"
"buffer_load_dwordx4 a[156:159], %[vo], %[rs], %[st] offen offset:3072 sc1\n\t"
"s_add_u32 %[st], %[so], 53248\n\t"
"buffer_load_dwordx4 a[220:223], %[vo], %[rs], %[st] offen offset:3072 sc1\n\t"
"s_add_u32 %[st], %[so], 8192\n\t"
"buffer_load_dwordx4 a[32:35], %[vo], %[rs], %[st] offen offset:0 sc1\n\t"
"s_add_u32 %[st], %[so], 24576\n\t"
"buffer_load_dwordx4 a[96:99], %[vo], %[rs], %[st] offen offset:0 sc1\n\t"
"s_add_u32 %[st], %[so], 40960\n\t"
"buffer_load_dwordx4 a[160:163], %[vo], %[rs], %[st] offen offset:0 sc1\n\t"
"s_add_u32 %[st], %[so], 57344\n\t"
"buffer_load_dwordx4 a[224:227], %[vo], %[rs], %[st] offen offset:0 sc1\n\t"
"s_add_u32 %[st], %[so], 8192\n\t"
"buffer_load_dwordx4 a[36:39], %[vo], %[rs], %[st] offen offset:1024 sc1\n\t"
"s_add_u32 %[st], %[so], 24576\n\t"
"buffer_load_dwordx4 a[100:103], %[vo], %[rs], %[st] offen offset:1024 sc1\n\t"
"s_add_u32 %[st], %[so], 40960\n\t"
"buffer_load_dwordx4 a[164:167], %[vo], %[rs], %[st] offen offset:1024 sc1\n\t"
"s_add_u32 %[st], %[so], 57344\n\t"
"buffer_load_dwordx4 a[228:231], %[vo], %[rs], %[st] offen offset:1024 sc1\n\t"
"s_add_u32 %[st], %[so], 8192\n\t"
"buffer_load_dwordx4 a[40:43], %[vo], %[rs], %[st] offen offset:2048 sc1\n\t"
"s_add_u32 %[st], %[so], 24576\n\t"
"buffer_load_dwordx4 a[104:107], %[vo], %[rs], %[st] offen offset:2048 sc1\n\t"
"s_add_u32 %[st], %[so], 40960\n\t"
"buffer_load_dwordx4 a[168:171], %[vo], %[rs], %[st] offen offset:2048 sc1\n\t"
"s_add_u32 %[st], %[so], 57344\n\t"
"buffer_load_dwordx4 a[232:235], %[vo], %[rs], %[st] offen offset:2048 sc1\n\t"
"s_add_u32 %[st], %[so], 8192\n\t"
"buffer_load_dwordx4 a[44:47], %[vo], %[rs], %[st] offen offset:3072 sc1\n\t"
"s_add_u32 %[st], %[so], 24576\n\t"
"buffer_load_dwordx4 a[108:111], %[vo], %[rs], %[st] offen offset:3072 sc1\n\t"
"s_add_u32 %[st], %[so], 40960\n\t"
"buffer_load_dwordx4 a[172:175], %[vo], %[rs], %[st] offen offset:3072 sc1\n\t"
"s_add_u32 %[st], %[so], 57344\n\t"
"buffer_load_dwordx4 a[236:239], %[vo], %[rs], %[st] offen offset:3072 sc1\n\t"
"s_add_u32 %[st], %[so], 12288\n\t"
"buffer_load_dwordx4 a[48:51], %[vo], %[rs], %[st] offen offset:0 sc1\n\t"
"s_add_u32 %[st], %[so], 28672\n\t"
"buffer_load_dwordx4 a[112:115], %[vo], %[rs], %[st] offen offset:0 sc1\n\t"
"s_add_u32 %[st], %[so], 45056\n\t"
"buffer_load_dwordx4 a[176:179], %[vo], %[rs], %[st] offen offset:0 sc1\n\t"
"s_add_u32 %[st], %[so], 61440\n\t"
"buffer_load_dwordx4 a[240:243], %[vo], %[rs], %[st] offen offset:0 sc1\n\t"
"s_add_u32 %[st], %[so], 12288\n\t"
"buffer_load_dwordx4 a[52:55], %[vo], %[rs], %[st] offen offset:1024 sc1\n\t"
"s_add_u32 %[st], %[so], 28672\n\t"
"buffer_load_dwordx4 a[116:119], %[vo], %[rs], %[st] offen offset:1024 sc1\n\t"
"s_add_u32 %[st], %[so], 45056\n\t"
"buffer_load_dwordx4 a[180:183], %[vo], %[rs], %[st] offen offset:1024 sc1\n\t"
"s_add_u32 %[st], %[so], 61440\n\t"
"buffer_load_dwordx4 a[244:247], %[vo], %[rs], %[st] offen offset:1024 sc1\n\t"
"s_add_u32 %[st], %[so], 12288\n\t"
"buffer_load_dwordx4 a[56:59], %[vo], %[rs], %[st] offen offset:2048 sc1\n\t"
"s_add_u32 %[st], %[so], 28672\n\t"
"buffer_load_dwordx4 a[120:123], %[vo], %[rs], %[st] offen offset:2048 sc1\n\t"
"s_add_u32 %[st], %[so], 45056\n\t"
"buffer_load_dwordx4 a[184:187], %[vo], %[rs], %[st] offen offset:2048 sc1\n\t"
"s_add_u32 %[st], %[so], 61440\n\t"
"buffer_load_dwordx4 a[248:251], %[vo], %[rs], %[st] offen offset:2048 sc1\n\t"
"s_add_u32 %[st], %[so], 12288\n\t"
"buffer_load_dwordx4 a[60:63], %[vo], %[rs], %[st] offen offset:3072 sc1\n\t"
"s_add_u32 %[st], %[so], 28672\n\t"
"buffer_load_dwordx4 a[124:127], %[vo], %[rs], %[st] offen offset:3072 sc1\n\t"
"s_add_u32 %[st], %[so], 45056\n\t"
"buffer_load_dwordx4 a[188:191], %[vo], %[rs], %[st] offen offset:3072 sc1\n\t"
"s_add_u32 %[st], %[so], 61440\n\t"
"buffer_load_dwordx4 a[252:255], %[vo], %[rs], %[st] offen offset:3072 sc1\n\t"
"s_waitcnt vmcnt(60)\n\t"
      : [st] "=&s"(st) : [rs] "s"(rx), [vo] "v"(vx), [so] "s"(so) : "memory", "scc", CLOB_A);
  };
  const int trow = 4 * g + q;
  int ta[4], tb[4];
  #pragma unroll
  for (int t = 0; t < 4; ++t) { ta[t] = trow * 128 + ((t ^ ((trow >> 1) & 3)) << 5) + pp * 8; tb[t] = ta[t] + 65536; }
  const __amdgpu_buffer_rsrc_t rw = mkrsrc(ws, W_END);
  unsigned tbl = 0;
  GAS unsigned* cnt = (GAS unsigned*)(ws + W_CTL) + C_CNT;
  const unsigned vo = lane * 16;
  const bool two = f < 120;
  const int h1 = f >> 2, hf1 = (f >> 1) & 1;
  const int h2 = f < 64 ? f >> 1 : (f - 64) >> 1;
  const unsigned add2 = f < 64 ? 256u : 65536u;
  #pragma unroll 1
  for (int hh = 0; hh < 2; ++hh) {
    const int seg = hh * 4 + w;
    u4 A[2][16]; f4 acc[2][4];
    if (hh) wait_done(ws, 128, 32, tag);
    #pragma unroll
    for (int k = 0; k < 16; ++k)
      #pragma unroll
      for (int i = 0; i < 2; ++i) A[i][k] = ld16(rx, vo + (k & 3) * 1024, (unsigned)((2 * seg + i) * 16384 + (k >> 2) * 4096));
    __builtin_amdgcn_sched_barrier(0);
    if (!hh) { asm volatile("s_waitcnt vmcnt(32)\n\ts_barrier" ::: "memory"); }
    if (f & 1) mini_tile<true>(A, acc, lds, ta); else mini_tile<false>(A, acc, lds, ta);
    if (!hh) tbl = get_tbl(ws, tag);
    if (f & 1) {
      #pragma unroll
      for (int t = 0; t < 4; ++t) st16(pack8(acc[0][t], acc[1][t]), rw, vo, fmap(tbl, F_KV + (unsigned)((seg * 32 + h1) * 16384 + 8192)) + (4 * hf1 + t) * 1024);
    } else {
      #pragma unroll
      for (int i = 0; i < 2; ++i)
        #pragma unroll
        for (int u = 0; u < 2; ++u) st16(pack8(acc[i][2 * u], acc[i][2 * u + 1]), rw, vo, fmap(tbl, F_KV + (unsigned)(seg * 32 + h1) * 16384u) + (unsigned)((i * 4 + 2 * hf1 + u) * 1024));
    }
    asm volatile("s_waitcnt vmcnt(0)" ::: "memory");
    if (lane == 0) __hip_atomic_fetch_add(cnt + h1 * 32, hh ? 1u : 1u << 24, RLX_AGENT);
  }
  wait_done(ws, 0, 32, tag);
  load_a(1u << 18);
  if (two) stage(128 + f, 1, 4);
  for (int r = 1; r <= (two ? 8 : 7); ++r) {
    const int rb = r & 7;
    const int seg0 = rb * 8 + w * 2;
    const unsigned so = (unsigned)((rb + 1) & 7) << 18;
    f4 acc[4][4];
    GAS unsigned* fl2 = cnt + (((rb + 7) & 7) * 32 + h2) * 32;
    const unsigned a2 = (two && r > 1) ? add2 : 0u;
    if (rb == 0) {
      asm volatile("s_waitcnt vmcnt(0)" ::: "memory");
      if (lane == 0) __hip_atomic_fetch_add(fl2, add2, RLX_AGENT);
    } else if (f & 1) {
      gemm_chunk<0>(acc, ta, rx, vx, so, fl2, a2);
      #pragma unroll
      for (int s = 0; s < 2; ++s)
        #pragma unroll
        for (int t = 0; t < 4; ++t)
          st16(pack8(acc[2 * s][t], acc[2 * s + 1][t]), rw, vo, fmap(tbl, F_KV + (unsigned)(((seg0 + s) * 32 + h1) * 16384 + 8192)) + (4 * hf1 + t) * 1024);
    } else {
      gemm_chunk<1>(acc, ta, rx, vx, so, fl2, a2);
      #pragma unroll
      for (int i = 0; i < 4; ++i)
        #pragma unroll
        for (int u = 0; u < 2; ++u)
          st16(pack8(acc[i][2 * u], acc[i][2 * u + 1]), rw, vo, fmap(tbl, F_KV + (unsigned)((seg0 + (i >> 1)) * 32 + h1) * 16384u) + (unsigned)(((i & 1) * 4 + 2 * hf1 + u) * 1024));
    }
    GAS unsigned* fl1 = cnt + (rb * 32 + h1) * 32;
    const unsigned a1 = rb ? 1u : 0u;
    if (r == 1 && two) asm volatile("s_barrier" ::: "memory");
    if (!two) {
      asm volatile("s_waitcnt vmcnt(0)" ::: "memory");
      if (lane == 0) __hip_atomic_fetch_add(fl1, 1u, RLX_AGENT);
    }
    if (!two) { if (rb < 7) load_a(so); continue; }
    gemm_chunk<2>(acc, tb, rb ? rx : mkrsrc(ws, 0u), vx, so, fl1, a1);
    if (f < 64) {
      #pragma unroll
      for (int i = 0; i < 4; ++i)
        #pragma unroll
        for (int u = 0; u < 2; ++u)
          st16(pack8(acc[i][2 * u], acc[i][2 * u + 1]), rw, vo, fmap(tbl, F_QF + (unsigned)((seg0 + (i >> 1)) * 32 + h2) * 8192u) + (unsigned)(((i & 1) * 4 + 2 * (f & 1) + u) * 1024));
    } else {
      #pragma unroll
      for (int i = 0; i < 4; ++i) {
        f4 y[2];
        #pragma unroll
        for (int t = 0; t < 2; ++t)
          #pragma unroll
          for (int r = 0; r < 4; ++r) y[t][r] = geglu(acc[i][t][r], acc[i][2 + t][r]);
        st16(pack8(y[0], y[1]), rw, vo, fmap(tbl, F_HF + (unsigned)(((seg0 + (i >> 1)) * 56 + (f - 64)) * 2048)) + (i & 1) * 1024);
      }
    }
  }
  asm volatile("s_waitcnt vmcnt(0)" ::: "memory");
  if (two && lane == 0) __hip_atomic_fetch_add(cnt + h2 * 32, add2, RLX_AGENT);
  BARRIER();
}

DI void t_role(const Params& p, unsigned char* ws, int hp, int ph, int w, unsigned tag, LAS unsigned char* lds) {
  constexpr int NPH = 8, L_B = 131072, L_RED = L_B + 24576;
  const int lane = lane_id(), tid = w * 64 + lane;
  const int g = lane >> 4, c = lane & 15, q = (lane >> 2) & 3, pp = lane & 3;
  const bool hasf = hp < 14;
  const unsigned vo = lane * 16;
  u4 Wt[8][8];
  #pragma unroll
  for (int r = 0; r < 3; ++r) {
    if (r == 2 && !hasf) break;
    const u16* src = r < 2 ? p.Wo + (size_t)(2 * hp + r) * DH * DM : p.wd + (size_t)hp * 128 * DM;
    #pragma unroll 4
    for (int i = 0; i < 32; ++i) {
      const int row = w * 32 + i;
      dma16(src + (size_t)row * DM + ((lane ^ ((row & 3) << 2)) * 8), row * 1024);
    }
    asm volatile("s_waitcnt vmcnt(0)\n\ts_barrier" ::: "memory");
    #pragma unroll
    for (int k = 0; k < 4; ++k) {
      u4 tmp[8];
      #pragma unroll
      for (int t = 0; t < 8; ++t) {
        const int chunk = (16 * w + 4 * (t >> 1) + pp) ^ (q << 2);
        LAS unsigned char* a = lds + (32 * k + 4 * g + q) * 1024 + chunk * 16 + (t & 1) * 8;
        u2 lo = trd(a), hi = trd(a + 16384);
        if (r < 2) Wt[4 * r + k][t] = (u4){lo.x, lo.y, hi.x, hi.y}; else tmp[t] = (u4){lo.x, lo.y, hi.x, hi.y};
      }
      if (r == 2) {
        BARRIER();
        #pragma unroll
        for (int t = 0; t < 8; ++t) *(LAS u4*)(lds + k * 32768 + w * 8192 + t * 1024 + vo) = tmp[t];
      }
    }
    BARRIER();
  }
  u4 sel0, sel1;
  {
    const unsigned one = 0x3F80u << (16 * (c & 1));
    const bool on = (c >> 2) == g;
    const unsigned lo = on && !(c & 2) ? one : 0u, hi = on && (c & 2) ? one : 0u;
    sel0 = (u4){lo, hi, 0u, 0u}; sel1 = (u4){0u, 0u, lo, hi};
  }
  const __amdgpu_buffer_rsrc_t rw = mkrsrc(ws, W_END);
  const unsigned tbl = get_tbl(ws, tag);
  unsigned* ctl = (unsigned*)(ws + W_CTL);
  const int qt = w & 1, hs = w >> 1, h = 2 * hp + hs;
  const float sc = 0.08838834764831845f * 1.4426950408889634f;
  const float NEG = -1.0e30f;
  LAS f4* red = (LAS f4*)(lds + L_RED);
  const unsigned ooff = (unsigned)(((h >> 4) * 16 + c) * DM + (h & 15) * 32 + 8 * g) * 2u;
  const __amdgpu_buffer_rsrc_t rxi = mkrsrc(p.x, SEQ * DM * 2), rout = mkrsrc(p.out, SEQ * DM * 2);
  auto red_load = [&](int s, u4 (&in)[8], u4& xv, __amdgpu_buffer_rsrc_t rr) {
    #pragma unroll
    for (int i = 0; i < 8; ++i) in[i] = ld16(rr, vo + (i & 3) * 1024, fmap(tbl, F_PART + (unsigned)(((s * 32 + h) * 16 + qt * 8) * 1024)) + (i >> 2) * 4096);
    xv = __builtin_amdgcn_raw_buffer_load_b128(rxi, ooff, s * (32 * DM * 2), 0);
  };
  auto red_sum = [&](const u4 (&in)[8], const u4& xv, f4& a, f4& b) {
    a = (f4){0.f, 0.f, 0.f, 0.f}; b = a;
    #pragma unroll
    for (int i = 0; i < 8; ++i) { a = mfma32(sel0, in[i], a); b = mfma32(sel1, in[i], b); }
    if (qt) { red[(hs * 2) * 64 + lane] = a; red[(hs * 2 + 1) * 64 + lane] = b; }
    else { a = mfma32(sel0, xv, a); b = mfma32(sel1, xv, b); }
  };
  auto red_fin = [&](int s, f4 a, f4 b) {
    if (!qt) { unsigned o = ooff + s * (32 * DM * 2); asm volatile("" : "+v"(o)); __builtin_amdgcn_raw_buffer_store_b128(pack8(a + red[(hs * 2) * 64 + lane], b + red[(hs * 2 + 1) * 64 + lane]), rout, o, 0, 0); }
  };

  const unsigned cnt_full = 16u + 8u * 256u + (hasf ? 8u * 65536u : 0u);
  u4 Q[4], K0[4], K1[4], HF[2], in[8], xv;
  auto pre_load = [&](int s, __amdgpu_buffer_rsrc_t rr) {
    const unsigned kb = fmap(tbl, F_KV + (unsigned)(s * 32 + h) * 16384u);
    const unsigned qb = fmap(tbl, F_QF + (unsigned)(s * 32 + h) * 8192u) + qt * 4096, hb = fmap(tbl, F_HF + (unsigned)((s * 56 + (hasf ? 4 * hp + w : 0)) * 2048));
    #pragma unroll
    for (int t = 0; t < 2; ++t) HF[t] = ld16(rr, vo + t * 1024, hb);
    red_load(s >= 2 * NPH ? s - 2 * NPH : s, in, xv, mkrsrc(ws, s >= 2 * NPH && s < NSEG ? W_END : 0u));
    #pragma unroll
    for (int t = 0; t < 4; ++t) { Q[t] = ld16(rr, vo + t * 1024, qb); K0[t] = ld16(rr, vo + t * 1024, kb); K1[t] = ld16(rr, vo + t * 1024, kb + qt * 4096); }
  };
  const bool fast = ph >= NPH - 2;
  if (fast) {
    const u4 pz = {POISON, POISON, POISON, POISON};
    #pragma unroll
    for (int i = 0; i < 8; ++i) st16(pz, rw, vo + i * 1024, fmap(tbl, F_PART + (unsigned)((((NSEG - NPH + ph) * 32 + h) * 16 + qt * 8) * 1024)));
  }
  auto has_poison = [&](unsigned acc_eq) { return __any(acc_eq != 0u); };
  if (!qt) wait_ge(ctl + C_CNT + h * 32, 0xFFFFFFFFu, cnt_full + (16u << 24));
  BARRIER();
  pre_load(ph, rw);
  for (int s = ph; s < NSEG; s += NPH) {
    const int sr = s - 2 * NPH;
    const unsigned vb = fmap(tbl, F_KV + (unsigned)(s * 32 + h) * 16384u + 8192u);
    const unsigned kmb = (unsigned)W_KMV + (unsigned)(s * 32 + h) * 4096u;
    u4 KM[4], V[8]; u2 VM[4];
    f4 s0 = {0.f, 0.f, 0.f, 0.f}, s1 = s0, sm = s0;
    *(LAS u4*)(lds + L_B + ((8 + w) * 2) * 1024 + vo) = HF[0];
    *(LAS u4*)(lds + L_B + ((8 + w) * 2 + 1) * 1024 + vo) = HF[1];
    f4 ra, rb;
    red_sum(in, xv, ra, rb);
    #pragma unroll
    for (int t = 0; t < 4; ++t) { s0 = mfma32(K0[t], Q[t], s0); s1 = mfma32(K1[t], Q[t], s1); }
    __builtin_amdgcn_sched_barrier(0);
    const unsigned prog0 = __hip_atomic_load((const GAS unsigned*)ctl + C_PROG + h * 32, RLX_AGENT);
    #pragma unroll
    for (int t = 0; t < 8; ++t) V[t] = ld16(rw, vo + (t & 3) * 1024, vb + (t >> 2) * 4096);
    __builtin_amdgcn_sched_barrier(0);
    asm volatile("s_waitcnt vmcnt(8)" ::: "memory");
    BARRIER();
    if (tid == 64 && s >= NPH) __hip_atomic_fetch_add((GAS unsigned*)ctl + C_SEG + (s - NPH) * 32, 1u, RLX_AGENT);
    if (sr >= 0) red_fin(sr, ra, rb);
    if (s + NPH >= NSEG) {
      if (w == 1) wait_ge(ctl + C_SEG + (s - NPH) * 32, 0xFFFFFFFFu, 16u);
      BARRIER();
      red_load(s - NPH, in, xv, rw);
      red_sum(in, xv, ra, rb);
      BARRIER();
      red_fin(s - NPH, ra, rb);
    }
    __builtin_amdgcn_sched_barrier(0);
    const bool late = (unsigned)__builtin_amdgcn_readfirstlane(prog0) <= (unsigned)s;
    asm volatile("" ::: "memory");
    auto ld_m = [&]() {
      #pragma unroll
      for (int t = 0; t < 4; ++t) { KM[t] = ld16(rw, g * 128 + (2 * (c >> 2) + (c & 1)) * 16, kmb + t * 512); VM[t] = ld8(rw, lane * 8, kmb + 2048 + t * 512); }
    };
    if (!late) ld_m();
    __builtin_amdgcn_sched_barrier(0);
    f4 acc[2][8];
    #pragma unroll
    for (int t = 0; t < 8; ++t) { acc[0][t] = (f4){0.f, 0.f, 0.f, 0.f}; acc[1][t] = acc[0][t]; }
    if (hasf) {
      u4 a[2][4], b[2][2];
      auto rd = [&](int j) {
        #pragma unroll
        for (int t = 0; t < 4; ++t) a[j & 1][t] = *(LAS u4*)(lds + (j >> 1) * 32768 + w * 8192 + ((j & 1) * 4 + t) * 1024 + vo);
        if (!(j & 1)) { b[(j >> 1) & 1][0] = *(LAS u4*)(lds + L_B + (16 + j) * 1024 + vo); b[(j >> 1) & 1][1] = *(LAS u4*)(lds + L_B + (17 + j) * 1024 + vo); }
      };
      rd(0);
      __builtin_amdgcn_sched_barrier(0);
      #pragma unroll
      for (int j = 0; j < 8; ++j) {
        if (j < 7) rd(j + 1);
        #pragma unroll
        for (int t = 0; t < 4; ++t) { const int tt = (j & 1) * 4 + t; acc[0][tt] = mfma32(a[j & 1][t], b[(j >> 1) & 1][0], acc[0][tt]); acc[1][tt] = mfma32(a[j & 1][t], b[(j >> 1) & 1][1], acc[1][tt]); }
        #pragma unroll
        for (int n = 0; n < 6; ++n) { __builtin_amdgcn_sched_group_barrier(0x8, 1, 0); __builtin_amdgcn_sched_group_barrier(0x100, 1, 0); }
        __builtin_amdgcn_sched_barrier(0);
      }
    }
    float mx = NEG;
    #pragma unroll
    for (int r = 0; r < 4; ++r) {
      const bool diag = (4 * g + r) <= c;
      s0[r] = (qt || diag) ? s0[r] : NEG;
      s1[r] = (qt && diag) ? s1[r] : NEG;
      mx = fmaxf(mx, fmaxf(s0[r], s1[r]));
    }
    __builtin_amdgcn_sched_barrier(0);
    if (late) {
      if (s < NSEG - 2) { wait_ge(ctl + C_PROG + h * 32, 0xFFFFFFFFu, (unsigned)s + 1u); ld_m(); }
      else {
        touch_code<1>(tid, 256);
        wait_ge(ctl + C_PROG + h * 32, 0xFFFFFFFFu, (unsigned)s - 1u);
        for (unsigned n = 0; n < (1u << 16); ++n) {
          asm volatile("" ::: "memory");
          ld_m();
          unsigned e = 0;
          #pragma unroll
          for (int t = 0; t < 4; ++t) e |= (KM[t].x == POISON) | (KM[t].y == POISON) | (KM[t].z == POISON) | (KM[t].w == POISON) | (VM[t].x == POISON) | (VM[t].y == POISON);
          if (!has_poison(e)) break;
        }
      }
    }
    #pragma unroll
    for (int t = 0; t < 4; ++t) sm = mfma32(KM[t], Q[t], sm);
    mx = fmaxf(mx, fmaxf(sm[0], sm[1]));
    mx = allmax_g(mx);
    const float nb = -mx * sc;
    float l = 0.f;
    #pragma unroll
    for (int r = 0; r < 4; ++r) {
      s0[r] = __builtin_amdgcn_exp2f(__builtin_fmaf(s0[r], sc, nb));
      s1[r] = __builtin_amdgcn_exp2f(__builtin_fmaf(s1[r], sc, nb));
      l += s0[r] + s1[r];
    }
    sm[0] = __builtin_amdgcn_exp2f(__builtin_fmaf(sm[0], sc, nb));
    sm[1] = __builtin_amdgcn_exp2f(__builtin_fmaf(sm[1], sc, nb));
    l += sm[0] + sm[1];
    const float rl = __builtin_amdgcn_rcpf(allsum_g(l));
    const u4 px = pack8(s0, s1);
    const unsigned pmd = pk_bf16(sm[0], sm[1]);
    const u2 pm0 = {pmd, 0u}, pm1 = {0u, pmd};
    #pragma unroll
    for (int u = 0; u < 4; ++u) {
      f4 o0 = mfma32(V[2 * u], px, (f4){0.f, 0.f, 0.f, 0.f}), o1 = mfma32(V[2 * u + 1], px, (f4){0.f, 0.f, 0.f, 0.f});
      o0 = mfma16(VM[u], pm0, o0); o1 = mfma16(VM[u], pm1, o1);
      *(LAS u4*)(lds + L_B + ((4 * hs + u) * 2 + qt) * 1024 + vo) = pack8(o0 * rl, o1 * rl);
    }
    const bool more = s + NPH < NSEG;
    if (more) {
      if (!qt) wait_ge(ctl + C_CNT + (((s >> 3) + 1) * 32 + h) * 32, 0xFFFFFFFFu, cnt_full);
      else if (w == 1 && s >= NPH) wait_ge(ctl + C_SEG + (s - NPH) * 32, 0xFFFFFFFFu, 16u);
    }
    BARRIER();
    pre_load(s + NPH, mkrsrc(ws, more ? W_END : 0u));
    #pragma unroll
    for (int k = 0; k < 8; ++k) {
      const u4 b0 = *(LAS u4*)(lds + L_B + (k * 2) * 1024 + vo), b1 = *(LAS u4*)(lds + L_B + (k * 2 + 1) * 1024 + vo);
      #pragma unroll
      for (int t = 0; t < 8; ++t) { acc[0][t] = mfma32(Wt[k][t], b0, acc[0][t]); acc[1][t] = mfma32(Wt[k][t], b1, acc[1][t]); }
    }
    __builtin_amdgcn_sched_group_barrier(0x100, 4, 0);
    #pragma unroll
    for (int k = 0; k < 8; ++k) {
      #pragma unroll
      for (int n = 0; n < 3; ++n) { __builtin_amdgcn_sched_group_barrier(0x8, 4, 0); __builtin_amdgcn_sched_group_barrier(0x20, 1, 0); }
      __builtin_amdgcn_sched_group_barrier(0x8, 4, 0); __builtin_amdgcn_sched_group_barrier(0x100, 2, 0);
    }
    __builtin_amdgcn_sched_barrier(0);
    #pragma unroll
    for (int i = 0; i < 2; ++i)
      #pragma unroll
      for (int u = 0; u < 4; ++u)
        st16(pack8(acc[i][2 * u], acc[i][2 * u + 1]), rw, vo, fmap(tbl, F_PART + (unsigned)(((s * 32 + i * 16 + 4 * w + u) * 16 + hp) * 1024)));
  }
  const int sl = NSEG - NPH + ph;
  f4 ra, rb;
  if (!fast) {
    asm volatile("s_waitcnt vmcnt(0)" ::: "memory");
    BARRIER();
    if (tid == 64) __hip_atomic_fetch_add((GAS unsigned*)ctl + C_SEG + sl * 32, 1u, RLX_AGENT);
    if (w == 0) wait_ge(ctl + C_SEG + sl * 32, 0xFFFFFFFFu, 16u);
    BARRIER();
    red_load(sl, in, xv, rw);
  } else {
    for (unsigned n = 0; n < (1u << 16); ++n) {
      asm volatile("" ::: "memory");
      red_load(sl, in, xv, rw);
      unsigned e = 0;
      #pragma unroll
      for (int i = 0; i < 8; ++i) e |= (in[i].x == POISON) | (in[i].y == POISON) | (in[i].z == POISON) | (in[i].w == POISON);
      if (!has_poison(e)) break;
    }
  }
  red_sum(in, xv, ra, rb);
  BARRIER();
  red_fin(sl, ra, rb);
  if (hp == 0 && ph == NPH - 1 && tid == 0) __hip_atomic_store((GAS unsigned*)ctl + C_EPOCH, tag, RLX_AGENT);
}

#define CLOB_S "s38", "s39", CL10("s",4), CL10("s",5), CL10("s",6), CL10("s",7), CL10("s",8), CL10("s",9)

DI void chain_role(const Params& p, unsigned char* ws, int me, int tw, int w, LAS unsigned char* lds) {
  const int lane = lane_id(), tid = w * 64 + lane;
  const int g = lane >> 4, c = lane & 15;
  LAS unsigned* tab = (LAS unsigned*)(lds + L_W) + tid;
  const int q = (lane >> 2) & 3, pp = lane & 3;
  const int trow = 16 * (g >> 1) + 4 * (g & 1) + (q & 1) + 8 * (q >> 1);
  const int rho = (q & 1) | ((g & 1) << 1) | ((q >> 1) << 2);
  const int lam0 = c ^ ((g & 1) << 1);
  tab[0 * 256] = lane * 16;
  tab[1 * 256] = tid * 16;
  tab[2 * 256] = L_W + tid * 16;
  tab[3 * 256] = L_W + 65536 + tid * 16;
  tab[4 * 256] = (32 * w + c) * 32 + (g ^ (c >> 2)) * 8;
  tab[5 * 256] = (4 * g + q) * 32 + (pp ^ g) * 8;
  const int r8 = 2 * (c >> 2) + (c & 1);
  tab[6 * 256] = (c & 2) ? 3072 + (g >> 1) * 256 + r8 * 32 + ((g & 1) ^ 1) * 16 : ((4 * w + g) >> 1) * 256 + r8 * 32 + (g & 1) * 16;
  tab[7 * 256] = (unsigned)(8 * w + 4 * tw + (c >> 2)) * 16384u + me * 256u + (4 * g + (c & 3)) * 16u;
  tab[8 * 256] = c < 8 ? me * 256u + g * 64u + c * 8u : 0x7FFF0000u;
  tab[9 * 256] = (lane & 1) ? 0xEEEEu : 0x4444u;
  tab[10 * 256] = lane < 8 ? 105 * 16384 + lane * 1024 : 0x7FFF0000;
  tab[11 * 256] = (c & 2) ? g * 128 + r8 * 16 : 0x7FFF0000;
  tab[12 * 256] = lane * 8;
  tab[13 * 256] = g * 8192 + (lam0 << 4);
  tab[14 * 256] = g * 1024 + ((lam0 & 7) << 4) + ((lam0 >> 3) << 16);
  tab[15 * 256] = trow * 256 + (((pp >> 1) ^ (rho << 1)) << 4) + (pp & 1) * 8;
  tab[16 * 256] = (c >> 1) * 1024 + (64 * w + 32 * (c & 1) + 4 * g) * 2;
  tab[17 * 256] = w == 0 ? (lane == 0 ? 105 * 16384 + ((me & 3) + 4 * tw) * 1024 : 0x7FFF0000) : lane * 128;
  asm volatile("s_waitcnt lgkmcnt(0)" ::: "memory");
  asm volatile(
"s_cmp_eq_u32 %[w], 1\n\t"
"s_cbranch_scc1 CHAIN_W1_%=\n\t"
"s_cmp_eq_u32 %[w], 2\n\t"
"s_cbranch_scc1 CHAIN_W2_%=\n\t"
"s_cmp_eq_u32 %[w], 3\n\t"
"s_cbranch_scc1 CHAIN_W3_%=\n\t"
"CHAIN_W0_%=:\n\t"
"v_mbcnt_lo_u32_b32 v127, -1, 0\n\t"
"v_mbcnt_hi_u32_b32 v127, -1, v127\n\t"
"v_lshlrev_b32 v127, 2, v127\n\t"
"v_add_u32 v127, 0, v127\n\t"
"ds_read_b32 v0, v127 offset:32768\n\t"
"ds_read_b32 v1, v127 offset:33792\n\t"
"ds_read_b32 v2, v127 offset:34816\n\t"
"ds_read_b32 v3, v127 offset:35840\n\t"
"ds_read_b32 v4, v127 offset:36864\n\t"
"ds_read_b32 v5, v127 offset:37888\n\t"
"ds_read_b32 v6, v127 offset:38912\n\t"
"ds_read_b32 v7, v127 offset:39936\n\t"
"ds_read_b32 v8, v127 offset:40960\n\t"
"ds_read_b32 v9, v127 offset:41984\n\t"
"ds_read_b32 v10, v127 offset:43008\n\t"
"ds_read_b32 v11, v127 offset:44032\n\t"
"ds_read_b32 v12, v127 offset:45056\n\t"
"ds_read_b32 v16, v127 offset:46080\n\t"
"ds_read_b32 v20, v127 offset:47104\n\t"
"s_waitcnt lgkmcnt(0)\n\t"
"ds_read_b32 v36, v127 offset:48128\n\t"
"ds_read_b32 v26, v127 offset:49152\n\t"
"ds_read_b32 v13, v127 offset:50176\n\t"
"s_waitcnt lgkmcnt(0)\n\t"
"s_barrier\n\t"
"v_xor_b32 v17, 64, v16\n\t"
"v_xor_b32 v21, 64, v20\n\t"
"v_xor_b32 v18, 128, v16\n\t"
"v_xor_b32 v22, 65536, v20\n\t"
"v_xor_b32 v19, 192, v16\n\t"
"v_xor_b32 v23, 65600, v20\n\t"
"v_xor_b32 v24, 0, v36\n\t"
"v_xor_b32 v25, 32, v36\n\t"
"v_xor_b32 v28, 0, v36\n\t"
"v_add_u32 v28, 0, v28\n\t"
"v_xor_b32 v29, 32, v36\n\t"
"v_add_u32 v29, 0, v29\n\t"
"v_xor_b32 v30, 64, v36\n\t"
"v_add_u32 v30, 0, v30\n\t"
"v_xor_b32 v31, 96, v36\n\t"
"v_add_u32 v31, 0, v31\n\t"
"v_xor_b32 v32, 128, v36\n\t"
"v_add_u32 v32, 0, v32\n\t"
"v_xor_b32 v33, 160, v36\n\t"
"v_add_u32 v33, 0, v33\n\t"
"v_xor_b32 v34, 192, v36\n\t"
"v_add_u32 v34, 0, v34\n\t"
"v_xor_b32 v35, 224, v36\n\t"
"v_add_u32 v35, 0, v35\n\t"
"s_mov_b32 s82, 0xff00ff00\n\t"
"s_mov_b32 s83, 0xff00ff00\n\t"
"s_mov_b32 s56, 0xffff\n\t"
"s_mov_b32 s57, 0\n\t"
"s_mov_b64 s[44:45], %[kv]\n\t"
"s_and_b32 s45, s45, 0xffff\n\t"
"s_mov_b32 s46, 67108864\n\t"
"s_mov_b32 s47, 0x20000\n\t"
"s_mov_b64 s[52:53], %[kmv]\n\t"
"s_and_b32 s53, s53, 0xffff\n\t"
"s_mov_b32 s54, %[kmsz]\n\t"
"s_mov_b32 s55, 0x20000\n\t"
"s_mov_b64 s[84:85], %[wq]\n\t"
"s_and_b32 s85, s85, 0xffff\n\t"
"s_mov_b32 s86, 4194304\n\t"
"s_mov_b32 s87, 0x20000\n\t"
"s_mov_b64 s[88:89], %[wk]\n\t"
"s_and_b32 s89, s89, 0xffff\n\t"
"s_mov_b32 s90, 4194304\n\t"
"s_mov_b32 s91, 0x20000\n\t"
"s_mov_b64 s[92:93], %[wv]\n\t"
"s_and_b32 s93, s93, 0xffff\n\t"
"s_mov_b32 s94, 4194304\n\t"
"s_mov_b32 s95, 0x20000\n\t"
"s_mov_b64 s[96:97], %[wo]\n\t"
"s_and_b32 s97, s97, 0xffff\n\t"
"s_mov_b32 s98, 130944\n\t"
"s_mov_b32 s99, 0x20000\n\t"
"s_mov_b64 s[60:61], %[mem0]\n\t"
"s_and_b32 s61, s61, 0xffff\n\t"
"s_mov_b32 s62, 8192\n\t"
"s_mov_b32 s63, 0x20000\n\t"
"buffer_load_dwordx2 v[48:49], v26, s[60:63], 0 offen offset:0\n\t"
"buffer_load_dwordx2 v[50:51], v26, s[60:63], 0 offen offset:32\n\t"
"buffer_load_dwordx2 v[52:53], v26, s[60:63], 0 offen offset:512\n\t"
"buffer_load_dwordx2 v[54:55], v26, s[60:63], 0 offen offset:544\n\t"
"v_mov_b32 v57, 0\n\t"
"global_load_dword v56, v57, %[ctl] offset:128 sc1\n\t"
"v_mov_b32 v60, 0xffbfffbf\n\t"
"v_mov_b32 v61, 0xffbfffbf\n\t"
"v_mov_b32 v62, 0xffbfffbf\n\t"
"v_mov_b32 v63, 0xffbfffbf\n\t"
"s_mov_b32 s74, 8126464\n\t"
"buffer_store_dwordx4 v[60:63], v1, s[52:55], s74 offen sc1\n\t"
"s_mov_b32 s74, 8257536\n\t"
"buffer_store_dwordx4 v[60:63], v1, s[52:55], s74 offen sc1\n\t"
"s_mov_b32 m0, 32768\n\t"
"s_mov_b32 s74, 2097152\n\t"
"buffer_load_dwordx4 v16, s[84:87], s74 offen lds\n\t"
"s_mov_b32 m0, 33792\n\t"
"s_mov_b32 s74, 2228224\n\t"
"buffer_load_dwordx4 v17, s[84:87], s74 offen lds\n\t"
"s_mov_b32 m0, 34816\n\t"
"s_mov_b32 s74, 2359296\n\t"
"buffer_load_dwordx4 v18, s[84:87], s74 offen lds\n\t"
"s_mov_b32 m0, 35840\n\t"
"s_mov_b32 s74, 2490368\n\t"
"buffer_load_dwordx4 v19, s[84:87], s74 offen lds\n\t"
"s_mov_b32 m0, 49152\n\t"
"s_mov_b32 s74, 2621440\n\t"
"buffer_load_dwordx4 v16, s[84:87], s74 offen lds\n\t"
"s_mov_b32 m0, 50176\n\t"
"s_mov_b32 s74, 2752512\n\t"
"buffer_load_dwordx4 v17, s[84:87], s74 offen lds\n\t"
"s_mov_b32 m0, 51200\n\t"
"s_mov_b32 s74, 2883584\n\t"
"buffer_load_dwordx4 v18, s[84:87], s74 offen lds\n\t"
"s_mov_b32 m0, 52224\n\t"
"s_mov_b32 s74, 3014656\n\t"
"buffer_load_dwordx4 v19, s[84:87], s74 offen lds\n\t"
"s_mov_b32 m0, 65536\n\t"
"s_mov_b32 s74, 3145728\n\t"
"buffer_load_dwordx4 v16, s[84:87], s74 offen lds\n\t"
"s_mov_b32 m0, 66560\n\t"
"s_mov_b32 s74, 3276800\n\t"
"buffer_load_dwordx4 v17, s[84:87], s74 offen lds\n\t"
"s_mov_b32 m0, 67584\n\t"
"s_mov_b32 s74, 3407872\n\t"
"buffer_load_dwordx4 v18, s[84:87], s74 offen lds\n\t"
"s_mov_b32 m0, 68608\n\t"
"s_mov_b32 s74, 3538944\n\t"
"buffer_load_dwordx4 v19, s[84:87], s74 offen lds\n\t"
"s_mov_b32 m0, 81920\n\t"
"s_mov_b32 s74, 3670016\n\t"
"buffer_load_dwordx4 v16, s[84:87], s74 offen lds\n\t"
"s_mov_b32 m0, 82944\n\t"
"s_mov_b32 s74, 3801088\n\t"
"buffer_load_dwordx4 v17, s[84:87], s74 offen lds\n\t"
"s_mov_b32 m0, 83968\n\t"
"s_mov_b32 s74, 3932160\n\t"
"buffer_load_dwordx4 v18, s[84:87], s74 offen lds\n\t"
"s_mov_b32 m0, 84992\n\t"
"s_mov_b32 s74, 4063232\n\t"
"buffer_load_dwordx4 v19, s[84:87], s74 offen lds\n\t"
"s_mov_b32 m0, 98304\n\t"
"s_mov_b32 s74, 2097152\n\t"
"buffer_load_dwordx4 v16, s[88:91], s74 offen lds\n\t"
"s_mov_b32 m0, 99328\n\t"
"s_mov_b32 s74, 2228224\n\t"
"buffer_load_dwordx4 v17, s[88:91], s74 offen lds\n\t"
"s_mov_b32 m0, 100352\n\t"
"s_mov_b32 s74, 2359296\n\t"
"buffer_load_dwordx4 v18, s[88:91], s74 offen lds\n\t"
"s_mov_b32 m0, 101376\n\t"
"s_mov_b32 s74, 2490368\n\t"
"buffer_load_dwordx4 v19, s[88:91], s74 offen lds\n\t"
"s_mov_b32 m0, 114688\n\t"
"s_mov_b32 s74, 2621440\n\t"
"buffer_load_dwordx4 v16, s[88:91], s74 offen lds\n\t"
"s_mov_b32 m0, 115712\n\t"
"s_mov_b32 s74, 2752512\n\t"
"buffer_load_dwordx4 v17, s[88:91], s74 offen lds\n\t"
"s_mov_b32 m0, 116736\n\t"
"s_mov_b32 s74, 2883584\n\t"
"buffer_load_dwordx4 v18, s[88:91], s74 offen lds\n\t"
"s_mov_b32 m0, 117760\n\t"
"s_mov_b32 s74, 3014656\n\t"
"buffer_load_dwordx4 v19, s[88:91], s74 offen lds\n\t"
"v_add_u32 v38, 32768, v24\n\t"
"v_add_u32 v39, 32768, v25\n\t"
"v_add_u32 v36, 98304, v24\n\t"
"v_add_u32 v37, 98304, v25\n\t"
"v_add_u32 v28, 32768, v28\n\t"
"v_add_u32 v29, 32768, v29\n\t"
"v_add_u32 v30, 32768, v30\n\t"
"v_add_u32 v31, 32768, v31\n\t"
"v_add_u32 v32, 32768, v32\n\t"
"v_add_u32 v33, 32768, v33\n\t"
"v_add_u32 v34, 32768, v34\n\t"
"v_add_u32 v35, 32768, v35\n\t"
"s_waitcnt vmcnt(16)\n\t"
"s_barrier\n\t"
"s_mov_b32 m0, 131072\n\t"
"s_mov_b32 s74, 3145728\n\t"
"buffer_load_dwordx4 v16, s[88:91], s74 offen lds\n\t"
"ds_read_b64_tr_b16 v[128:129], v38 offset:0\n\t"
"ds_read_b64_tr_b16 v[130:131], v38 offset:512\n\t"
"s_mov_b32 m0, 132096\n\t"
"s_mov_b32 s74, 3276800\n\t"
"buffer_load_dwordx4 v17, s[88:91], s74 offen lds\n\t"
"ds_read_b64_tr_b16 v[132:133], v38 offset:8192\n\t"
"ds_read_b64_tr_b16 v[134:135], v38 offset:8704\n\t"
"s_mov_b32 m0, 133120\n\t"
"s_mov_b32 s74, 3407872\n\t"
"buffer_load_dwordx4 v18, s[88:91], s74 offen lds\n\t"
"ds_read_b64_tr_b16 a[32:33], v39 offset:0\n\t"
"ds_read_b64_tr_b16 a[34:35], v39 offset:512\n\t"
"s_mov_b32 m0, 134144\n\t"
"s_mov_b32 s74, 3538944\n\t"
"buffer_load_dwordx4 v19, s[88:91], s74 offen lds\n\t"
"ds_read_b64_tr_b16 a[36:37], v39 offset:8192\n\t"
"ds_read_b64_tr_b16 a[38:39], v39 offset:8704\n\t"
"s_mov_b32 m0, 147456\n\t"
"s_mov_b32 s74, 3670016\n\t"
"buffer_load_dwordx4 v16, s[88:91], s74 offen lds\n\t"
"ds_read_b64_tr_b16 v[136:137], v38 offset:16384\n\t"
"ds_read_b64_tr_b16 v[138:139], v38 offset:16896\n\t"
"s_mov_b32 m0, 148480\n\t"
"s_mov_b32 s74, 3801088\n\t"
"buffer_load_dwordx4 v17, s[88:91], s74 offen lds\n\t"
"ds_read_b64_tr_b16 v[140:141], v38 offset:24576\n\t"
"ds_read_b64_tr_b16 v[142:143], v38 offset:25088\n\t"
"s_mov_b32 m0, 149504\n\t"
"s_mov_b32 s74, 3932160\n\t"
"buffer_load_dwordx4 v18, s[88:91], s74 offen lds\n\t"
"ds_read_b64_tr_b16 a[40:41], v39 offset:16384\n\t"
"ds_read_b64_tr_b16 a[42:43], v39 offset:16896\n\t"
"s_mov_b32 m0, 150528\n\t"
"s_mov_b32 s74, 4063232\n\t"
"buffer_load_dwordx4 v19, s[88:91], s74 offen lds\n\t"
"ds_read_b64_tr_b16 a[44:45], v39 offset:24576\n\t"
"ds_read_b64_tr_b16 a[46:47], v39 offset:25088\n\t"
"s_waitcnt lgkmcnt(0)\n\t"
"s_waitcnt vmcnt(16)\n\t"
"s_barrier\n\t"
"s_mov_b32 m0, 32768\n\t"
"s_mov_b32 s74, 0\n\t"
"buffer_load_dwordx4 v16, s[92:95], s74 offen lds\n\t"
"ds_read_b64_tr_b16 v[144:145], v38 offset:32768\n\t"
"ds_read_b64_tr_b16 v[146:147], v38 offset:33280\n\t"
"s_mov_b32 m0, 33792\n\t"
"s_mov_b32 s74, 131072\n\t"
"buffer_load_dwordx4 v17, s[92:95], s74 offen lds\n\t"
"ds_read_b64_tr_b16 v[148:149], v38 offset:40960\n\t"
"ds_read_b64_tr_b16 v[150:151], v38 offset:41472\n\t"
"s_mov_b32 m0, 34816\n\t"
"s_mov_b32 s74, 262144\n\t"
"buffer_load_dwordx4 v18, s[92:95], s74 offen lds\n\t"
"ds_read_b64_tr_b16 a[48:49], v39 offset:32768\n\t"
"ds_read_b64_tr_b16 a[50:51], v39 offset:33280\n\t"
"s_mov_b32 m0, 35840\n\t"
"s_mov_b32 s74, 393216\n\t"
"buffer_load_dwordx4 v19, s[92:95], s74 offen lds\n\t"
"ds_read_b64_tr_b16 a[52:53], v39 offset:40960\n\t"
"ds_read_b64_tr_b16 a[54:55], v39 offset:41472\n\t"
"s_mov_b32 m0, 49152\n\t"
"s_mov_b32 s74, 524288\n\t"
"buffer_load_dwordx4 v16, s[92:95], s74 offen lds\n\t"
"ds_read_b64_tr_b16 v[152:153], v38 offset:49152\n\t"
"ds_read_b64_tr_b16 v[154:155], v38 offset:49664\n\t"
"s_mov_b32 m0, 50176\n\t"
"s_mov_b32 s74, 655360\n\t"
"buffer_load_dwordx4 v17, s[92:95], s74 offen lds\n\t"
"ds_read_b64_tr_b16 v[156:157], v38 offset:57344\n\t"
"ds_read_b64_tr_b16 v[158:159], v38 offset:57856\n\t"
"s_mov_b32 m0, 51200\n\t"
"s_mov_b32 s74, 786432\n\t"
"buffer_load_dwordx4 v18, s[92:95], s74 offen lds\n\t"
"ds_read_b64_tr_b16 a[56:57], v39 offset:49152\n\t"
"ds_read_b64_tr_b16 a[58:59], v39 offset:49664\n\t"
"s_mov_b32 m0, 52224\n\t"
"s_mov_b32 s74, 917504\n\t"
"buffer_load_dwordx4 v19, s[92:95], s74 offen lds\n\t"
"ds_read_b64_tr_b16 a[60:61], v39 offset:57344\n\t"
"ds_read_b64_tr_b16 a[62:63], v39 offset:57856\n\t"
"s_waitcnt lgkmcnt(0)\n\t"
"s_waitcnt vmcnt(16)\n\t"
"s_barrier\n\t"
"s_mov_b32 m0, 65536\n\t"
"s_mov_b32 s74, 1048576\n\t"
"buffer_load_dwordx4 v16, s[92:95], s74 offen lds\n\t"
"ds_read_b64_tr_b16 v[160:161], v36 offset:0\n\t"
"ds_read_b64_tr_b16 v[162:163], v36 offset:512\n\t"
"s_mov_b32 m0, 66560\n\t"
"s_mov_b32 s74, 1179648\n\t"
"buffer_load_dwordx4 v17, s[92:95], s74 offen lds\n\t"
"ds_read_b64_tr_b16 v[164:165], v36 offset:8192\n\t"
"ds_read_b64_tr_b16 v[166:167], v36 offset:8704\n\t"
"s_mov_b32 m0, 67584\n\t"
"s_mov_b32 s74, 1310720\n\t"
"buffer_load_dwordx4 v18, s[92:95], s74 offen lds\n\t"
"ds_read_b64_tr_b16 a[96:97], v37 offset:0\n\t"
"ds_read_b64_tr_b16 a[98:99], v37 offset:512\n\t"
"s_mov_b32 m0, 68608\n\t"
"s_mov_b32 s74, 1441792\n\t"
"buffer_load_dwordx4 v19, s[92:95], s74 offen lds\n\t"
"ds_read_b64_tr_b16 a[100:101], v37 offset:8192\n\t"
"ds_read_b64_tr_b16 a[102:103], v37 offset:8704\n\t"
"s_mov_b32 m0, 81920\n\t"
"s_mov_b32 s74, 1572864\n\t"
"buffer_load_dwordx4 v16, s[92:95], s74 offen lds\n\t"
"ds_read_b64_tr_b16 v[168:169], v36 offset:16384\n\t"
"ds_read_b64_tr_b16 v[170:171], v36 offset:16896\n\t"
"s_mov_b32 m0, 82944\n\t"
"s_mov_b32 s74, 1703936\n\t"
"buffer_load_dwordx4 v17, s[92:95], s74 offen lds\n\t"
"ds_read_b64_tr_b16 v[172:173], v36 offset:24576\n\t"
"ds_read_b64_tr_b16 v[174:175], v36 offset:25088\n\t"
"s_mov_b32 m0, 83968\n\t"
"s_mov_b32 s74, 1835008\n\t"
"buffer_load_dwordx4 v18, s[92:95], s74 offen lds\n\t"
"ds_read_b64_tr_b16 a[104:105], v37 offset:16384\n\t"
"ds_read_b64_tr_b16 a[106:107], v37 offset:16896\n\t"
"s_mov_b32 m0, 84992\n\t"
"s_mov_b32 s74, 1966080\n\t"
"buffer_load_dwordx4 v19, s[92:95], s74 offen lds\n\t"
"ds_read_b64_tr_b16 a[108:109], v37 offset:24576\n\t"
"ds_read_b64_tr_b16 a[110:111], v37 offset:25088\n\t"
"s_waitcnt lgkmcnt(0)\n\t"
"s_waitcnt vmcnt(16)\n\t"
"s_barrier\n\t"
"s_mov_b32 m0, 98304\n\t"
"s_mov_b32 s74, 2097152\n\t"
"buffer_load_dwordx4 v16, s[92:95], s74 offen lds\n\t"
"ds_read_b64_tr_b16 v[176:177], v36 offset:32768\n\t"
"ds_read_b64_tr_b16 v[178:179], v36 offset:33280\n\t"
"s_mov_b32 m0, 99328\n\t"
"s_mov_b32 s74, 2228224\n\t"
"buffer_load_dwordx4 v17, s[92:95], s74 offen lds\n\t"
"ds_read_b64_tr_b16 v[180:181], v36 offset:40960\n\t"
"ds_read_b64_tr_b16 v[182:183], v36 offset:41472\n\t"
"s_mov_b32 m0, 100352\n\t"
"s_mov_b32 s74, 2359296\n\t"
"buffer_load_dwordx4 v18, s[92:95], s74 offen lds\n\t"
"ds_read_b64_tr_b16 a[112:113], v37 offset:32768\n\t"
"ds_read_b64_tr_b16 a[114:115], v37 offset:33280\n\t"
"s_mov_b32 m0, 101376\n\t"
"s_mov_b32 s74, 2490368\n\t"
"buffer_load_dwordx4 v19, s[92:95], s74 offen lds\n\t"
"ds_read_b64_tr_b16 a[116:117], v37 offset:40960\n\t"
"ds_read_b64_tr_b16 a[118:119], v37 offset:41472\n\t"
"s_mov_b32 m0, 114688\n\t"
"s_mov_b32 s74, 2621440\n\t"
"buffer_load_dwordx4 v16, s[92:95], s74 offen lds\n\t"
"ds_read_b64_tr_b16 v[184:185], v36 offset:49152\n\t"
"ds_read_b64_tr_b16 v[186:187], v36 offset:49664\n\t"
"s_mov_b32 m0, 115712\n\t"
"s_mov_b32 s74, 2752512\n\t"
"buffer_load_dwordx4 v17, s[92:95], s74 offen lds\n\t"
"ds_read_b64_tr_b16 v[188:189], v36 offset:57344\n\t"
"ds_read_b64_tr_b16 v[190:191], v36 offset:57856\n\t"
"s_mov_b32 m0, 116736\n\t"
"s_mov_b32 s74, 2883584\n\t"
"buffer_load_dwordx4 v18, s[92:95], s74 offen lds\n\t"
"ds_read_b64_tr_b16 a[120:121], v37 offset:49152\n\t"
"ds_read_b64_tr_b16 a[122:123], v37 offset:49664\n\t"
"s_mov_b32 m0, 117760\n\t"
"s_mov_b32 s74, 3014656\n\t"
"buffer_load_dwordx4 v19, s[92:95], s74 offen lds\n\t"
"ds_read_b64_tr_b16 a[124:125], v37 offset:57344\n\t"
"ds_read_b64_tr_b16 a[126:127], v37 offset:57856\n\t"
"s_waitcnt lgkmcnt(0)\n\t"
"s_waitcnt vmcnt(16)\n\t"
"s_barrier\n\t"
"s_mov_b32 m0, 131072\n\t"
"s_mov_b32 s74, 3145728\n\t"
"buffer_load_dwordx4 v16, s[92:95], s74 offen lds\n\t"
"ds_read_b64_tr_b16 a[128:129], v38 offset:0\n\t"
"ds_read_b64_tr_b16 a[130:131], v38 offset:512\n\t"
"s_mov_b32 m0, 132096\n\t"
"s_mov_b32 s74, 3276800\n\t"
"buffer_load_dwordx4 v17, s[92:95], s74 offen lds\n\t"
"ds_read_b64_tr_b16 a[132:133], v38 offset:8192\n\t"
"ds_read_b64_tr_b16 a[134:135], v38 offset:8704\n\t"
"s_mov_b32 m0, 133120\n\t"
"s_mov_b32 s74, 3407872\n\t"
"buffer_load_dwordx4 v18, s[92:95], s74 offen lds\n\t"
"ds_read_b64_tr_b16 a[192:193], v39 offset:0\n\t"
"ds_read_b64_tr_b16 a[194:195], v39 offset:512\n\t"
"s_mov_b32 m0, 134144\n\t"
"s_mov_b32 s74, 3538944\n\t"
"buffer_load_dwordx4 v19, s[92:95], s74 offen lds\n\t"
"ds_read_b64_tr_b16 a[196:197], v39 offset:8192\n\t"
"ds_read_b64_tr_b16 a[198:199], v39 offset:8704\n\t"
"s_mov_b32 m0, 147456\n\t"
"s_mov_b32 s74, 3670016\n\t"
"buffer_load_dwordx4 v16, s[92:95], s74 offen lds\n\t"
"ds_read_b64_tr_b16 a[136:137], v38 offset:16384\n\t"
"ds_read_b64_tr_b16 a[138:139], v38 offset:16896\n\t"
"s_mov_b32 m0, 148480\n\t"
"s_mov_b32 s74, 3801088\n\t"
"buffer_load_dwordx4 v17, s[92:95], s74 offen lds\n\t"
"ds_read_b64_tr_b16 a[140:141], v38 offset:24576\n\t"
"ds_read_b64_tr_b16 a[142:143], v38 offset:25088\n\t"
"s_mov_b32 m0, 149504\n\t"
"s_mov_b32 s74, 3932160\n\t"
"buffer_load_dwordx4 v18, s[92:95], s74 offen lds\n\t"
"ds_read_b64_tr_b16 a[200:201], v39 offset:16384\n\t"
"ds_read_b64_tr_b16 a[202:203], v39 offset:16896\n\t"
"s_mov_b32 m0, 150528\n\t"
"s_mov_b32 s74, 4063232\n\t"
"buffer_load_dwordx4 v19, s[92:95], s74 offen lds\n\t"
"ds_read_b64_tr_b16 a[204:205], v39 offset:24576\n\t"
"ds_read_b64_tr_b16 a[206:207], v39 offset:25088\n\t"
"s_waitcnt lgkmcnt(0)\n\t"
"s_waitcnt vmcnt(16)\n\t"
"s_barrier\n\t"
"s_mov_b32 m0, 32768\n\t"
"s_mov_b32 s74, 0\n\t"
"buffer_load_dwordx4 v20, s[96:99], s74 offen lds\n\t"
"ds_read_b64_tr_b16 a[144:145], v38 offset:32768\n\t"
"ds_read_b64_tr_b16 a[146:147], v38 offset:33280\n\t"
"s_mov_b32 m0, 33792\n\t"
"s_mov_b32 s74, 16384\n\t"
"buffer_load_dwordx4 v21, s[96:99], s74 offen lds\n\t"
"ds_read_b64_tr_b16 a[148:149], v38 offset:40960\n\t"
"ds_read_b64_tr_b16 a[150:151], v38 offset:41472\n\t"
"s_mov_b32 m0, 34816\n\t"
"s_mov_b32 s74, 4096\n\t"
"buffer_load_dwordx4 v22, s[96:99], s74 offen lds\n\t"
"ds_read_b64_tr_b16 a[208:209], v39 offset:32768\n\t"
"ds_read_b64_tr_b16 a[210:211], v39 offset:33280\n\t"
"s_mov_b32 m0, 35840\n\t"
"s_mov_b32 s74, 20480\n\t"
"buffer_load_dwordx4 v23, s[96:99], s74 offen lds\n\t"
"ds_read_b64_tr_b16 a[212:213], v39 offset:40960\n\t"
"ds_read_b64_tr_b16 a[214:215], v39 offset:41472\n\t"
"s_mov_b32 m0, 49152\n\t"
"s_mov_b32 s74, 256\n\t"
"buffer_load_dwordx4 v20, s[96:99], s74 offen lds\n\t"
"ds_read_b64_tr_b16 a[152:153], v38 offset:49152\n\t"
"ds_read_b64_tr_b16 a[154:155], v38 offset:49664\n\t"
"s_mov_b32 m0, 50176\n\t"
"s_mov_b32 s74, 16640\n\t"
"buffer_load_dwordx4 v21, s[96:99], s74 offen lds\n\t"
"ds_read_b64_tr_b16 a[156:157], v38 offset:57344\n\t"
"ds_read_b64_tr_b16 a[158:159], v38 offset:57856\n\t"
"s_mov_b32 m0, 51200\n\t"
"s_mov_b32 s74, 4352\n\t"
"buffer_load_dwordx4 v22, s[96:99], s74 offen lds\n\t"
"ds_read_b64_tr_b16 a[216:217], v39 offset:49152\n\t"
"ds_read_b64_tr_b16 a[218:219], v39 offset:49664\n\t"
"s_mov_b32 m0, 52224\n\t"
"s_mov_b32 s74, 20736\n\t"
"buffer_load_dwordx4 v23, s[96:99], s74 offen lds\n\t"
"ds_read_b64_tr_b16 a[220:221], v39 offset:57344\n\t"
"ds_read_b64_tr_b16 a[222:223], v39 offset:57856\n\t"
"s_waitcnt lgkmcnt(0)\n\t"
"s_waitcnt vmcnt(16)\n\t"
"s_barrier\n\t"
"s_mov_b32 m0, 65536\n\t"
"s_mov_b32 s74, 512\n\t"
"buffer_load_dwordx4 v20, s[96:99], s74 offen lds\n\t"
"ds_read_b64_tr_b16 a[160:161], v36 offset:0\n\t"
"ds_read_b64_tr_b16 a[162:163], v36 offset:512\n\t"
"s_mov_b32 m0, 66560\n\t"
"s_mov_b32 s74, 16896\n\t"
"buffer_load_dwordx4 v21, s[96:99], s74 offen lds\n\t"
"ds_read_b64_tr_b16 a[164:165], v36 offset:8192\n\t"
"ds_read_b64_tr_b16 a[166:167], v36 offset:8704\n\t"
"s_mov_b32 m0, 67584\n\t"
"s_mov_b32 s74, 4608\n\t"
"buffer_load_dwordx4 v22, s[96:99], s74 offen lds\n\t"
"ds_read_b64_tr_b16 a[224:225], v37 offset:0\n\t"
"ds_read_b64_tr_b16 a[226:227], v37 offset:512\n\t"
"s_mov_b32 m0, 68608\n\t"
"s_mov_b32 s74, 20992\n\t"
"buffer_load_dwordx4 v23, s[96:99], s74 offen lds\n\t"
"ds_read_b64_tr_b16 a[228:229], v37 offset:8192\n\t"
"ds_read_b64_tr_b16 a[230:231], v37 offset:8704\n\t"
"s_mov_b32 m0, 81920\n\t"
"s_mov_b32 s74, 768\n\t"
"buffer_load_dwordx4 v20, s[96:99], s74 offen lds\n\t"
"ds_read_b64_tr_b16 a[168:169], v36 offset:16384\n\t"
"ds_read_b64_tr_b16 a[170:171], v36 offset:16896\n\t"
"s_mov_b32 m0, 82944\n\t"
"s_mov_b32 s74, 17152\n\t"
"buffer_load_dwordx4 v21, s[96:99], s74 offen lds\n\t"
"ds_read_b64_tr_b16 a[172:173], v36 offset:24576\n\t"
"ds_read_b64_tr_b16 a[174:175], v36 offset:25088\n\t"
"s_mov_b32 m0, 83968\n\t"
"s_mov_b32 s74, 4864\n\t"
"buffer_load_dwordx4 v22, s[96:99], s74 offen lds\n\t"
"ds_read_b64_tr_b16 a[232:233], v37 offset:16384\n\t"
"ds_read_b64_tr_b16 a[234:235], v37 offset:16896\n\t"
"s_mov_b32 m0, 84992\n\t"
"s_mov_b32 s74, 21248\n\t"
"buffer_load_dwordx4 v23, s[96:99], s74 offen lds\n\t"
"ds_read_b64_tr_b16 a[236:237], v37 offset:24576\n\t"
"ds_read_b64_tr_b16 a[238:239], v37 offset:25088\n\t"
"s_waitcnt lgkmcnt(0)\n\t"
"s_waitcnt vmcnt(16)\n\t"
"s_barrier\n\t"
"s_mov_b32 m0, 98304\n\t"
"s_mov_b32 s74, 0\n\t"
"buffer_load_dwordx4 v16, s[84:87], s74 offen lds\n\t"
"ds_read_b64_tr_b16 a[176:177], v36 offset:32768\n\t"
"ds_read_b64_tr_b16 a[178:179], v36 offset:33280\n\t"
"s_mov_b32 m0, 99328\n\t"
"s_mov_b32 s74, 131072\n\t"
"buffer_load_dwordx4 v17, s[84:87], s74 offen lds\n\t"
"ds_read_b64_tr_b16 a[180:181], v36 offset:40960\n\t"
"ds_read_b64_tr_b16 a[182:183], v36 offset:41472\n\t"
"s_mov_b32 m0, 100352\n\t"
"s_mov_b32 s74, 262144\n\t"
"buffer_load_dwordx4 v18, s[84:87], s74 offen lds\n\t"
"ds_read_b64_tr_b16 a[240:241], v37 offset:32768\n\t"
"ds_read_b64_tr_b16 a[242:243], v37 offset:33280\n\t"
"s_mov_b32 m0, 101376\n\t"
"s_mov_b32 s74, 393216\n\t"
"buffer_load_dwordx4 v19, s[84:87], s74 offen lds\n\t"
"ds_read_b64_tr_b16 a[244:245], v37 offset:40960\n\t"
"ds_read_b64_tr_b16 a[246:247], v37 offset:41472\n\t"
"s_mov_b32 m0, 114688\n\t"
"s_mov_b32 s74, 524288\n\t"
"buffer_load_dwordx4 v16, s[84:87], s74 offen lds\n\t"
"ds_read_b64_tr_b16 a[184:185], v36 offset:49152\n\t"
"ds_read_b64_tr_b16 a[186:187], v36 offset:49664\n\t"
"s_mov_b32 m0, 115712\n\t"
"s_mov_b32 s74, 655360\n\t"
"buffer_load_dwordx4 v17, s[84:87], s74 offen lds\n\t"
"ds_read_b64_tr_b16 a[188:189], v36 offset:57344\n\t"
"ds_read_b64_tr_b16 a[190:191], v36 offset:57856\n\t"
"s_mov_b32 m0, 116736\n\t"
"s_mov_b32 s74, 786432\n\t"
"buffer_load_dwordx4 v18, s[84:87], s74 offen lds\n\t"
"ds_read_b64_tr_b16 a[248:249], v37 offset:49152\n\t"
"ds_read_b64_tr_b16 a[250:251], v37 offset:49664\n\t"
"s_mov_b32 m0, 117760\n\t"
"s_mov_b32 s74, 917504\n\t"
"buffer_load_dwordx4 v19, s[84:87], s74 offen lds\n\t"
"ds_read_b64_tr_b16 a[252:253], v37 offset:57344\n\t"
"ds_read_b64_tr_b16 a[254:255], v37 offset:57856\n\t"
"s_waitcnt lgkmcnt(0)\n\t"
"s_waitcnt vmcnt(16)\n\t"
"s_barrier\n\t"
"s_mov_b32 m0, 131072\n\t"
"s_mov_b32 s74, 1048576\n\t"
"buffer_load_dwordx4 v16, s[84:87], s74 offen lds\n\t"
"ds_read_b64_tr_b16 v[192:193], v28 offset:0\n\t"
"ds_read_b64_tr_b16 v[194:195], v28 offset:512\n\t"
"ds_read_b64_tr_b16 v[196:197], v28 offset:8192\n\t"
"ds_read_b64_tr_b16 v[198:199], v28 offset:8704\n\t"
"s_mov_b32 m0, 132096\n\t"
"s_mov_b32 s74, 1179648\n\t"
"buffer_load_dwordx4 v17, s[84:87], s74 offen lds\n\t"
"ds_read_b64_tr_b16 v[200:201], v29 offset:0\n\t"
"ds_read_b64_tr_b16 v[202:203], v29 offset:512\n\t"
"ds_read_b64_tr_b16 v[204:205], v29 offset:8192\n\t"
"ds_read_b64_tr_b16 v[206:207], v29 offset:8704\n\t"
"s_mov_b32 m0, 133120\n\t"
"s_mov_b32 s74, 1310720\n\t"
"buffer_load_dwordx4 v18, s[84:87], s74 offen lds\n\t"
"ds_read_b64_tr_b16 v[208:209], v30 offset:0\n\t"
"ds_read_b64_tr_b16 v[210:211], v30 offset:512\n\t"
"ds_read_b64_tr_b16 v[212:213], v30 offset:8192\n\t"
"ds_read_b64_tr_b16 v[214:215], v30 offset:8704\n\t"
"s_mov_b32 m0, 134144\n\t"
"s_mov_b32 s74, 1441792\n\t"
"buffer_load_dwordx4 v19, s[84:87], s74 offen lds\n\t"
"ds_read_b64_tr_b16 v[216:217], v31 offset:0\n\t"
"ds_read_b64_tr_b16 v[218:219], v31 offset:512\n\t"
"ds_read_b64_tr_b16 v[220:221], v31 offset:8192\n\t"
"ds_read_b64_tr_b16 v[222:223], v31 offset:8704\n\t"
"s_mov_b32 m0, 147456\n\t"
"s_mov_b32 s74, 1572864\n\t"
"buffer_load_dwordx4 v16, s[84:87], s74 offen lds\n\t"
"ds_read_b64_tr_b16 v[224:225], v32 offset:0\n\t"
"ds_read_b64_tr_b16 v[226:227], v32 offset:512\n\t"
"ds_read_b64_tr_b16 v[228:229], v32 offset:8192\n\t"
"ds_read_b64_tr_b16 v[230:231], v32 offset:8704\n\t"
"s_mov_b32 m0, 148480\n\t"
"s_mov_b32 s74, 1703936\n\t"
"buffer_load_dwordx4 v17, s[84:87], s74 offen lds\n\t"
"ds_read_b64_tr_b16 v[232:233], v33 offset:0\n\t"
"ds_read_b64_tr_b16 v[234:235], v33 offset:512\n\t"
"ds_read_b64_tr_b16 v[236:237], v33 offset:8192\n\t"
"ds_read_b64_tr_b16 v[238:239], v33 offset:8704\n\t"
"s_mov_b32 m0, 149504\n\t"
"s_mov_b32 s74, 1835008\n\t"
"buffer_load_dwordx4 v18, s[84:87], s74 offen lds\n\t"
"ds_read_b64_tr_b16 v[240:241], v34 offset:0\n\t"
"ds_read_b64_tr_b16 v[242:243], v34 offset:512\n\t"
"ds_read_b64_tr_b16 v[244:245], v34 offset:8192\n\t"
"ds_read_b64_tr_b16 v[246:247], v34 offset:8704\n\t"
"s_mov_b32 m0, 150528\n\t"
"s_mov_b32 s74, 1966080\n\t"
"buffer_load_dwordx4 v19, s[84:87], s74 offen lds\n\t"
"ds_read_b64_tr_b16 v[248:249], v35 offset:0\n\t"
"ds_read_b64_tr_b16 v[250:251], v35 offset:512\n\t"
"ds_read_b64_tr_b16 v[252:253], v35 offset:8192\n\t"
"ds_read_b64_tr_b16 v[254:255], v35 offset:8704\n\t"
"s_waitcnt lgkmcnt(0)\n\t"
"s_waitcnt vmcnt(16)\n\t"
"s_barrier\n\t"
"s_mov_b32 m0, 32768\n\t"
"s_mov_b32 s74, 0\n\t"
"buffer_load_dwordx4 v16, s[88:91], s74 offen lds\n\t"
"s_mov_b32 m0, 33792\n\t"
"s_mov_b32 s74, 131072\n\t"
"buffer_load_dwordx4 v17, s[88:91], s74 offen lds\n\t"
"s_mov_b32 m0, 34816\n\t"
"s_mov_b32 s74, 262144\n\t"
"buffer_load_dwordx4 v18, s[88:91], s74 offen lds\n\t"
"s_mov_b32 m0, 35840\n\t"
"s_mov_b32 s74, 393216\n\t"
"buffer_load_dwordx4 v19, s[88:91], s74 offen lds\n\t"
"s_mov_b32 m0, 49152\n\t"
"s_mov_b32 s74, 524288\n\t"
"buffer_load_dwordx4 v16, s[88:91], s74 offen lds\n\t"
"s_mov_b32 m0, 50176\n\t"
"s_mov_b32 s74, 655360\n\t"
"buffer_load_dwordx4 v17, s[88:91], s74 offen lds\n\t"
"s_mov_b32 m0, 51200\n\t"
"s_mov_b32 s74, 786432\n\t"
"buffer_load_dwordx4 v18, s[88:91], s74 offen lds\n\t"
"s_mov_b32 m0, 52224\n\t"
"s_mov_b32 s74, 917504\n\t"
"buffer_load_dwordx4 v19, s[88:91], s74 offen lds\n\t"
"global_load_dwordx2 v[58:59], v57, %[ctl] sc1\n\t"
"s_waitcnt vmcnt(16)\n\t"
"s_barrier\n\t"
"s_mov_b32 m0, 65536\n\t"
"s_mov_b32 s74, 1048576\n\t"
"buffer_load_dwordx4 v16, s[88:91], s74 offen lds\n\t"
"ds_read_b64_tr_b16 v[64:65], v36 offset:0\n\t"
"ds_read_b64_tr_b16 v[66:67], v36 offset:512\n\t"
"s_mov_b32 m0, 66560\n\t"
"s_mov_b32 s74, 1179648\n\t"
"buffer_load_dwordx4 v17, s[88:91], s74 offen lds\n\t"
"ds_read_b64_tr_b16 v[68:69], v36 offset:8192\n\t"
"ds_read_b64_tr_b16 v[70:71], v36 offset:8704\n\t"
"s_mov_b32 m0, 67584\n\t"
"s_mov_b32 s74, 1310720\n\t"
"buffer_load_dwordx4 v18, s[88:91], s74 offen lds\n\t"
"ds_read_b64_tr_b16 a[0:1], v37 offset:0\n\t"
"ds_read_b64_tr_b16 a[2:3], v37 offset:512\n\t"
"s_mov_b32 m0, 68608\n\t"
"s_mov_b32 s74, 1441792\n\t"
"buffer_load_dwordx4 v19, s[88:91], s74 offen lds\n\t"
"ds_read_b64_tr_b16 a[4:5], v37 offset:8192\n\t"
"ds_read_b64_tr_b16 a[6:7], v37 offset:8704\n\t"
"s_mov_b32 m0, 81920\n\t"
"s_mov_b32 s74, 1572864\n\t"
"buffer_load_dwordx4 v16, s[88:91], s74 offen lds\n\t"
"ds_read_b64_tr_b16 v[80:81], v36 offset:16384\n\t"
"ds_read_b64_tr_b16 v[82:83], v36 offset:16896\n\t"
"s_mov_b32 m0, 82944\n\t"
"s_mov_b32 s74, 1703936\n\t"
"buffer_load_dwordx4 v17, s[88:91], s74 offen lds\n\t"
"ds_read_b64_tr_b16 v[84:85], v36 offset:24576\n\t"
"ds_read_b64_tr_b16 v[86:87], v36 offset:25088\n\t"
"s_mov_b32 m0, 83968\n\t"
"s_mov_b32 s74, 1835008\n\t"
"buffer_load_dwordx4 v18, s[88:91], s74 offen lds\n\t"
"ds_read_b64_tr_b16 a[8:9], v37 offset:16384\n\t"
"ds_read_b64_tr_b16 a[10:11], v37 offset:16896\n\t"
"s_mov_b32 m0, 84992\n\t"
"s_mov_b32 s74, 1966080\n\t"
"buffer_load_dwordx4 v19, s[88:91], s74 offen lds\n\t"
"ds_read_b64_tr_b16 a[12:13], v37 offset:24576\n\t"
"ds_read_b64_tr_b16 a[14:15], v37 offset:25088\n\t"
"s_waitcnt lgkmcnt(0)\n\t"
"s_waitcnt vmcnt(16)\n\t"
"s_barrier\n\t"
"ds_read_b64_tr_b16 v[96:97], v36 offset:32768\n\t"
"ds_read_b64_tr_b16 v[98:99], v36 offset:33280\n\t"
"ds_read_b64_tr_b16 v[100:101], v36 offset:40960\n\t"
"ds_read_b64_tr_b16 v[102:103], v36 offset:41472\n\t"
"ds_read_b64_tr_b16 a[16:17], v37 offset:32768\n\t"
"ds_read_b64_tr_b16 a[18:19], v37 offset:33280\n\t"
"ds_read_b64_tr_b16 a[20:21], v37 offset:40960\n\t"
"ds_read_b64_tr_b16 a[22:23], v37 offset:41472\n\t"
"ds_read_b64_tr_b16 v[112:113], v36 offset:49152\n\t"
"ds_read_b64_tr_b16 v[114:115], v36 offset:49664\n\t"
"ds_read_b64_tr_b16 v[116:117], v36 offset:57344\n\t"
"ds_read_b64_tr_b16 v[118:119], v36 offset:57856\n\t"
"ds_read_b64_tr_b16 a[24:25], v37 offset:49152\n\t"
"ds_read_b64_tr_b16 a[26:27], v37 offset:49664\n\t"
"ds_read_b64_tr_b16 a[28:29], v37 offset:57344\n\t"
"ds_read_b64_tr_b16 a[30:31], v37 offset:57856\n\t"
"s_waitcnt lgkmcnt(0)\n\t"
"s_waitcnt vmcnt(8)\n\t"
"v_readfirstlane_b32 s76, v56\n\t"
"s_nop 3\n\t"
"s_add_u32 s76, s76, 1\n\t"
"s_mov_b32 s60, 0\n\t"
"v_mov_b32 v56, 0\n\t"
"s_branch CW0_polc3_%=\n\t"
"CW0_pol1_%=:\n\t"
"global_load_dwordx2 v[58:59], v56, %[ctl] sc1\n\t"
"s_waitcnt vmcnt(0)\n\t"
"CW0_polc3_%=:\n\t"
"v_readfirstlane_b32 s74, v58\n\t"
"v_readfirstlane_b32 s75, v59\n\t"
"s_nop 3\n\t"
"s_cmp_eq_u32 s75, s76\n\t"
"s_cbranch_scc1 CW0_pold2_%=\n\t"
"s_sleep 2\n\t"
"s_add_u32 s60, s60, 1\n\t"
"s_cmp_lt_u32 s60, 0x10000\n\t"
"s_cbranch_scc1 CW0_pol1_%=\n\t"
"CW0_pold2_%=:\n\t"
"s_andn2_b32 s48, s74, 0xfff\n\t"
"s_lshr_b32 s80, s74, 1\n\t"
"s_and_b32 s74, s74, 1\n\t"
"s_lshl_b32 s74, s74, 13\n\t"
"s_mov_b64 s[40:41], %[comm]\n\t"
"s_add_u32 s40, s40, s74\n\t"
"s_addc_u32 s41, s41, 0\n\t"
"s_and_b32 s41, s41, 0xffff\n\t"
"s_mov_b32 s42, 0x1fe000\n\t"
"s_mov_b32 s43, 0x20000\n\t"
"s_add_u32 s75, %[me7], 4096\n\t"
"v_mov_b32 v56, s75\n\t"
"buffer_load_dword v8, v10, s[40:43], 0 offen sc1\n\t"
"s_lshl_b32 s81, %[me14], 1\n\t"
"s_add_u32 s81, s81, 0\n\t"
"s_lshr_b32 s74, s81, 24\n\t"
"s_lshr_b32 s74, s80, s74\n\t"
"s_lshr_b32 s75, s81, 21\n\t"
"s_xor_b32 s74, s74, s75\n\t"
"s_and_b32 s74, s74, 1\n\t"
"s_lshl_b32 s74, s74, 13\n\t"
"s_or_b32 s76, s81, s74\n\t"
"s_add_u32 s61, s81, 1048576\n\t"
"s_lshr_b32 s74, s61, 24\n\t"
"s_lshr_b32 s74, s80, s74\n\t"
"s_lshr_b32 s75, s61, 21\n\t"
"s_xor_b32 s74, s74, s75\n\t"
"s_and_b32 s74, s74, 1\n\t"
"s_lshl_b32 s74, s74, 13\n\t"
"s_or_b32 s61, s61, s74\n\t"
"s_barrier\n\t"
"ds_read_b64_tr_b16 v[72:73], v38 offset:0\n\t"
"ds_read_b64_tr_b16 v[74:75], v38 offset:512\n\t"
"ds_read_b64_tr_b16 v[76:77], v38 offset:8192\n\t"
"ds_read_b64_tr_b16 v[78:79], v38 offset:8704\n\t"
"ds_read_b64_tr_b16 a[64:65], v39 offset:0\n\t"
"ds_read_b64_tr_b16 a[66:67], v39 offset:512\n\t"
"ds_read_b64_tr_b16 a[68:69], v39 offset:8192\n\t"
"ds_read_b64_tr_b16 a[70:71], v39 offset:8704\n\t"
"ds_read_b64_tr_b16 v[88:89], v38 offset:16384\n\t"
"ds_read_b64_tr_b16 v[90:91], v38 offset:16896\n\t"
"ds_read_b64_tr_b16 v[92:93], v38 offset:24576\n\t"
"ds_read_b64_tr_b16 v[94:95], v38 offset:25088\n\t"
"ds_read_b64_tr_b16 a[72:73], v39 offset:16384\n\t"
"ds_read_b64_tr_b16 a[74:75], v39 offset:16896\n\t"
"ds_read_b64_tr_b16 a[76:77], v39 offset:24576\n\t"
"ds_read_b64_tr_b16 a[78:79], v39 offset:25088\n\t"
"s_waitcnt lgkmcnt(0)\n\t"
"s_waitcnt vmcnt(0)\n\t"
"global_load_dword v57, v56, %[ctl] sc1\n\t"
"s_barrier\n\t"
"ds_read_b64_tr_b16 v[104:105], v38 offset:32768\n\t"
"ds_read_b64_tr_b16 v[106:107], v38 offset:33280\n\t"
"ds_read_b64_tr_b16 v[108:109], v38 offset:40960\n\t"
"ds_read_b64_tr_b16 v[110:111], v38 offset:41472\n\t"
"ds_read_b64_tr_b16 a[80:81], v39 offset:32768\n\t"
"ds_read_b64_tr_b16 a[82:83], v39 offset:33280\n\t"
"ds_read_b64_tr_b16 a[84:85], v39 offset:40960\n\t"
"ds_read_b64_tr_b16 a[86:87], v39 offset:41472\n\t"
"ds_read_b64_tr_b16 v[120:121], v38 offset:49152\n\t"
"ds_read_b64_tr_b16 v[122:123], v38 offset:49664\n\t"
"ds_read_b64_tr_b16 v[124:125], v38 offset:57344\n\t"
"ds_read_b64_tr_b16 v[126:127], v38 offset:57856\n\t"
"ds_read_b64_tr_b16 a[88:89], v39 offset:49152\n\t"
"ds_read_b64_tr_b16 a[90:91], v39 offset:49664\n\t"
"ds_read_b64_tr_b16 a[92:93], v39 offset:57344\n\t"
"ds_read_b64_tr_b16 a[94:95], v39 offset:57856\n\t"
"s_waitcnt lgkmcnt(0)\n\t"
"s_waitcnt vmcnt(0)\n\t"
"s_mov_b32 s60, 0\n\t"
"s_branch CW0_kc6_%=\n\t"
"CW0_kw4_%=:\n\t"
"global_load_dword v57, v56, %[ctl] sc1\n\t"
"s_waitcnt vmcnt(0)\n\t"
"CW0_kc6_%=:\n\t"
"v_readfirstlane_b32 s75, v57\n\t"
"s_nop 3\n\t"
"s_lshr_b32 s75, s75, 24\n\t"
"s_and_b32 s75, s75, 0xff\n\t"
"s_cmp_ge_u32 s75, 16\n\t"
"s_cbranch_scc1 CW0_kd5_%=\n\t"
"s_sleep 2\n\t"
"s_add_u32 s60, s60, 1\n\t"
"s_cmp_lt_u32 s60, 0x8000\n\t"
"s_cbranch_scc1 CW0_kw4_%=\n\t"
"CW0_kd5_%=:\n\t"
"s_barrier\n\t"
"s_mov_b32 m0, 16384\n\t"
"s_nop 0\n\t"
"buffer_load_dwordx4 v0, s[44:47], s76 offen offset:0 sc1 lds\n\t"
"buffer_load_dwordx4 v0, s[44:47], s76 offen offset:1024 sc1 lds\n\t"
"buffer_load_dwordx4 v0, s[44:47], s76 offen offset:2048 sc1 lds\n\t"
"buffer_load_dwordx4 v0, s[44:47], s76 offen offset:3072 sc1 lds\n\t"
"ds_write_b128 v2, v[64:67] offset:0\n\t"
"ds_write_b128 v2, v[68:71] offset:4096\n\t"
"ds_write_b128 v2, v[72:75] offset:8192\n\t"
"ds_write_b128 v2, v[76:79] offset:12288\n\t"
"ds_write_b128 v2, v[80:83] offset:16384\n\t"
"ds_write_b128 v2, v[84:87] offset:20480\n\t"
"ds_write_b128 v2, v[88:91] offset:24576\n\t"
"ds_write_b128 v2, v[92:95] offset:28672\n\t"
"ds_write_b128 v2, v[96:99] offset:32768\n\t"
"ds_write_b128 v2, v[100:103] offset:36864\n\t"
"ds_write_b128 v2, v[104:107] offset:40960\n\t"
"ds_write_b128 v2, v[108:111] offset:45056\n\t"
"ds_write_b128 v2, v[112:115] offset:49152\n\t"
"ds_write_b128 v2, v[116:119] offset:53248\n\t"
"ds_write_b128 v2, v[120:123] offset:57344\n\t"
"ds_write_b128 v2, v[124:127] offset:61440\n\t"
"s_waitcnt lgkmcnt(0)\n\t"
"v_mov_b64 v[16:17], v[48:49]\n\t"
"v_mov_b64 v[18:19], v[50:51]\n\t"
"v_mov_b64 v[32:33], v[52:53]\n\t"
"v_mov_b64 v[34:35], v[54:55]\n\t"
"s_mov_b32 s62, 0xffbfffbf\n\t"
"s_mov_b32 s63, 0xffbfffbf\n\t"
"s_mov_b32 s78, 0x3f803f80\n\t"
"s_mov_b32 s79, 0x3f803f80\n\t"
"s_mov_b32 s38, 0x3e0293ee\n\t"
"s_mov_b32 s39, 0x3e0293ee\n\t"
"s_mov_b32 s72, 0xbe0293ee\n\t"
"s_sub_u32 s77, 100, s48\n\t"
"s_mov_b32 s64, 0\n\t"
"ds_read_b128 v[56:59], v2 offset:0\n\t"
"ds_read_b128 v[60:63], v2 offset:4096\n\t"
"ds_read_b128 v[72:75], v2 offset:8192\n\t"
"ds_read_b128 v[76:79], v2 offset:12288\n\t"
"ds_read_b128 v[80:83], v2 offset:16384\n\t"
"ds_read_b128 v[84:87], v2 offset:20480\n\t"
"ds_read_b128 v[88:91], v2 offset:24576\n\t"
"ds_read_b128 v[92:95], v2 offset:28672\n\t"
"ds_read_b128 v[96:99], v2 offset:32768\n\t"
"ds_read_b128 v[100:103], v2 offset:36864\n\t"
"ds_read_b128 v[104:107], v2 offset:40960\n\t"
"ds_read_b128 v[108:111], v2 offset:45056\n\t"
"ds_read_b128 v[112:115], v2 offset:49152\n\t"
"ds_read_b128 v[116:119], v2 offset:53248\n\t"
"ds_read_b128 v[120:123], v2 offset:57344\n\t"
"ds_read_b128 v[124:127], v2 offset:61440\n\t"
"v_mov_b64 v[48:49], 0\n\t"
"v_mov_b64 v[50:51], 0\n\t"
"v_mov_b64 v[52:53], 0\n\t"
"v_mov_b64 v[54:55], 0\n\t"
"v_mov_b64 v[64:65], 0\n\t"
"v_mov_b64 v[66:67], 0\n\t"
"v_mov_b64 v[68:69], 0\n\t"
"v_mov_b64 v[70:71], 0\n\t"
"s_mov_b32 s65, 0\n\t"
"s_mov_b32 s66, 112\n\t"
"s_mov_b32 s67, s48\n\t"
"s_add_u32 s65, s65, 524288\n\t"
"s_cmp_eq_u32 s65, 1572864\n\t"
"s_cselect_b32 s65, 0, s65\n\t"
"s_lshl_b32 s71, s64, 17\n\t"
"s_and_b32 s68, s64, 1\n\t"
"s_lshl_b32 s68, s68, 9\n\t"
"s_add_u32 s81, s81, 1048576\n\t"
"s_lshr_b32 s74, s81, 24\n\t"
"s_lshr_b32 s74, s80, s74\n\t"
"s_lshr_b32 s75, s81, 21\n\t"
"s_xor_b32 s74, s74, s75\n\t"
"s_and_b32 s74, s74, 1\n\t"
"s_lshl_b32 s74, s74, 13\n\t"
"s_or_b32 s76, s81, s74\n\t"
"s_waitcnt lgkmcnt(0)\n\t"
"s_waitcnt vmcnt(0)\n\t"
"s_mov_b32 s60, 0\n\t"
"CW0_top7_%=:\n\t"
"s_waitcnt vmcnt(1)\n\t"
"CW0_top211_%=:\n\t"
"ds_write_b128 v1, v[16:19] offset:0\n\t"
"v_smfmac_f32_16x16x64_bf16 v[48:51], v[16:19], v[56:63], v9\n\t"
"v_max3_u32 v15, v16, v17, v18\n\t"
"v_smfmac_f32_16x16x64_bf16 v[52:55], v[16:19], v[72:79], v9\n\t"
"v_smfmac_f32_16x16x64_bf16 v[64:67], v[16:19], a[128:135], v9\n\t"
"v_smfmac_f32_16x16x64_bf16 v[68:71], v[16:19], a[192:199], v9\n\t"
"s_waitcnt vmcnt(0)\n\t"
"ds_write_b128 v1, v[32:35] offset:4096\n\t"
"v_max3_u32 v15, v15, v19, v32\n\t"
"v_max3_u32 v15, v15, v33, v34\n\t"
"v_max_u32 v15, v15, v35\n\t"
"v_cmp_le_u32 vcc, s62, v15\n\t"
"s_cbranch_vccnz CW0_art9_%=\n\t"
"CW0_aok10_%=:\n\t"
"v_smfmac_f32_16x16x64_bf16 v[48:51], v[32:35], v[128:135], v9\n\t"
"v_mov_b64 v[56:57], 0\n\t"
"v_smfmac_f32_16x16x64_bf16 v[52:55], v[32:35], v[160:167], v9\n\t"
"v_mov_b64 v[58:59], 0\n\t"
"v_mov_b64 v[60:61], 0\n\t"
"v_mov_b64 v[62:63], 0\n\t"
"s_waitcnt lgkmcnt(0)\n\t"
"s_barrier\n\t"
"ds_read_b128 v[20:23], v0 offset:1024\n\t"
"ds_read_b128 v[24:27], v0 offset:2048\n\t"
"v_smfmac_f32_16x16x64_bf16 v[56:59], v[16:19], a[0:7], v9\n\t"
"ds_read_b128 v[28:31], v0 offset:3072\n\t"
"v_smfmac_f32_16x16x64_bf16 v[56:59], v[32:35], a[32:39], v9\n\t"
"ds_read_b128 v[36:39], v0 offset:5120\n\t"
"v_smfmac_f32_16x16x64_bf16 v[60:63], v[16:19], a[64:71], v9\n\t"
"ds_read_b128 v[40:43], v0 offset:6144\n\t"
"v_smfmac_f32_16x16x64_bf16 v[60:63], v[32:35], a[96:103], v9\n\t"
"ds_read_b128 v[44:47], v0 offset:7168\n\t"
"v_smfmac_f32_16x16x64_bf16 v[64:67], v[32:35], a[160:167], v9\n\t"
"s_waitcnt lgkmcnt(4)\n\t"
"v_smfmac_f32_16x16x64_bf16 v[48:51], v[20:23], v[80:87], v9\n\t"
"s_memrealtime s[50:51]\n\t"
"v_smfmac_f32_16x16x64_bf16 v[52:55], v[20:23], v[88:95], v9\n\t"
"v_smfmac_f32_16x16x64_bf16 v[48:51], v[24:27], v[96:103], v9\n\t"
"v_smfmac_f32_16x16x64_bf16 v[52:55], v[24:27], v[104:111], v9\n\t"
"s_waitcnt lgkmcnt(0)\n\t"
"s_add_u32 s49, s50, s77\n\t"
"v_smfmac_f32_16x16x64_bf16 v[48:51], v[28:31], v[112:119], v9\n\t"
"v_mov_b32 v15, s49\n\t"
"v_smfmac_f32_16x16x64_bf16 v[52:55], v[28:31], v[120:127], v9\n\t"
"buffer_atomic_smax v15, v13, s[40:43], s68 offen sc1\n\t"
"v_smfmac_f32_16x16x64_bf16 v[48:51], v[36:39], v[136:143], v9\n\t"
"ds_read_b128 v[72:75], v0 offset:16384\n\t"
"v_smfmac_f32_16x16x64_bf16 v[52:55], v[36:39], v[168:175], v9\n\t"
"ds_read_b128 v[76:79], v0 offset:17408\n\t"
"v_smfmac_f32_16x16x64_bf16 v[48:51], v[40:43], v[144:151], v9\n\t"
"ds_read_b128 v[80:83], v0 offset:18432\n\t"
"v_smfmac_f32_16x16x64_bf16 v[52:55], v[40:43], v[176:183], v9\n\t"
"ds_read_b128 v[84:87], v0 offset:19456\n\t"
"v_smfmac_f32_16x16x64_bf16 v[48:51], v[44:47], v[152:159], v9\n\t"
"ds_read_b128 v[88:91], v0 offset:20480\n\t"
"v_smfmac_f32_16x16x64_bf16 v[52:55], v[44:47], v[184:191], v9\n\t"
"ds_read_b128 v[92:95], v0 offset:21504\n\t"
"v_smfmac_f32_16x16x64_bf16 v[56:59], v[20:23], a[8:15], v9\n\t"
"ds_read_b128 v[96:99], v0 offset:22528\n\t"
"v_smfmac_f32_16x16x64_bf16 v[60:63], v[20:23], a[72:79], v9\n\t"
"ds_read_b128 v[100:103], v0 offset:23552\n\t"
"v_smfmac_f32_16x16x64_bf16 v[56:59], v[24:27], a[16:23], v9\n\t"
"ds_read_b128 v[104:107], v0 offset:24576\n\t"
"v_smfmac_f32_16x16x64_bf16 v[60:63], v[24:27], a[80:87], v9\n\t"
"ds_read_b128 v[108:111], v0 offset:25600\n\t"
"v_smfmac_f32_16x16x64_bf16 v[56:59], v[28:31], a[24:31], v9\n\t"
"v_add_f32 v48, v48, v49\n\t"
"v_smfmac_f32_16x16x64_bf16 v[60:63], v[28:31], a[88:95], v9\n\t"
"v_add_f32 v50, v50, v51\n\t"
"v_smfmac_f32_16x16x64_bf16 v[56:59], v[36:39], a[40:47], v9\n\t"
"v_cvt_pk_bf16_f32 v124, v48, v50\n\t"
"v_smfmac_f32_16x16x64_bf16 v[60:63], v[36:39], a[104:111], v9\n\t"
"v_add_f32 v52, v52, v53\n\t"
"v_smfmac_f32_16x16x64_bf16 v[56:59], v[40:43], a[48:55], v9\n\t"
"v_add_f32 v54, v54, v55\n\t"
"v_smfmac_f32_16x16x64_bf16 v[60:63], v[40:43], a[112:119], v9\n\t"
"v_cvt_pk_bf16_f32 v125, v52, v54\n\t"
"v_smfmac_f32_16x16x64_bf16 v[56:59], v[44:47], a[56:63], v9\n\t"
"ds_write_b64 v4, v[124:125] offset:8192\n\t"
"v_smfmac_f32_16x16x64_bf16 v[60:63], v[44:47], a[120:127], v9\n\t"
"v_smfmac_f32_16x16x64_bf16 v[64:67], v[20:23], a[136:143], v9\n\t"
"s_nop 4\n\t"
"v_add_f32 v56, v56, v57\n\t"
"v_add_f32 v58, v58, v59\n\t"
"v_cvt_pk_bf16_f32 v126, v56, v58\n\t"
"v_add_f32 v60, v60, v61\n\t"
"v_add_f32 v62, v62, v63\n\t"
"v_cvt_pk_bf16_f32 v127, v60, v62\n\t"
"ds_write_b64 v4, v[126:127] offset:8704\n\t"
"v_smfmac_f32_16x16x64_bf16 v[68:71], v[20:23], a[200:207], v9\n\t"
"v_smfmac_f32_16x16x64_bf16 v[64:67], v[24:27], a[144:151], v9\n\t"
"v_smfmac_f32_16x16x64_bf16 v[68:71], v[24:27], a[208:215], v9\n\t"
"v_smfmac_f32_16x16x64_bf16 v[64:67], v[28:31], a[152:159], v9\n\t"
"s_waitcnt lgkmcnt(0)\n\t"
"s_barrier\n\t"
"ds_read_b64_tr_b16 v[48:49], v5 offset:8192\n\t"
"ds_read_b64_tr_b16 v[50:51], v5 offset:8704\n\t"
"ds_read_b64_tr_b16 v[52:53], v5 offset:9216\n\t"
"v_smfmac_f32_16x16x64_bf16 v[68:71], v[28:31], a[216:223], v9\n\t"
"ds_read_b64_tr_b16 v[54:55], v5 offset:9728\n\t"
"v_smfmac_f32_16x16x64_bf16 v[68:71], v[32:35], a[224:231], v9\n\t"
"ds_read_b64_tr_b16 v[56:57], v5 offset:10240\n\t"
"v_smfmac_f32_16x16x64_bf16 v[64:67], v[36:39], a[168:175], v9\n\t"
"ds_read_b64_tr_b16 v[58:59], v5 offset:10752\n\t"
"s_waitcnt lgkmcnt(4)\n\t"
"v_mfma_f32_16x16x32_bf16 v[112:115], v[72:75], v[48:51], 0\n\t"
"ds_read_b64_tr_b16 v[60:61], v5 offset:11264\n\t"
"v_mfma_f32_16x16x32_bf16 v[116:119], v[88:91], v[48:51], 0\n\t"
"ds_read_b64_tr_b16 v[62:63], v5 offset:11776\n\t"
"s_waitcnt lgkmcnt(4)\n\t"
"v_mfma_f32_16x16x32_bf16 v[112:115], v[76:79], v[52:55], v[112:115]\n\t"
"v_mov_b64 v[16:17], 0\n\t"
"v_mov_b64 v[18:19], 0\n\t"
"v_mfma_f32_16x16x32_bf16 v[116:119], v[92:95], v[52:55], v[116:119]\n\t"
"v_mov_b64 v[20:21], 0\n\t"
"s_waitcnt lgkmcnt(2)\n\t"
"v_mfma_f32_16x16x32_bf16 v[112:115], v[80:83], v[56:59], v[112:115]\n\t"
"v_mov_b64 v[22:23], 0\n\t"
"v_mov_b64 v[24:25], 0\n\t"
"v_mfma_f32_16x16x32_bf16 v[116:119], v[96:99], v[56:59], v[116:119]\n\t"
"v_mov_b64 v[26:27], 0\n\t"
"s_waitcnt lgkmcnt(0)\n\t"
"v_mfma_f32_16x16x32_bf16 v[112:115], v[84:87], v[60:63], v[112:115]\n\t"
"v_mov_b64 v[28:29], 0\n\t"
"v_mov_b64 v[30:31], 0\n\t"
"v_mfma_f32_16x16x32_bf16 v[116:119], v[100:103], v[60:63], v[116:119]\n\t"
"v_mov_b32 v81, 0\n\t"
"v_mov_b32 v82, 0\n\t"
"v_mfma_f32_16x16x32_bf16 v[120:123], v[48:51], v[48:51], 0\n\t"
"v_mov_b64 v[124:125], s[78:79]\n\t"
"v_mov_b64 v[126:127], s[78:79]\n\t"
"v_mfma_f32_16x16x32_bf16 v[120:123], v[52:55], v[52:55], v[120:123]\n\t"
"v_max3_f32 v72, v112, v113, v114\n\t"
"v_mfma_f32_16x16x32_bf16 v[120:123], v[56:59], v[56:59], v[120:123]\n\t"
"v_max3_f32 v72, v72, v115, v116\n\t"
"v_mfma_f32_16x16x32_bf16 v[120:123], v[60:63], v[60:63], v[120:123]\n\t"
"v_max3_f32 v72, v72, v117, v118\n\t"
"v_max_f32 v72, v72, v119\n\t"
"v_smfmac_f32_16x16x64_bf16 v[68:71], v[36:39], a[232:239], v9\n\t"
"s_nop 4\n\t"
"v_max3_f32 v72, v72, v122, v123\n\t"
"v_mov_b32 v73, v72\n\t"
"v_smfmac_f32_16x16x64_bf16 v[64:67], v[40:43], a[176:183], v9\n\t"
"s_nop 0\n\t"
"v_permlane32_swap_b32 v72, v73\n\t"
"v_max_f32 v72, v72, v73\n\t"
"v_mov_b32 v73, v72\n\t"
"v_smfmac_f32_16x16x64_bf16 v[68:71], v[40:43], a[240:247], v9\n\t"
"s_nop 0\n\t"
"v_permlane16_swap_b32 v72, v73\n\t"
"v_max_f32 v72, v72, v73\n\t"
"v_mul_f32 v74, s72, v72\n\t"
"v_pk_fma_f32 v[112:113], v[112:113], s[38:39], v[74:75] op_sel_hi:[1,1,0]\n\t"
"v_pk_fma_f32 v[114:115], v[114:115], s[38:39], v[74:75] op_sel_hi:[1,1,0]\n\t"
"v_pk_fma_f32 v[116:117], v[116:117], s[38:39], v[74:75] op_sel_hi:[1,1,0]\n\t"
"v_pk_fma_f32 v[118:119], v[118:119], s[38:39], v[74:75] op_sel_hi:[1,1,0]\n\t"
"v_pk_fma_f32 v[122:123], v[122:123], s[38:39], v[74:75] op_sel_hi:[1,1,0]\n\t"
"v_exp_f32 v112, v112\n\t"
"v_exp_f32 v113, v113\n\t"
"v_smfmac_f32_16x16x64_bf16 v[64:67], v[44:47], a[184:191], v9\n\t"
"v_exp_f32 v114, v114\n\t"
"v_exp_f32 v115, v115\n\t"
"v_exp_f32 v116, v116\n\t"
"v_smfmac_f32_16x16x64_bf16 v[68:71], v[44:47], a[248:255], v9\n\t"
"v_exp_f32 v117, v117\n\t"
"v_exp_f32 v118, v118\n\t"
"v_exp_f32 v119, v119\n\t"
"v_cvt_pk_bf16_f32 v76, v112, v113\n\t"
"v_cvt_pk_bf16_f32 v77, v114, v115\n\t"
"v_cvt_pk_bf16_f32 v78, v116, v117\n\t"
"v_cvt_pk_bf16_f32 v79, v118, v119\n\t"
"v_exp_f32 v122, v122\n\t"
"v_exp_f32 v123, v123\n\t"
"v_mfma_f32_16x16x32_bf16 v[72:75], v[124:127], v[76:79], 0\n\t"
"v_cvt_pk_bf16_f32 v80, v122, v123\n\t"
"v_cvt_pk_bf16_f32 v83, v122, v123\n\t"
"v_mfma_f32_16x16x32_bf16 v[48:51], v[104:107], v[76:79], 0\n\t"
"v_add_f32 v64, v64, v65\n\t"
"v_add_f32 v66, v66, v67\n\t"
"v_mfma_f32_16x16x16_bf16 v[72:75], v[124:125], v[80:81], v[72:75]\n\t"
"v_add_f32 v68, v68, v69\n\t"
"v_add_f32 v70, v70, v71\n\t"
"v_cvt_pk_bf16_f32 v84, v64, v66\n\t"
"v_cvt_pk_bf16_f32 v85, v68, v70\n\t"
"v_mfma_f32_16x16x32_bf16 v[52:55], v[108:111], v[76:79], 0\n\t"
"v_mov_b64 v[64:65], v[84:85]\n\t"
"v_mfma_f32_16x16x16_bf16 v[48:51], v[84:85], v[80:81], v[48:51]\n\t"
"v_mfma_f32_16x16x16_bf16 v[52:55], v[84:85], v[82:83], v[52:55]\n\t"
"v_rcp_f32 v124, v72\n\t"
"s_nop 5\n\t"
"v_pk_mul_f32 v[48:49], v[48:49], v[124:125] op_sel_hi:[1,0]\n\t"
"v_pk_mul_f32 v[50:51], v[50:51], v[124:125] op_sel_hi:[1,0]\n\t"
"v_pk_mul_f32 v[52:53], v[52:53], v[124:125] op_sel_hi:[1,0]\n\t"
"v_pk_mul_f32 v[54:55], v[54:55], v[124:125] op_sel_hi:[1,0]\n\t"
"v_cvt_pk_bf16_f32 v120, v48, v49\n\t"
"v_cvt_pk_bf16_f32 v121, v50, v51\n\t"
"v_cvt_pk_bf16_f32 v122, v52, v53\n\t"
"v_cvt_pk_bf16_f32 v123, v54, v55\n\t"
"ds_write_b128 v6, v[120:123] offset:12288\n\t"
"s_cmp_eq_u32 s64, 63\n\t"
"s_cbranch_scc1 CW0_fin8_%=\n\t"
"s_waitcnt lgkmcnt(0)\n\t"
"s_barrier\n\t"
"ds_read_b128 v[120:123], v0 offset:12288\n\t"
"ds_read_b128 v[124:127], v0 offset:13312\n\t"
"s_waitcnt lgkmcnt(1)\n\t"
"v_smfmac_f32_16x16x64_bf16 v[16:19], v[120:123], v[192:199], v9\n\t"
"v_smfmac_f32_16x16x64_bf16 v[20:23], v[120:123], v[200:207], v9\n\t"
"s_waitcnt lgkmcnt(0)\n\t"
"v_smfmac_f32_16x16x64_bf16 v[16:19], v[124:127], v[224:231], v9\n\t"
"s_nop 0\n\t"
"v_smfmac_f32_16x16x64_bf16 v[20:23], v[124:127], v[232:239], v9\n\t"
"s_nop 0\n\t"
"v_smfmac_f32_16x16x64_bf16 v[24:27], v[120:123], v[208:215], v9\n\t"
"s_nop 0\n\t"
"v_smfmac_f32_16x16x64_bf16 v[28:31], v[120:123], v[216:223], v9\n\t"
"s_nop 0\n\t"
"v_smfmac_f32_16x16x64_bf16 v[24:27], v[124:127], v[240:247], v9\n\t"
"v_add_f32 v16, v16, v17\n\t"
"v_smfmac_f32_16x16x64_bf16 v[28:31], v[124:127], v[248:255], v9\n\t"
"v_add_f32 v18, v18, v19\n\t"
"v_cvt_pk_bf16_f32 v68, v16, v18\n\t"
"v_add_f32 v20, v20, v21\n\t"
"v_add_f32 v22, v22, v23\n\t"
"v_cvt_pk_bf16_f32 v69, v20, v22\n\t"
"s_nop 0\n\t"
"v_add_f32 v24, v24, v25\n\t"
"v_add_f32 v26, v26, v27\n\t"
"v_cvt_pk_bf16_f32 v70, v24, v26\n\t"
"v_add_f32 v28, v28, v29\n\t"
"v_add_f32 v30, v30, v31\n\t"
"v_cvt_pk_bf16_f32 v71, v28, v30\n\t"
"buffer_store_dwordx4 v[68:71], v7, s[40:43], s65 offen sc1\n\t"
"s_memrealtime s[74:75]\n\t"
"s_waitcnt lgkmcnt(0)\n\t"
"s_mov_b32 s59, s74\n\t"
"s_sub_u32 s74, s74, s50\n\t"
"s_add_u32 s77, s77, s48\n\t"
"s_add_u32 s77, s77, 4\n\t"
"s_min_u32 s77, s77, s74\n\t"
"s_min_u32 s77, s77, 88\n\t"
"s_sub_u32 s77, s77, s48\n\t"
"ds_read_b64_tr_b16 v[68:69], v5 offset:8192\n\t"
"ds_read_b64_tr_b16 v[70:71], v5 offset:8704\n\t"
"s_and_b32 s74, s64, 3\n\t"
"s_lshl_b32 s74, s74, 16\n\t"
"s_add_u32 s69, s74, 1769472\n\t"
"s_add_u32 s70, s74, 1804288\n\t"
"buffer_load_dword v8, v10, s[40:43], s68 offen sc1\n\t"
"buffer_load_dwordx4 v0, s[44:47], s76 offen offset:0 sc1 lds\n\t"
"buffer_load_dwordx4 v0, s[44:47], s76 offen offset:1024 sc1 lds\n\t"
"buffer_load_dwordx4 v0, s[44:47], s76 offen offset:2048 sc1 lds\n\t"
"buffer_load_dwordx4 v0, s[44:47], s76 offen offset:3072 sc1 lds\n\t"
"ds_read_b128 v[56:59], v2 offset:0\n\t"
"ds_read_b128 v[60:63], v2 offset:4096\n\t"
"ds_read_b128 v[72:75], v2 offset:8192\n\t"
"ds_read_b128 v[76:79], v2 offset:12288\n\t"
"ds_read_b128 v[80:83], v2 offset:16384\n\t"
"ds_read_b128 v[84:87], v2 offset:20480\n\t"
"ds_read_b128 v[88:91], v2 offset:24576\n\t"
"ds_read_b128 v[92:95], v2 offset:28672\n\t"
"ds_read_b128 v[96:99], v2 offset:32768\n\t"
"ds_read_b128 v[100:103], v2 offset:36864\n\t"
"ds_read_b128 v[104:107], v2 offset:40960\n\t"
"ds_read_b128 v[108:111], v2 offset:45056\n\t"
"ds_read_b128 v[112:115], v2 offset:49152\n\t"
"ds_read_b128 v[116:119], v2 offset:53248\n\t"
"ds_read_b128 v[120:123], v2 offset:57344\n\t"
"ds_read_b128 v[124:127], v2 offset:61440\n\t"
"buffer_store_dwordx2 v[64:65], v12, s[52:55], s71 offen offset:2048 sc1\n\t"
"s_waitcnt lgkmcnt(15)\n\t"
"buffer_store_dwordx4 v[68:71], v11, s[52:55], s71 offen offset:0 sc1\n\t"
"v_mov_b64 v[48:49], 0\n\t"
"v_mov_b64 v[50:51], 0\n\t"
"v_mov_b64 v[52:53], 0\n\t"
"v_mov_b64 v[54:55], 0\n\t"
"v_mov_b64 v[64:65], 0\n\t"
"v_mov_b64 v[66:67], 0\n\t"
"v_mov_b64 v[68:69], 0\n\t"
"v_mov_b64 v[70:71], 0\n\t"
"s_add_u32 s64, s64, 1\n\t"
"s_add_u32 s65, s65, 524288\n\t"
"s_cmp_eq_u32 s65, 1572864\n\t"
"s_cselect_b32 s65, 0, s65\n\t"
"s_lshl_b32 s71, s64, 17\n\t"
"s_and_b32 s68, s64, 1\n\t"
"s_lshl_b32 s68, s68, 9\n\t"
"s_add_u32 s81, s81, 1048576\n\t"
"s_lshr_b32 s74, s81, 24\n\t"
"s_lshr_b32 s74, s80, s74\n\t"
"s_lshr_b32 s75, s81, 21\n\t"
"s_xor_b32 s74, s74, s75\n\t"
"s_and_b32 s74, s74, 1\n\t"
"s_lshl_b32 s74, s74, 13\n\t"
"s_or_b32 s76, s81, s74\n\t"
"s_waitcnt vmcnt(6)\n\t"
"v_max_i32_dpp v8, v8, v8 row_shl:4 row_mask:0xf bank_mask:0xf\n\t"
"s_nop 0\n\t"
"v_readlane_b32 s74, v8, 0\n\t"
"v_readlane_b32 s75, v8, 1\n\t"
"v_readlane_b32 s50, v8, 2\n\t"
"v_readlane_b32 s51, v8, 3\n\t"
"s_max_i32 s74, s74, s75\n\t"
"s_max_i32 s50, s50, s51\n\t"
"s_max_i32 s74, s74, s50\n\t"
"s_add_u32 s58, s74, s48\n\t"
"s_sub_i32 s74, s58, s59\n\t"
"s_min_i32 s74, s74, 60\n\t"
"s_max_i32 s74, s74, -2\n\t"
"s_add_u32 s58, s59, s74\n\t"
"s_waitcnt lgkmcnt(0)\n\t"
"s_add_u32 s75, s58, %[dC]\n\t"
"CW0_sp12_%=:\n\t"
"s_memrealtime s[50:51]\n\t"
"s_waitcnt lgkmcnt(0)\n\t"
"s_sub_i32 s60, s75, s50\n\t"
"s_cmp_gt_i32 s60, 4\n\t"
"s_cbranch_scc1 CW0_sp12_%=\n\t"
"s_cmp_lt_i32 s60, 2\n\t"
"s_cbranch_scc1 CW0_sd13_%=\n\t"
"s_sub_u32 s60, s60, 2\n\t"
"CW0_fd14_%=:\n\t"
"s_sub_u32 s60, s60, 1\n\t"
"s_cbranch_scc0 CW0_fd14_%=\n\t"
"CW0_sd13_%=:\n\t"
"buffer_load_dwordx4 v[16:19], v0, s[40:43], s69 offen sc1\n\t"
"buffer_load_dwordx4 v[32:35], v0, s[40:43], s70 offen sc1\n\t"
"s_mov_b32 s60, 0\n\t"
"s_branch CW0_top7_%=\n\t"
"CW0_art9_%=:\n\t"
"v_cmp_eq_u32 vcc, s62, v15\n\t"
"s_cbranch_vccnz CW0_go15_%=\n\t"
"s_mov_b64 s[74:75], 0\n\t"
"v_cmp_eq_u32 vcc, s62, v16\n\t"
"s_nop 1\n\t"
"s_or_b64 s[74:75], s[74:75], vcc\n\t"
"v_cmp_eq_u32 vcc, s62, v17\n\t"
"s_nop 1\n\t"
"s_or_b64 s[74:75], s[74:75], vcc\n\t"
"v_cmp_eq_u32 vcc, s62, v18\n\t"
"s_nop 1\n\t"
"s_or_b64 s[74:75], s[74:75], vcc\n\t"
"v_cmp_eq_u32 vcc, s62, v19\n\t"
"s_nop 1\n\t"
"s_or_b64 s[74:75], s[74:75], vcc\n\t"
"v_cmp_eq_u32 vcc, s62, v32\n\t"
"s_nop 1\n\t"
"s_or_b64 s[74:75], s[74:75], vcc\n\t"
"v_cmp_eq_u32 vcc, s62, v33\n\t"
"s_nop 1\n\t"
"s_or_b64 s[74:75], s[74:75], vcc\n\t"
"v_cmp_eq_u32 vcc, s62, v34\n\t"
"s_nop 1\n\t"
"s_or_b64 s[74:75], s[74:75], vcc\n\t"
"v_cmp_eq_u32 vcc, s62, v35\n\t"
"s_nop 1\n\t"
"s_or_b64 s[74:75], s[74:75], vcc\n\t"
"s_cbranch_scc0 CW0_aok10_%=\n\t"
"CW0_go15_%=:\n\t"
"s_add_u32 s60, s60, 1\n\t"
"s_cmp_ge_u32 s60, 0x4000\n\t"
"s_cbranch_scc1 CW0_aok10_%=\n\t"
"s_waitcnt lgkmcnt(0)\n\t"
"buffer_load_dwordx4 v[16:19], v0, s[40:43], s69 offen sc1\n\t"
"buffer_load_dwordx4 v[32:35], v0, s[40:43], s70 offen sc1\n\t"
"v_mov_b64 v[48:49], 0\n\t"
"v_mov_b64 v[50:51], 0\n\t"
"v_mov_b64 v[52:53], 0\n\t"
"v_mov_b64 v[54:55], 0\n\t"
"v_mov_b64 v[64:65], 0\n\t"
"v_mov_b64 v[66:67], 0\n\t"
"v_mov_b64 v[68:69], 0\n\t"
"v_mov_b64 v[70:71], 0\n\t"
"s_branch CW0_top7_%=\n\t"
"CW0_fin8_%=:\n\t"
"s_waitcnt lgkmcnt(0)\n\t"
"s_nop 9\n\t"
"ds_read_b64_tr_b16 v[68:69], v5 offset:8192\n\t"
"ds_read_b64_tr_b16 v[70:71], v5 offset:8704\n\t"
"buffer_store_dwordx2 v[64:65], v12, s[52:55], s71 offen offset:2048 sc1\n\t"
"s_waitcnt lgkmcnt(0)\n\t"
"buffer_store_dwordx4 v[68:71], v11, s[52:55], s71 offen offset:0 sc1\n\t"
"s_waitcnt vmcnt(0)\n\t"
"s_barrier\n\t"
"s_branch CHAIN_END_%=\n\t"
"CHAIN_W1_%=:\n\t"
"v_mbcnt_lo_u32_b32 v127, -1, 0\n\t"
"v_mbcnt_hi_u32_b32 v127, -1, v127\n\t"
"v_lshlrev_b32 v127, 2, v127\n\t"
"v_add_u32 v127, 256, v127\n\t"
"ds_read_b32 v0, v127 offset:32768\n\t"
"ds_read_b32 v1, v127 offset:33792\n\t"
"ds_read_b32 v2, v127 offset:34816\n\t"
"ds_read_b32 v3, v127 offset:35840\n\t"
"ds_read_b32 v4, v127 offset:36864\n\t"
"ds_read_b32 v5, v127 offset:37888\n\t"
"ds_read_b32 v6, v127 offset:38912\n\t"
"ds_read_b32 v7, v127 offset:39936\n\t"
"ds_read_b32 v8, v127 offset:40960\n\t"
"ds_read_b32 v9, v127 offset:41984\n\t"
"ds_read_b32 v10, v127 offset:43008\n\t"
"ds_read_b32 v11, v127 offset:44032\n\t"
"ds_read_b32 v12, v127 offset:45056\n\t"
"ds_read_b32 v16, v127 offset:46080\n\t"
"ds_read_b32 v20, v127 offset:47104\n\t"
"s_waitcnt lgkmcnt(0)\n\t"
"ds_read_b32 v36, v127 offset:48128\n\t"
"ds_read_b32 v26, v127 offset:49152\n\t"
"ds_read_b32 v13, v127 offset:50176\n\t"
"s_waitcnt lgkmcnt(0)\n\t"
"s_barrier\n\t"
"v_xor_b32 v17, 64, v16\n\t"
"v_xor_b32 v21, 64, v20\n\t"
"v_xor_b32 v18, 128, v16\n\t"
"v_xor_b32 v22, 65536, v20\n\t"
"v_xor_b32 v19, 192, v16\n\t"
"v_xor_b32 v23, 65600, v20\n\t"
"v_xor_b32 v24, 64, v36\n\t"
"v_xor_b32 v25, 96, v36\n\t"
"v_xor_b32 v28, 0, v36\n\t"
"v_add_u32 v28, 16384, v28\n\t"
"v_xor_b32 v29, 32, v36\n\t"
"v_add_u32 v29, 16384, v29\n\t"
"v_xor_b32 v30, 64, v36\n\t"
"v_add_u32 v30, 16384, v30\n\t"
"v_xor_b32 v31, 96, v36\n\t"
"v_add_u32 v31, 16384, v31\n\t"
"v_xor_b32 v32, 128, v36\n\t"
"v_add_u32 v32, 16384, v32\n\t"
"v_xor_b32 v33, 160, v36\n\t"
"v_add_u32 v33, 16384, v33\n\t"
"v_xor_b32 v34, 192, v36\n\t"
"v_add_u32 v34, 16384, v34\n\t"
"v_xor_b32 v35, 224, v36\n\t"
"v_add_u32 v35, 16384, v35\n\t"
"s_mov_b32 s82, 0xff00ff00\n\t"
"s_mov_b32 s83, 0xff00ff00\n\t"
"s_mov_b32 s56, 0xffff\n\t"
"s_mov_b32 s57, 0\n\t"
"s_mov_b64 s[44:45], %[kv]\n\t"
"s_and_b32 s45, s45, 0xffff\n\t"
"s_mov_b32 s46, 67108864\n\t"
"s_mov_b32 s47, 0x20000\n\t"
"s_mov_b64 s[52:53], %[kmv]\n\t"
"s_and_b32 s53, s53, 0xffff\n\t"
"s_mov_b32 s54, %[kmsz]\n\t"
"s_mov_b32 s55, 0x20000\n\t"
"s_mov_b64 s[84:85], %[wq]\n\t"
"s_and_b32 s85, s85, 0xffff\n\t"
"s_mov_b32 s86, 4194304\n\t"
"s_mov_b32 s87, 0x20000\n\t"
"s_mov_b64 s[88:89], %[wk]\n\t"
"s_and_b32 s89, s89, 0xffff\n\t"
"s_mov_b32 s90, 4194304\n\t"
"s_mov_b32 s91, 0x20000\n\t"
"s_mov_b64 s[92:93], %[wv]\n\t"
"s_and_b32 s93, s93, 0xffff\n\t"
"s_mov_b32 s94, 4194304\n\t"
"s_mov_b32 s95, 0x20000\n\t"
"s_mov_b64 s[96:97], %[wo]\n\t"
"s_and_b32 s97, s97, 0xffff\n\t"
"s_mov_b32 s98, 130944\n\t"
"s_mov_b32 s99, 0x20000\n\t"
"s_mov_b64 s[60:61], %[mem0]\n\t"
"s_and_b32 s61, s61, 0xffff\n\t"
"s_mov_b32 s62, 8192\n\t"
"s_mov_b32 s63, 0x20000\n\t"
"buffer_load_dwordx2 v[48:49], v26, s[60:63], 0 offen offset:0\n\t"
"buffer_load_dwordx2 v[50:51], v26, s[60:63], 0 offen offset:32\n\t"
"buffer_load_dwordx2 v[52:53], v26, s[60:63], 0 offen offset:512\n\t"
"buffer_load_dwordx2 v[54:55], v26, s[60:63], 0 offen offset:544\n\t"
"v_mov_b32 v57, 0\n\t"
"global_load_dword v56, v57, %[ctl] offset:128 sc1\n\t"
"v_mov_b32 v60, 0xffbfffbf\n\t"
"v_mov_b32 v61, 0xffbfffbf\n\t"
"v_mov_b32 v62, 0xffbfffbf\n\t"
"v_mov_b32 v63, 0xffbfffbf\n\t"
"s_mov_b32 s74, 8126464\n\t"
"buffer_store_dwordx4 v[60:63], v1, s[52:55], s74 offen sc1\n\t"
"s_mov_b32 s74, 8257536\n\t"
"buffer_store_dwordx4 v[60:63], v1, s[52:55], s74 offen sc1\n\t"
"s_mov_b32 m0, 36864\n\t"
"s_mov_b32 s74, 2129920\n\t"
"buffer_load_dwordx4 v16, s[84:87], s74 offen lds\n\t"
"s_mov_b32 m0, 37888\n\t"
"s_mov_b32 s74, 2260992\n\t"
"buffer_load_dwordx4 v17, s[84:87], s74 offen lds\n\t"
"s_mov_b32 m0, 38912\n\t"
"s_mov_b32 s74, 2392064\n\t"
"buffer_load_dwordx4 v18, s[84:87], s74 offen lds\n\t"
"s_mov_b32 m0, 39936\n\t"
"s_mov_b32 s74, 2523136\n\t"
"buffer_load_dwordx4 v19, s[84:87], s74 offen lds\n\t"
"s_mov_b32 m0, 53248\n\t"
"s_mov_b32 s74, 2654208\n\t"
"buffer_load_dwordx4 v16, s[84:87], s74 offen lds\n\t"
"s_mov_b32 m0, 54272\n\t"
"s_mov_b32 s74, 2785280\n\t"
"buffer_load_dwordx4 v17, s[84:87], s74 offen lds\n\t"
"s_mov_b32 m0, 55296\n\t"
"s_mov_b32 s74, 2916352\n\t"
"buffer_load_dwordx4 v18, s[84:87], s74 offen lds\n\t"
"s_mov_b32 m0, 56320\n\t"
"s_mov_b32 s74, 3047424\n\t"
"buffer_load_dwordx4 v19, s[84:87], s74 offen lds\n\t"
"s_mov_b32 m0, 69632\n\t"
"s_mov_b32 s74, 3178496\n\t"
"buffer_load_dwordx4 v16, s[84:87], s74 offen lds\n\t"
"s_mov_b32 m0, 70656\n\t"
"s_mov_b32 s74, 3309568\n\t"
"buffer_load_dwordx4 v17, s[84:87], s74 offen lds\n\t"
"s_mov_b32 m0, 71680\n\t"
"s_mov_b32 s74, 3440640\n\t"
"buffer_load_dwordx4 v18, s[84:87], s74 offen lds\n\t"
"s_mov_b32 m0, 72704\n\t"
"s_mov_b32 s74, 3571712\n\t"
"buffer_load_dwordx4 v19, s[84:87], s74 offen lds\n\t"
"s_mov_b32 m0, 86016\n\t"
"s_mov_b32 s74, 3702784\n\t"
"buffer_load_dwordx4 v16, s[84:87], s74 offen lds\n\t"
"s_mov_b32 m0, 87040\n\t"
"s_mov_b32 s74, 3833856\n\t"
"buffer_load_dwordx4 v17, s[84:87], s74 offen lds\n\t"
"s_mov_b32 m0, 88064\n\t"
"s_mov_b32 s74, 3964928\n\t"
"buffer_load_dwordx4 v18, s[84:87], s74 offen lds\n\t"
"s_mov_b32 m0, 89088\n\t"
"s_mov_b32 s74, 4096000\n\t"
"buffer_load_dwordx4 v19, s[84:87], s74 offen lds\n\t"
"s_mov_b32 m0, 102400\n\t"
"s_mov_b32 s74, 2129920\n\t"
"buffer_load_dwordx4 v16, s[88:91], s74 offen lds\n\t"
"s_mov_b32 m0, 103424\n\t"
"s_mov_b32 s74, 2260992\n\t"
"buffer_load_dwordx4 v17, s[88:91], s74 offen lds\n\t"
"s_mov_b32 m0, 104448\n\t"
"s_mov_b32 s74, 2392064\n\t"
"buffer_load_dwordx4 v18, s[88:91], s74 offen lds\n\t"
"s_mov_b32 m0, 105472\n\t"
"s_mov_b32 s74, 2523136\n\t"
"buffer_load_dwordx4 v19, s[88:91], s74 offen lds\n\t"
"s_mov_b32 m0, 118784\n\t"
"s_mov_b32 s74, 2654208\n\t"
"buffer_load_dwordx4 v16, s[88:91], s74 offen lds\n\t"
"s_mov_b32 m0, 119808\n\t"
"s_mov_b32 s74, 2785280\n\t"
"buffer_load_dwordx4 v17, s[88:91], s74 offen lds\n\t"
"s_mov_b32 m0, 120832\n\t"
"s_mov_b32 s74, 2916352\n\t"
"buffer_load_dwordx4 v18, s[88:91], s74 offen lds\n\t"
"s_mov_b32 m0, 121856\n\t"
"s_mov_b32 s74, 3047424\n\t"
"buffer_load_dwordx4 v19, s[88:91], s74 offen lds\n\t"
"v_add_u32 v38, 32768, v24\n\t"
"v_add_u32 v39, 32768, v25\n\t"
"v_add_u32 v36, 98304, v24\n\t"
"v_add_u32 v37, 98304, v25\n\t"
"v_add_u32 v28, 32768, v28\n\t"
"v_add_u32 v29, 32768, v29\n\t"
"v_add_u32 v30, 32768, v30\n\t"
"v_add_u32 v31, 32768, v31\n\t"
"v_add_u32 v32, 32768, v32\n\t"
"v_add_u32 v33, 32768, v33\n\t"
"v_add_u32 v34, 32768, v34\n\t"
"v_add_u32 v35, 32768, v35\n\t"
"s_waitcnt vmcnt(16)\n\t"
"s_barrier\n\t"
"s_mov_b32 m0, 135168\n\t"
"s_mov_b32 s74, 3178496\n\t"
"buffer_load_dwordx4 v16, s[88:91], s74 offen lds\n\t"
"ds_read_b64_tr_b16 v[128:129], v38 offset:0\n\t"
"ds_read_b64_tr_b16 v[130:131], v38 offset:512\n\t"
"s_mov_b32 m0, 136192\n\t"
"s_mov_b32 s74, 3309568\n\t"
"buffer_load_dwordx4 v17, s[88:91], s74 offen lds\n\t"
"ds_read_b64_tr_b16 v[132:133], v38 offset:8192\n\t"
"ds_read_b64_tr_b16 v[134:135], v38 offset:8704\n\t"
"s_mov_b32 m0, 137216\n\t"
"s_mov_b32 s74, 3440640\n\t"
"buffer_load_dwordx4 v18, s[88:91], s74 offen lds\n\t"
"ds_read_b64_tr_b16 a[32:33], v39 offset:0\n\t"
"ds_read_b64_tr_b16 a[34:35], v39 offset:512\n\t"
"s_mov_b32 m0, 138240\n\t"
"s_mov_b32 s74, 3571712\n\t"
"buffer_load_dwordx4 v19, s[88:91], s74 offen lds\n\t"
"ds_read_b64_tr_b16 a[36:37], v39 offset:8192\n\t"
"ds_read_b64_tr_b16 a[38:39], v39 offset:8704\n\t"
"s_mov_b32 m0, 151552\n\t"
"s_mov_b32 s74, 3702784\n\t"
"buffer_load_dwordx4 v16, s[88:91], s74 offen lds\n\t"
"ds_read_b64_tr_b16 v[136:137], v38 offset:16384\n\t"
"ds_read_b64_tr_b16 v[138:139], v38 offset:16896\n\t"
"s_mov_b32 m0, 152576\n\t"
"s_mov_b32 s74, 3833856\n\t"
"buffer_load_dwordx4 v17, s[88:91], s74 offen lds\n\t"
"ds_read_b64_tr_b16 v[140:141], v38 offset:24576\n\t"
"ds_read_b64_tr_b16 v[142:143], v38 offset:25088\n\t"
"s_mov_b32 m0, 153600\n\t"
"s_mov_b32 s74, 3964928\n\t"
"buffer_load_dwordx4 v18, s[88:91], s74 offen lds\n\t"
"ds_read_b64_tr_b16 a[40:41], v39 offset:16384\n\t"
"ds_read_b64_tr_b16 a[42:43], v39 offset:16896\n\t"
"s_mov_b32 m0, 154624\n\t"
"s_mov_b32 s74, 4096000\n\t"
"buffer_load_dwordx4 v19, s[88:91], s74 offen lds\n\t"
"ds_read_b64_tr_b16 a[44:45], v39 offset:24576\n\t"
"ds_read_b64_tr_b16 a[46:47], v39 offset:25088\n\t"
"s_waitcnt lgkmcnt(0)\n\t"
"s_waitcnt vmcnt(16)\n\t"
"s_barrier\n\t"
"s_mov_b32 m0, 36864\n\t"
"s_mov_b32 s74, 32768\n\t"
"buffer_load_dwordx4 v16, s[92:95], s74 offen lds\n\t"
"ds_read_b64_tr_b16 v[144:145], v38 offset:32768\n\t"
"ds_read_b64_tr_b16 v[146:147], v38 offset:33280\n\t"
"s_mov_b32 m0, 37888\n\t"
"s_mov_b32 s74, 163840\n\t"
"buffer_load_dwordx4 v17, s[92:95], s74 offen lds\n\t"
"ds_read_b64_tr_b16 v[148:149], v38 offset:40960\n\t"
"ds_read_b64_tr_b16 v[150:151], v38 offset:41472\n\t"
"s_mov_b32 m0, 38912\n\t"
"s_mov_b32 s74, 294912\n\t"
"buffer_load_dwordx4 v18, s[92:95], s74 offen lds\n\t"
"ds_read_b64_tr_b16 a[48:49], v39 offset:32768\n\t"
"ds_read_b64_tr_b16 a[50:51], v39 offset:33280\n\t"
"s_mov_b32 m0, 39936\n\t"
"s_mov_b32 s74, 425984\n\t"
"buffer_load_dwordx4 v19, s[92:95], s74 offen lds\n\t"
"ds_read_b64_tr_b16 a[52:53], v39 offset:40960\n\t"
"ds_read_b64_tr_b16 a[54:55], v39 offset:41472\n\t"
"s_mov_b32 m0, 53248\n\t"
"s_mov_b32 s74, 557056\n\t"
"buffer_load_dwordx4 v16, s[92:95], s74 offen lds\n\t"
"ds_read_b64_tr_b16 v[152:153], v38 offset:49152\n\t"
"ds_read_b64_tr_b16 v[154:155], v38 offset:49664\n\t"
"s_mov_b32 m0, 54272\n\t"
"s_mov_b32 s74, 688128\n\t"
"buffer_load_dwordx4 v17, s[92:95], s74 offen lds\n\t"
"ds_read_b64_tr_b16 v[156:157], v38 offset:57344\n\t"
"ds_read_b64_tr_b16 v[158:159], v38 offset:57856\n\t"
"s_mov_b32 m0, 55296\n\t"
"s_mov_b32 s74, 819200\n\t"
"buffer_load_dwordx4 v18, s[92:95], s74 offen lds\n\t"
"ds_read_b64_tr_b16 a[56:57], v39 offset:49152\n\t"
"ds_read_b64_tr_b16 a[58:59], v39 offset:49664\n\t"
"s_mov_b32 m0, 56320\n\t"
"s_mov_b32 s74, 950272\n\t"
"buffer_load_dwordx4 v19, s[92:95], s74 offen lds\n\t"
"ds_read_b64_tr_b16 a[60:61], v39 offset:57344\n\t"
"ds_read_b64_tr_b16 a[62:63], v39 offset:57856\n\t"
"s_waitcnt lgkmcnt(0)\n\t"
"s_waitcnt vmcnt(16)\n\t"
"s_barrier\n\t"
"s_mov_b32 m0, 69632\n\t"
"s_mov_b32 s74, 1081344\n\t"
"buffer_load_dwordx4 v16, s[92:95], s74 offen lds\n\t"
"ds_read_b64_tr_b16 v[160:161], v36 offset:0\n\t"
"ds_read_b64_tr_b16 v[162:163], v36 offset:512\n\t"
"s_mov_b32 m0, 70656\n\t"
"s_mov_b32 s74, 1212416\n\t"
"buffer_load_dwordx4 v17, s[92:95], s74 offen lds\n\t"
"ds_read_b64_tr_b16 v[164:165], v36 offset:8192\n\t"
"ds_read_b64_tr_b16 v[166:167], v36 offset:8704\n\t"
"s_mov_b32 m0, 71680\n\t"
"s_mov_b32 s74, 1343488\n\t"
"buffer_load_dwordx4 v18, s[92:95], s74 offen lds\n\t"
"ds_read_b64_tr_b16 a[96:97], v37 offset:0\n\t"
"ds_read_b64_tr_b16 a[98:99], v37 offset:512\n\t"
"s_mov_b32 m0, 72704\n\t"
"s_mov_b32 s74, 1474560\n\t"
"buffer_load_dwordx4 v19, s[92:95], s74 offen lds\n\t"
"ds_read_b64_tr_b16 a[100:101], v37 offset:8192\n\t"
"ds_read_b64_tr_b16 a[102:103], v37 offset:8704\n\t"
"s_mov_b32 m0, 86016\n\t"
"s_mov_b32 s74, 1605632\n\t"
"buffer_load_dwordx4 v16, s[92:95], s74 offen lds\n\t"
"ds_read_b64_tr_b16 v[168:169], v36 offset:16384\n\t"
"ds_read_b64_tr_b16 v[170:171], v36 offset:16896\n\t"
"s_mov_b32 m0, 87040\n\t"
"s_mov_b32 s74, 1736704\n\t"
"buffer_load_dwordx4 v17, s[92:95], s74 offen lds\n\t"
"ds_read_b64_tr_b16 v[172:173], v36 offset:24576\n\t"
"ds_read_b64_tr_b16 v[174:175], v36 offset:25088\n\t"
"s_mov_b32 m0, 88064\n\t"
"s_mov_b32 s74, 1867776\n\t"
"buffer_load_dwordx4 v18, s[92:95], s74 offen lds\n\t"
"ds_read_b64_tr_b16 a[104:105], v37 offset:16384\n\t"
"ds_read_b64_tr_b16 a[106:107], v37 offset:16896\n\t"
"s_mov_b32 m0, 89088\n\t"
"s_mov_b32 s74, 1998848\n\t"
"buffer_load_dwordx4 v19, s[92:95], s74 offen lds\n\t"
"ds_read_b64_tr_b16 a[108:109], v37 offset:24576\n\t"
"ds_read_b64_tr_b16 a[110:111], v37 offset:25088\n\t"
"s_waitcnt lgkmcnt(0)\n\t"
"s_waitcnt vmcnt(16)\n\t"
"s_barrier\n\t"
"s_mov_b32 m0, 102400\n\t"
"s_mov_b32 s74, 2129920\n\t"
"buffer_load_dwordx4 v16, s[92:95], s74 offen lds\n\t"
"ds_read_b64_tr_b16 v[176:177], v36 offset:32768\n\t"
"ds_read_b64_tr_b16 v[178:179], v36 offset:33280\n\t"
"s_mov_b32 m0, 103424\n\t"
"s_mov_b32 s74, 2260992\n\t"
"buffer_load_dwordx4 v17, s[92:95], s74 offen lds\n\t"
"ds_read_b64_tr_b16 v[180:181], v36 offset:40960\n\t"
"ds_read_b64_tr_b16 v[182:183], v36 offset:41472\n\t"
"s_mov_b32 m0, 104448\n\t"
"s_mov_b32 s74, 2392064\n\t"
"buffer_load_dwordx4 v18, s[92:95], s74 offen lds\n\t"
"ds_read_b64_tr_b16 a[112:113], v37 offset:32768\n\t"
"ds_read_b64_tr_b16 a[114:115], v37 offset:33280\n\t"
"s_mov_b32 m0, 105472\n\t"
"s_mov_b32 s74, 2523136\n\t"
"buffer_load_dwordx4 v19, s[92:95], s74 offen lds\n\t"
"ds_read_b64_tr_b16 a[116:117], v37 offset:40960\n\t"
"ds_read_b64_tr_b16 a[118:119], v37 offset:41472\n\t"
"s_mov_b32 m0, 118784\n\t"
"s_mov_b32 s74, 2654208\n\t"
"buffer_load_dwordx4 v16, s[92:95], s74 offen lds\n\t"
"ds_read_b64_tr_b16 v[184:185], v36 offset:49152\n\t"
"ds_read_b64_tr_b16 v[186:187], v36 offset:49664\n\t"
"s_mov_b32 m0, 119808\n\t"
"s_mov_b32 s74, 2785280\n\t"
"buffer_load_dwordx4 v17, s[92:95], s74 offen lds\n\t"
"ds_read_b64_tr_b16 v[188:189], v36 offset:57344\n\t"
"ds_read_b64_tr_b16 v[190:191], v36 offset:57856\n\t"
"s_mov_b32 m0, 120832\n\t"
"s_mov_b32 s74, 2916352\n\t"
"buffer_load_dwordx4 v18, s[92:95], s74 offen lds\n\t"
"ds_read_b64_tr_b16 a[120:121], v37 offset:49152\n\t"
"ds_read_b64_tr_b16 a[122:123], v37 offset:49664\n\t"
"s_mov_b32 m0, 121856\n\t"
"s_mov_b32 s74, 3047424\n\t"
"buffer_load_dwordx4 v19, s[92:95], s74 offen lds\n\t"
"ds_read_b64_tr_b16 a[124:125], v37 offset:57344\n\t"
"ds_read_b64_tr_b16 a[126:127], v37 offset:57856\n\t"
"s_waitcnt lgkmcnt(0)\n\t"
"s_waitcnt vmcnt(16)\n\t"
"s_barrier\n\t"
"s_mov_b32 m0, 135168\n\t"
"s_mov_b32 s74, 3178496\n\t"
"buffer_load_dwordx4 v16, s[92:95], s74 offen lds\n\t"
"ds_read_b64_tr_b16 a[128:129], v38 offset:0\n\t"
"ds_read_b64_tr_b16 a[130:131], v38 offset:512\n\t"
"s_mov_b32 m0, 136192\n\t"
"s_mov_b32 s74, 3309568\n\t"
"buffer_load_dwordx4 v17, s[92:95], s74 offen lds\n\t"
"ds_read_b64_tr_b16 a[132:133], v38 offset:8192\n\t"
"ds_read_b64_tr_b16 a[134:135], v38 offset:8704\n\t"
"s_mov_b32 m0, 137216\n\t"
"s_mov_b32 s74, 3440640\n\t"
"buffer_load_dwordx4 v18, s[92:95], s74 offen lds\n\t"
"ds_read_b64_tr_b16 a[192:193], v39 offset:0\n\t"
"ds_read_b64_tr_b16 a[194:195], v39 offset:512\n\t"
"s_mov_b32 m0, 138240\n\t"
"s_mov_b32 s74, 3571712\n\t"
"buffer_load_dwordx4 v19, s[92:95], s74 offen lds\n\t"
"ds_read_b64_tr_b16 a[196:197], v39 offset:8192\n\t"
"ds_read_b64_tr_b16 a[198:199], v39 offset:8704\n\t"
"s_mov_b32 m0, 151552\n\t"
"s_mov_b32 s74, 3702784\n\t"
"buffer_load_dwordx4 v16, s[92:95], s74 offen lds\n\t"
"ds_read_b64_tr_b16 a[136:137], v38 offset:16384\n\t"
"ds_read_b64_tr_b16 a[138:139], v38 offset:16896\n\t"
"s_mov_b32 m0, 152576\n\t"
"s_mov_b32 s74, 3833856\n\t"
"buffer_load_dwordx4 v17, s[92:95], s74 offen lds\n\t"
"ds_read_b64_tr_b16 a[140:141], v38 offset:24576\n\t"
"ds_read_b64_tr_b16 a[142:143], v38 offset:25088\n\t"
"s_mov_b32 m0, 153600\n\t"
"s_mov_b32 s74, 3964928\n\t"
"buffer_load_dwordx4 v18, s[92:95], s74 offen lds\n\t"
"ds_read_b64_tr_b16 a[200:201], v39 offset:16384\n\t"
"ds_read_b64_tr_b16 a[202:203], v39 offset:16896\n\t"
"s_mov_b32 m0, 154624\n\t"
"s_mov_b32 s74, 4096000\n\t"
"buffer_load_dwordx4 v19, s[92:95], s74 offen lds\n\t"
"ds_read_b64_tr_b16 a[204:205], v39 offset:24576\n\t"
"ds_read_b64_tr_b16 a[206:207], v39 offset:25088\n\t"
"s_waitcnt lgkmcnt(0)\n\t"
"s_waitcnt vmcnt(16)\n\t"
"s_barrier\n\t"
"s_mov_b32 m0, 36864\n\t"
"s_mov_b32 s74, 8192\n\t"
"buffer_load_dwordx4 v20, s[96:99], s74 offen lds\n\t"
"ds_read_b64_tr_b16 a[144:145], v38 offset:32768\n\t"
"ds_read_b64_tr_b16 a[146:147], v38 offset:33280\n\t"
"s_mov_b32 m0, 37888\n\t"
"s_mov_b32 s74, 24576\n\t"
"buffer_load_dwordx4 v21, s[96:99], s74 offen lds\n\t"
"ds_read_b64_tr_b16 a[148:149], v38 offset:40960\n\t"
"ds_read_b64_tr_b16 a[150:151], v38 offset:41472\n\t"
"s_mov_b32 m0, 38912\n\t"
"s_mov_b32 s74, 12288\n\t"
"buffer_load_dwordx4 v22, s[96:99], s74 offen lds\n\t"
"ds_read_b64_tr_b16 a[208:209], v39 offset:32768\n\t"
"ds_read_b64_tr_b16 a[210:211], v39 offset:33280\n\t"
"s_mov_b32 m0, 39936\n\t"
"s_mov_b32 s74, 28672\n\t"
"buffer_load_dwordx4 v23, s[96:99], s74 offen lds\n\t"
"ds_read_b64_tr_b16 a[212:213], v39 offset:40960\n\t"
"ds_read_b64_tr_b16 a[214:215], v39 offset:41472\n\t"
"s_mov_b32 m0, 53248\n\t"
"s_mov_b32 s74, 8448\n\t"
"buffer_load_dwordx4 v20, s[96:99], s74 offen lds\n\t"
"ds_read_b64_tr_b16 a[152:153], v38 offset:49152\n\t"
"ds_read_b64_tr_b16 a[154:155], v38 offset:49664\n\t"
"s_mov_b32 m0, 54272\n\t"
"s_mov_b32 s74, 24832\n\t"
"buffer_load_dwordx4 v21, s[96:99], s74 offen lds\n\t"
"ds_read_b64_tr_b16 a[156:157], v38 offset:57344\n\t"
"ds_read_b64_tr_b16 a[158:159], v38 offset:57856\n\t"
"s_mov_b32 m0, 55296\n\t"
"s_mov_b32 s74, 12544\n\t"
"buffer_load_dwordx4 v22, s[96:99], s74 offen lds\n\t"
"ds_read_b64_tr_b16 a[216:217], v39 offset:49152\n\t"
"ds_read_b64_tr_b16 a[218:219], v39 offset:49664\n\t"
"s_mov_b32 m0, 56320\n\t"
"s_mov_b32 s74, 28928\n\t"
"buffer_load_dwordx4 v23, s[96:99], s74 offen lds\n\t"
"ds_read_b64_tr_b16 a[220:221], v39 offset:57344\n\t"
"ds_read_b64_tr_b16 a[222:223], v39 offset:57856\n\t"
"s_waitcnt lgkmcnt(0)\n\t"
"s_waitcnt vmcnt(16)\n\t"
"s_barrier\n\t"
"s_mov_b32 m0, 69632\n\t"
"s_mov_b32 s74, 8704\n\t"
"buffer_load_dwordx4 v20, s[96:99], s74 offen lds\n\t"
"ds_read_b64_tr_b16 a[160:161], v36 offset:0\n\t"
"ds_read_b64_tr_b16 a[162:163], v36 offset:512\n\t"
"s_mov_b32 m0, 70656\n\t"
"s_mov_b32 s74, 25088\n\t"
"buffer_load_dwordx4 v21, s[96:99], s74 offen lds\n\t"
"ds_read_b64_tr_b16 a[164:165], v36 offset:8192\n\t"
"ds_read_b64_tr_b16 a[166:167], v36 offset:8704\n\t"
"s_mov_b32 m0, 71680\n\t"
"s_mov_b32 s74, 12800\n\t"
"buffer_load_dwordx4 v22, s[96:99], s74 offen lds\n\t"
"ds_read_b64_tr_b16 a[224:225], v37 offset:0\n\t"
"ds_read_b64_tr_b16 a[226:227], v37 offset:512\n\t"
"s_mov_b32 m0, 72704\n\t"
"s_mov_b32 s74, 29184\n\t"
"buffer_load_dwordx4 v23, s[96:99], s74 offen lds\n\t"
"ds_read_b64_tr_b16 a[228:229], v37 offset:8192\n\t"
"ds_read_b64_tr_b16 a[230:231], v37 offset:8704\n\t"
"s_mov_b32 m0, 86016\n\t"
"s_mov_b32 s74, 8960\n\t"
"buffer_load_dwordx4 v20, s[96:99], s74 offen lds\n\t"
"ds_read_b64_tr_b16 a[168:169], v36 offset:16384\n\t"
"ds_read_b64_tr_b16 a[170:171], v36 offset:16896\n\t"
"s_mov_b32 m0, 87040\n\t"
"s_mov_b32 s74, 25344\n\t"
"buffer_load_dwordx4 v21, s[96:99], s74 offen lds\n\t"
"ds_read_b64_tr_b16 a[172:173], v36 offset:24576\n\t"
"ds_read_b64_tr_b16 a[174:175], v36 offset:25088\n\t"
"s_mov_b32 m0, 88064\n\t"
"s_mov_b32 s74, 13056\n\t"
"buffer_load_dwordx4 v22, s[96:99], s74 offen lds\n\t"
"ds_read_b64_tr_b16 a[232:233], v37 offset:16384\n\t"
"ds_read_b64_tr_b16 a[234:235], v37 offset:16896\n\t"
"s_mov_b32 m0, 89088\n\t"
"s_mov_b32 s74, 29440\n\t"
"buffer_load_dwordx4 v23, s[96:99], s74 offen lds\n\t"
"ds_read_b64_tr_b16 a[236:237], v37 offset:24576\n\t"
"ds_read_b64_tr_b16 a[238:239], v37 offset:25088\n\t"
"s_waitcnt lgkmcnt(0)\n\t"
"s_waitcnt vmcnt(16)\n\t"
"s_barrier\n\t"
"s_mov_b32 m0, 102400\n\t"
"s_mov_b32 s74, 32768\n\t"
"buffer_load_dwordx4 v16, s[84:87], s74 offen lds\n\t"
"ds_read_b64_tr_b16 a[176:177], v36 offset:32768\n\t"
"ds_read_b64_tr_b16 a[178:179], v36 offset:33280\n\t"
"s_mov_b32 m0, 103424\n\t"
"s_mov_b32 s74, 163840\n\t"
"buffer_load_dwordx4 v17, s[84:87], s74 offen lds\n\t"
"ds_read_b64_tr_b16 a[180:181], v36 offset:40960\n\t"
"ds_read_b64_tr_b16 a[182:183], v36 offset:41472\n\t"
"s_mov_b32 m0, 104448\n\t"
"s_mov_b32 s74, 294912\n\t"
"buffer_load_dwordx4 v18, s[84:87], s74 offen lds\n\t"
"ds_read_b64_tr_b16 a[240:241], v37 offset:32768\n\t"
"ds_read_b64_tr_b16 a[242:243], v37 offset:33280\n\t"
"s_mov_b32 m0, 105472\n\t"
"s_mov_b32 s74, 425984\n\t"
"buffer_load_dwordx4 v19, s[84:87], s74 offen lds\n\t"
"ds_read_b64_tr_b16 a[244:245], v37 offset:40960\n\t"
"ds_read_b64_tr_b16 a[246:247], v37 offset:41472\n\t"
"s_mov_b32 m0, 118784\n\t"
"s_mov_b32 s74, 557056\n\t"
"buffer_load_dwordx4 v16, s[84:87], s74 offen lds\n\t"
"ds_read_b64_tr_b16 a[184:185], v36 offset:49152\n\t"
"ds_read_b64_tr_b16 a[186:187], v36 offset:49664\n\t"
"s_mov_b32 m0, 119808\n\t"
"s_mov_b32 s74, 688128\n\t"
"buffer_load_dwordx4 v17, s[84:87], s74 offen lds\n\t"
"ds_read_b64_tr_b16 a[188:189], v36 offset:57344\n\t"
"ds_read_b64_tr_b16 a[190:191], v36 offset:57856\n\t"
"s_mov_b32 m0, 120832\n\t"
"s_mov_b32 s74, 819200\n\t"
"buffer_load_dwordx4 v18, s[84:87], s74 offen lds\n\t"
"ds_read_b64_tr_b16 a[248:249], v37 offset:49152\n\t"
"ds_read_b64_tr_b16 a[250:251], v37 offset:49664\n\t"
"s_mov_b32 m0, 121856\n\t"
"s_mov_b32 s74, 950272\n\t"
"buffer_load_dwordx4 v19, s[84:87], s74 offen lds\n\t"
"ds_read_b64_tr_b16 a[252:253], v37 offset:57344\n\t"
"ds_read_b64_tr_b16 a[254:255], v37 offset:57856\n\t"
"s_waitcnt lgkmcnt(0)\n\t"
"s_waitcnt vmcnt(16)\n\t"
"s_barrier\n\t"
"s_mov_b32 m0, 135168\n\t"
"s_mov_b32 s74, 1081344\n\t"
"buffer_load_dwordx4 v16, s[84:87], s74 offen lds\n\t"
"ds_read_b64_tr_b16 v[192:193], v28 offset:0\n\t"
"ds_read_b64_tr_b16 v[194:195], v28 offset:512\n\t"
"ds_read_b64_tr_b16 v[196:197], v28 offset:8192\n\t"
"ds_read_b64_tr_b16 v[198:199], v28 offset:8704\n\t"
"s_mov_b32 m0, 136192\n\t"
"s_mov_b32 s74, 1212416\n\t"
"buffer_load_dwordx4 v17, s[84:87], s74 offen lds\n\t"
"ds_read_b64_tr_b16 v[200:201], v29 offset:0\n\t"
"ds_read_b64_tr_b16 v[202:203], v29 offset:512\n\t"
"ds_read_b64_tr_b16 v[204:205], v29 offset:8192\n\t"
"ds_read_b64_tr_b16 v[206:207], v29 offset:8704\n\t"
"s_mov_b32 m0, 137216\n\t"
"s_mov_b32 s74, 1343488\n\t"
"buffer_load_dwordx4 v18, s[84:87], s74 offen lds\n\t"
"ds_read_b64_tr_b16 v[208:209], v30 offset:0\n\t"
"ds_read_b64_tr_b16 v[210:211], v30 offset:512\n\t"
"ds_read_b64_tr_b16 v[212:213], v30 offset:8192\n\t"
"ds_read_b64_tr_b16 v[214:215], v30 offset:8704\n\t"
"s_mov_b32 m0, 138240\n\t"
"s_mov_b32 s74, 1474560\n\t"
"buffer_load_dwordx4 v19, s[84:87], s74 offen lds\n\t"
"ds_read_b64_tr_b16 v[216:217], v31 offset:0\n\t"
"ds_read_b64_tr_b16 v[218:219], v31 offset:512\n\t"
"ds_read_b64_tr_b16 v[220:221], v31 offset:8192\n\t"
"ds_read_b64_tr_b16 v[222:223], v31 offset:8704\n\t"
"s_mov_b32 m0, 151552\n\t"
"s_mov_b32 s74, 1605632\n\t"
"buffer_load_dwordx4 v16, s[84:87], s74 offen lds\n\t"
"ds_read_b64_tr_b16 v[224:225], v32 offset:0\n\t"
"ds_read_b64_tr_b16 v[226:227], v32 offset:512\n\t"
"ds_read_b64_tr_b16 v[228:229], v32 offset:8192\n\t"
"ds_read_b64_tr_b16 v[230:231], v32 offset:8704\n\t"
"s_mov_b32 m0, 152576\n\t"
"s_mov_b32 s74, 1736704\n\t"
"buffer_load_dwordx4 v17, s[84:87], s74 offen lds\n\t"
"ds_read_b64_tr_b16 v[232:233], v33 offset:0\n\t"
"ds_read_b64_tr_b16 v[234:235], v33 offset:512\n\t"
"ds_read_b64_tr_b16 v[236:237], v33 offset:8192\n\t"
"ds_read_b64_tr_b16 v[238:239], v33 offset:8704\n\t"
"s_mov_b32 m0, 153600\n\t"
"s_mov_b32 s74, 1867776\n\t"
"buffer_load_dwordx4 v18, s[84:87], s74 offen lds\n\t"
"ds_read_b64_tr_b16 v[240:241], v34 offset:0\n\t"
"ds_read_b64_tr_b16 v[242:243], v34 offset:512\n\t"
"ds_read_b64_tr_b16 v[244:245], v34 offset:8192\n\t"
"ds_read_b64_tr_b16 v[246:247], v34 offset:8704\n\t"
"s_mov_b32 m0, 154624\n\t"
"s_mov_b32 s74, 1998848\n\t"
"buffer_load_dwordx4 v19, s[84:87], s74 offen lds\n\t"
"ds_read_b64_tr_b16 v[248:249], v35 offset:0\n\t"
"ds_read_b64_tr_b16 v[250:251], v35 offset:512\n\t"
"ds_read_b64_tr_b16 v[252:253], v35 offset:8192\n\t"
"ds_read_b64_tr_b16 v[254:255], v35 offset:8704\n\t"
"s_waitcnt lgkmcnt(0)\n\t"
"s_waitcnt vmcnt(16)\n\t"
"s_barrier\n\t"
"s_mov_b32 m0, 36864\n\t"
"s_mov_b32 s74, 32768\n\t"
"buffer_load_dwordx4 v16, s[88:91], s74 offen lds\n\t"
"s_mov_b32 m0, 37888\n\t"
"s_mov_b32 s74, 163840\n\t"
"buffer_load_dwordx4 v17, s[88:91], s74 offen lds\n\t"
"s_mov_b32 m0, 38912\n\t"
"s_mov_b32 s74, 294912\n\t"
"buffer_load_dwordx4 v18, s[88:91], s74 offen lds\n\t"
"s_mov_b32 m0, 39936\n\t"
"s_mov_b32 s74, 425984\n\t"
"buffer_load_dwordx4 v19, s[88:91], s74 offen lds\n\t"
"s_mov_b32 m0, 53248\n\t"
"s_mov_b32 s74, 557056\n\t"
"buffer_load_dwordx4 v16, s[88:91], s74 offen lds\n\t"
"s_mov_b32 m0, 54272\n\t"
"s_mov_b32 s74, 688128\n\t"
"buffer_load_dwordx4 v17, s[88:91], s74 offen lds\n\t"
"s_mov_b32 m0, 55296\n\t"
"s_mov_b32 s74, 819200\n\t"
"buffer_load_dwordx4 v18, s[88:91], s74 offen lds\n\t"
"s_mov_b32 m0, 56320\n\t"
"s_mov_b32 s74, 950272\n\t"
"buffer_load_dwordx4 v19, s[88:91], s74 offen lds\n\t"
"global_load_dwordx2 v[58:59], v57, %[ctl] sc1\n\t"
"s_waitcnt vmcnt(16)\n\t"
"s_barrier\n\t"
"s_mov_b32 m0, 69632\n\t"
"s_mov_b32 s74, 1081344\n\t"
"buffer_load_dwordx4 v16, s[88:91], s74 offen lds\n\t"
"ds_read_b64_tr_b16 v[64:65], v36 offset:0\n\t"
"ds_read_b64_tr_b16 v[66:67], v36 offset:512\n\t"
"s_mov_b32 m0, 70656\n\t"
"s_mov_b32 s74, 1212416\n\t"
"buffer_load_dwordx4 v17, s[88:91], s74 offen lds\n\t"
"ds_read_b64_tr_b16 v[68:69], v36 offset:8192\n\t"
"ds_read_b64_tr_b16 v[70:71], v36 offset:8704\n\t"
"s_mov_b32 m0, 71680\n\t"
"s_mov_b32 s74, 1343488\n\t"
"buffer_load_dwordx4 v18, s[88:91], s74 offen lds\n\t"
"ds_read_b64_tr_b16 a[0:1], v37 offset:0\n\t"
"ds_read_b64_tr_b16 a[2:3], v37 offset:512\n\t"
"s_mov_b32 m0, 72704\n\t"
"s_mov_b32 s74, 1474560\n\t"
"buffer_load_dwordx4 v19, s[88:91], s74 offen lds\n\t"
"ds_read_b64_tr_b16 a[4:5], v37 offset:8192\n\t"
"ds_read_b64_tr_b16 a[6:7], v37 offset:8704\n\t"
"s_mov_b32 m0, 86016\n\t"
"s_mov_b32 s74, 1605632\n\t"
"buffer_load_dwordx4 v16, s[88:91], s74 offen lds\n\t"
"ds_read_b64_tr_b16 v[80:81], v36 offset:16384\n\t"
"ds_read_b64_tr_b16 v[82:83], v36 offset:16896\n\t"
"s_mov_b32 m0, 87040\n\t"
"s_mov_b32 s74, 1736704\n\t"
"buffer_load_dwordx4 v17, s[88:91], s74 offen lds\n\t"
"ds_read_b64_tr_b16 v[84:85], v36 offset:24576\n\t"
"ds_read_b64_tr_b16 v[86:87], v36 offset:25088\n\t"
"s_mov_b32 m0, 88064\n\t"
"s_mov_b32 s74, 1867776\n\t"
"buffer_load_dwordx4 v18, s[88:91], s74 offen lds\n\t"
"ds_read_b64_tr_b16 a[8:9], v37 offset:16384\n\t"
"ds_read_b64_tr_b16 a[10:11], v37 offset:16896\n\t"
"s_mov_b32 m0, 89088\n\t"
"s_mov_b32 s74, 1998848\n\t"
"buffer_load_dwordx4 v19, s[88:91], s74 offen lds\n\t"
"ds_read_b64_tr_b16 a[12:13], v37 offset:24576\n\t"
"ds_read_b64_tr_b16 a[14:15], v37 offset:25088\n\t"
"s_waitcnt lgkmcnt(0)\n\t"
"s_waitcnt vmcnt(16)\n\t"
"s_barrier\n\t"
"ds_read_b64_tr_b16 v[96:97], v36 offset:32768\n\t"
"ds_read_b64_tr_b16 v[98:99], v36 offset:33280\n\t"
"ds_read_b64_tr_b16 v[100:101], v36 offset:40960\n\t"
"ds_read_b64_tr_b16 v[102:103], v36 offset:41472\n\t"
"ds_read_b64_tr_b16 a[16:17], v37 offset:32768\n\t"
"ds_read_b64_tr_b16 a[18:19], v37 offset:33280\n\t"
"ds_read_b64_tr_b16 a[20:21], v37 offset:40960\n\t"
"ds_read_b64_tr_b16 a[22:23], v37 offset:41472\n\t"
"ds_read_b64_tr_b16 v[112:113], v36 offset:49152\n\t"
"ds_read_b64_tr_b16 v[114:115], v36 offset:49664\n\t"
"ds_read_b64_tr_b16 v[116:117], v36 offset:57344\n\t"
"ds_read_b64_tr_b16 v[118:119], v36 offset:57856\n\t"
"ds_read_b64_tr_b16 a[24:25], v37 offset:49152\n\t"
"ds_read_b64_tr_b16 a[26:27], v37 offset:49664\n\t"
"ds_read_b64_tr_b16 a[28:29], v37 offset:57344\n\t"
"ds_read_b64_tr_b16 a[30:31], v37 offset:57856\n\t"
"s_waitcnt lgkmcnt(0)\n\t"
"s_waitcnt vmcnt(8)\n\t"
"v_readfirstlane_b32 s76, v56\n\t"
"s_nop 3\n\t"
"s_add_u32 s76, s76, 1\n\t"
"s_mov_b32 s60, 0\n\t"
"v_mov_b32 v56, 0\n\t"
"s_branch CW1_polc3_%=\n\t"
"CW1_pol1_%=:\n\t"
"global_load_dwordx2 v[58:59], v56, %[ctl] sc1\n\t"
"s_waitcnt vmcnt(0)\n\t"
"CW1_polc3_%=:\n\t"
"v_readfirstlane_b32 s74, v58\n\t"
"v_readfirstlane_b32 s75, v59\n\t"
"s_nop 3\n\t"
"s_cmp_eq_u32 s75, s76\n\t"
"s_cbranch_scc1 CW1_pold2_%=\n\t"
"s_sleep 2\n\t"
"s_add_u32 s60, s60, 1\n\t"
"s_cmp_lt_u32 s60, 0x10000\n\t"
"s_cbranch_scc1 CW1_pol1_%=\n\t"
"CW1_pold2_%=:\n\t"
"s_andn2_b32 s48, s74, 0xfff\n\t"
"s_lshr_b32 s80, s74, 1\n\t"
"s_and_b32 s74, s74, 1\n\t"
"s_lshl_b32 s74, s74, 13\n\t"
"s_mov_b64 s[40:41], %[comm]\n\t"
"s_add_u32 s40, s40, s74\n\t"
"s_addc_u32 s41, s41, 0\n\t"
"s_and_b32 s41, s41, 0xffff\n\t"
"s_mov_b32 s42, 0x1fe000\n\t"
"s_mov_b32 s43, 0x20000\n\t"
"s_add_u32 s75, %[me7], 4096\n\t"
"v_mov_b32 v56, s75\n\t"
"buffer_load_dword v8, v10, s[40:43], 0 offen sc1\n\t"
"s_lshl_b32 s81, %[me14], 1\n\t"
"s_add_u32 s81, s81, 4096\n\t"
"s_lshr_b32 s74, s81, 24\n\t"
"s_lshr_b32 s74, s80, s74\n\t"
"s_lshr_b32 s75, s81, 21\n\t"
"s_xor_b32 s74, s74, s75\n\t"
"s_and_b32 s74, s74, 1\n\t"
"s_lshl_b32 s74, s74, 13\n\t"
"s_or_b32 s76, s81, s74\n\t"
"s_add_u32 s61, s81, 1044480\n\t"
"s_lshr_b32 s74, s61, 24\n\t"
"s_lshr_b32 s74, s80, s74\n\t"
"s_lshr_b32 s75, s61, 21\n\t"
"s_xor_b32 s74, s74, s75\n\t"
"s_and_b32 s74, s74, 1\n\t"
"s_lshl_b32 s74, s74, 13\n\t"
"s_or_b32 s61, s61, s74\n\t"
"s_barrier\n\t"
"ds_read_b64_tr_b16 v[72:73], v38 offset:0\n\t"
"ds_read_b64_tr_b16 v[74:75], v38 offset:512\n\t"
"ds_read_b64_tr_b16 v[76:77], v38 offset:8192\n\t"
"ds_read_b64_tr_b16 v[78:79], v38 offset:8704\n\t"
"ds_read_b64_tr_b16 a[64:65], v39 offset:0\n\t"
"ds_read_b64_tr_b16 a[66:67], v39 offset:512\n\t"
"ds_read_b64_tr_b16 a[68:69], v39 offset:8192\n\t"
"ds_read_b64_tr_b16 a[70:71], v39 offset:8704\n\t"
"ds_read_b64_tr_b16 v[88:89], v38 offset:16384\n\t"
"ds_read_b64_tr_b16 v[90:91], v38 offset:16896\n\t"
"ds_read_b64_tr_b16 v[92:93], v38 offset:24576\n\t"
"ds_read_b64_tr_b16 v[94:95], v38 offset:25088\n\t"
"ds_read_b64_tr_b16 a[72:73], v39 offset:16384\n\t"
"ds_read_b64_tr_b16 a[74:75], v39 offset:16896\n\t"
"ds_read_b64_tr_b16 a[76:77], v39 offset:24576\n\t"
"ds_read_b64_tr_b16 a[78:79], v39 offset:25088\n\t"
"s_waitcnt lgkmcnt(0)\n\t"
"s_waitcnt vmcnt(0)\n\t"
"global_load_dword v57, v56, %[ctl] sc1\n\t"
"s_barrier\n\t"
"ds_read_b64_tr_b16 v[104:105], v38 offset:32768\n\t"
"ds_read_b64_tr_b16 v[106:107], v38 offset:33280\n\t"
"ds_read_b64_tr_b16 v[108:109], v38 offset:40960\n\t"
"ds_read_b64_tr_b16 v[110:111], v38 offset:41472\n\t"
"ds_read_b64_tr_b16 a[80:81], v39 offset:32768\n\t"
"ds_read_b64_tr_b16 a[82:83], v39 offset:33280\n\t"
"ds_read_b64_tr_b16 a[84:85], v39 offset:40960\n\t"
"ds_read_b64_tr_b16 a[86:87], v39 offset:41472\n\t"
"ds_read_b64_tr_b16 v[120:121], v38 offset:49152\n\t"
"ds_read_b64_tr_b16 v[122:123], v38 offset:49664\n\t"
"ds_read_b64_tr_b16 v[124:125], v38 offset:57344\n\t"
"ds_read_b64_tr_b16 v[126:127], v38 offset:57856\n\t"
"ds_read_b64_tr_b16 a[88:89], v39 offset:49152\n\t"
"ds_read_b64_tr_b16 a[90:91], v39 offset:49664\n\t"
"ds_read_b64_tr_b16 a[92:93], v39 offset:57344\n\t"
"ds_read_b64_tr_b16 a[94:95], v39 offset:57856\n\t"
"s_waitcnt lgkmcnt(0)\n\t"
"s_waitcnt vmcnt(0)\n\t"
"s_mov_b32 s60, 0\n\t"
"s_branch CW1_kc6_%=\n\t"
"CW1_kw4_%=:\n\t"
"global_load_dword v57, v56, %[ctl] sc1\n\t"
"s_waitcnt vmcnt(0)\n\t"
"CW1_kc6_%=:\n\t"
"v_readfirstlane_b32 s75, v57\n\t"
"s_nop 3\n\t"
"s_lshr_b32 s75, s75, 24\n\t"
"s_and_b32 s75, s75, 0xff\n\t"
"s_cmp_ge_u32 s75, 16\n\t"
"s_cbranch_scc1 CW1_kd5_%=\n\t"
"s_sleep 2\n\t"
"s_add_u32 s60, s60, 1\n\t"
"s_cmp_lt_u32 s60, 0x8000\n\t"
"s_cbranch_scc1 CW1_kw4_%=\n\t"
"CW1_kd5_%=:\n\t"
"s_barrier\n\t"
"s_mov_b32 m0, 20480\n\t"
"s_nop 0\n\t"
"buffer_load_dwordx4 v0, s[44:47], s76 offen offset:0 sc1 lds\n\t"
"buffer_load_dwordx4 v0, s[44:47], s76 offen offset:1024 sc1 lds\n\t"
"buffer_load_dwordx4 v0, s[44:47], s76 offen offset:2048 sc1 lds\n\t"
"buffer_load_dwordx4 v0, s[44:47], s76 offen offset:3072 sc1 lds\n\t"
"buffer_load_dword v14, v13, s[44:47], s61 offen sc1\n\t"
"ds_write_b128 v2, v[64:67] offset:0\n\t"
"ds_write_b128 v2, v[68:71] offset:4096\n\t"
"ds_write_b128 v2, v[72:75] offset:8192\n\t"
"ds_write_b128 v2, v[76:79] offset:12288\n\t"
"ds_write_b128 v2, v[80:83] offset:16384\n\t"
"ds_write_b128 v2, v[84:87] offset:20480\n\t"
"ds_write_b128 v2, v[88:91] offset:24576\n\t"
"ds_write_b128 v2, v[92:95] offset:28672\n\t"
"ds_write_b128 v2, v[96:99] offset:32768\n\t"
"ds_write_b128 v2, v[100:103] offset:36864\n\t"
"ds_write_b128 v2, v[104:107] offset:40960\n\t"
"ds_write_b128 v2, v[108:111] offset:45056\n\t"
"ds_write_b128 v2, v[112:115] offset:49152\n\t"
"ds_write_b128 v2, v[116:119] offset:53248\n\t"
"ds_write_b128 v2, v[120:123] offset:57344\n\t"
"ds_write_b128 v2, v[124:127] offset:61440\n\t"
"s_waitcnt lgkmcnt(0)\n\t"
"v_mov_b64 v[20:21], v[48:49]\n\t"
"v_mov_b64 v[22:23], v[50:51]\n\t"
"v_mov_b64 v[36:37], v[52:53]\n\t"
"v_mov_b64 v[38:39], v[54:55]\n\t"
"s_mov_b32 s62, 0xffbfffbf\n\t"
"s_mov_b32 s63, 0xffbfffbf\n\t"
"s_mov_b32 s78, 0x3f803f80\n\t"
"s_mov_b32 s79, 0x3f803f80\n\t"
"s_mov_b32 s38, 0x3e0293ee\n\t"
"s_mov_b32 s39, 0x3e0293ee\n\t"
"s_mov_b32 s72, 0xbe0293ee\n\t"
"s_sub_u32 s77, 100, s48\n\t"
"s_mov_b32 s64, 0\n\t"
"ds_read_b128 v[56:59], v2 offset:16384\n\t"
"ds_read_b128 v[60:63], v2 offset:20480\n\t"
"ds_read_b128 v[72:75], v2 offset:24576\n\t"
"ds_read_b128 v[76:79], v2 offset:28672\n\t"
"ds_read_b128 v[80:83], v2 offset:0\n\t"
"ds_read_b128 v[84:87], v2 offset:4096\n\t"
"ds_read_b128 v[88:91], v2 offset:8192\n\t"
"ds_read_b128 v[92:95], v2 offset:12288\n\t"
"ds_read_b128 v[96:99], v2 offset:32768\n\t"
"ds_read_b128 v[100:103], v2 offset:36864\n\t"
"ds_read_b128 v[104:107], v2 offset:40960\n\t"
"ds_read_b128 v[108:111], v2 offset:45056\n\t"
"ds_read_b128 v[112:115], v2 offset:49152\n\t"
"ds_read_b128 v[116:119], v2 offset:53248\n\t"
"ds_read_b128 v[120:123], v2 offset:57344\n\t"
"ds_read_b128 v[124:127], v2 offset:61440\n\t"
"v_mov_b64 v[48:49], 0\n\t"
"v_mov_b64 v[50:51], 0\n\t"
"v_mov_b64 v[52:53], 0\n\t"
"v_mov_b64 v[54:55], 0\n\t"
"v_mov_b64 v[64:65], 0\n\t"
"v_mov_b64 v[66:67], 0\n\t"
"v_mov_b64 v[68:69], 0\n\t"
"v_mov_b64 v[70:71], 0\n\t"
"s_mov_b32 s65, 0\n\t"
"s_mov_b32 s66, 112\n\t"
"s_mov_b32 s67, s48\n\t"
"s_add_u32 s65, s65, 524288\n\t"
"s_cmp_eq_u32 s65, 1572864\n\t"
"s_cselect_b32 s65, 0, s65\n\t"
"s_lshl_b32 s71, s64, 17\n\t"
"s_and_b32 s68, s64, 1\n\t"
"s_lshl_b32 s68, s68, 9\n\t"
"s_add_u32 s81, s81, 1048576\n\t"
"s_lshr_b32 s74, s81, 24\n\t"
"s_lshr_b32 s74, s80, s74\n\t"
"s_lshr_b32 s75, s81, 21\n\t"
"s_xor_b32 s74, s74, s75\n\t"
"s_and_b32 s74, s74, 1\n\t"
"s_lshl_b32 s74, s74, 13\n\t"
"s_or_b32 s76, s81, s74\n\t"
"s_add_u32 s61, s81, 1044480\n\t"
"s_lshr_b32 s74, s61, 24\n\t"
"s_lshr_b32 s74, s80, s74\n\t"
"s_lshr_b32 s75, s61, 21\n\t"
"s_xor_b32 s74, s74, s75\n\t"
"s_and_b32 s74, s74, 1\n\t"
"s_lshl_b32 s74, s74, 13\n\t"
"s_or_b32 s61, s61, s74\n\t"
"s_waitcnt lgkmcnt(0)\n\t"
"s_waitcnt vmcnt(0)\n\t"
"s_mov_b32 s60, 0\n\t"
"CW1_top7_%=:\n\t"
"s_waitcnt vmcnt(1)\n\t"
"CW1_top211_%=:\n\t"
"ds_write_b128 v1, v[20:23] offset:0\n\t"
"v_smfmac_f32_16x16x64_bf16 v[48:51], v[20:23], v[56:63], v9\n\t"
"v_max3_u32 v15, v20, v21, v22\n\t"
"v_smfmac_f32_16x16x64_bf16 v[52:55], v[20:23], v[72:79], v9\n\t"
"v_smfmac_f32_16x16x64_bf16 v[64:67], v[20:23], a[136:143], v9\n\t"
"v_smfmac_f32_16x16x64_bf16 v[68:71], v[20:23], a[200:207], v9\n\t"
"s_waitcnt vmcnt(0)\n\t"
"ds_write_b128 v1, v[36:39] offset:4096\n\t"
"v_max3_u32 v15, v15, v23, v36\n\t"
"v_max3_u32 v15, v15, v37, v38\n\t"
"v_max_u32 v15, v15, v39\n\t"
"v_cmp_le_u32 vcc, s62, v15\n\t"
"s_cbranch_vccnz CW1_art9_%=\n\t"
"CW1_aok10_%=:\n\t"
"v_smfmac_f32_16x16x64_bf16 v[48:51], v[36:39], v[136:143], v9\n\t"
"v_mov_b64 v[56:57], 0\n\t"
"v_smfmac_f32_16x16x64_bf16 v[52:55], v[36:39], v[168:175], v9\n\t"
"v_mov_b64 v[58:59], 0\n\t"
"v_mov_b64 v[60:61], 0\n\t"
"v_mov_b64 v[62:63], 0\n\t"
"s_waitcnt lgkmcnt(0)\n\t"
"s_barrier\n\t"
"ds_read_b128 v[16:19], v0 offset:0\n\t"
"ds_read_b128 v[24:27], v0 offset:2048\n\t"
"v_smfmac_f32_16x16x64_bf16 v[56:59], v[20:23], a[8:15], v9\n\t"
"ds_read_b128 v[28:31], v0 offset:3072\n\t"
"v_smfmac_f32_16x16x64_bf16 v[56:59], v[36:39], a[40:47], v9\n\t"
"ds_read_b128 v[32:35], v0 offset:4096\n\t"
"v_smfmac_f32_16x16x64_bf16 v[60:63], v[20:23], a[72:79], v9\n\t"
"ds_read_b128 v[40:43], v0 offset:6144\n\t"
"v_smfmac_f32_16x16x64_bf16 v[60:63], v[36:39], a[104:111], v9\n\t"
"ds_read_b128 v[44:47], v0 offset:7168\n\t"
"v_smfmac_f32_16x16x64_bf16 v[64:67], v[36:39], a[168:175], v9\n\t"
"s_waitcnt lgkmcnt(4)\n\t"
"v_smfmac_f32_16x16x64_bf16 v[48:51], v[16:19], v[80:87], v9\n\t"
"v_smfmac_f32_16x16x64_bf16 v[52:55], v[16:19], v[88:95], v9\n\t"
"buffer_load_dword v14, v13, s[44:47], s61 offen sc1\n\t"
"v_smfmac_f32_16x16x64_bf16 v[48:51], v[24:27], v[96:103], v9\n\t"
"v_smfmac_f32_16x16x64_bf16 v[52:55], v[24:27], v[104:111], v9\n\t"
"s_waitcnt lgkmcnt(0)\n\t"
"v_smfmac_f32_16x16x64_bf16 v[48:51], v[28:31], v[112:119], v9\n\t"
"v_smfmac_f32_16x16x64_bf16 v[52:55], v[28:31], v[120:127], v9\n\t"
"v_smfmac_f32_16x16x64_bf16 v[48:51], v[32:35], v[128:135], v9\n\t"
"ds_read_b128 v[72:75], v0 offset:16384\n\t"
"v_smfmac_f32_16x16x64_bf16 v[52:55], v[32:35], v[160:167], v9\n\t"
"ds_read_b128 v[76:79], v0 offset:17408\n\t"
"v_smfmac_f32_16x16x64_bf16 v[48:51], v[40:43], v[144:151], v9\n\t"
"ds_read_b128 v[80:83], v0 offset:18432\n\t"
"v_smfmac_f32_16x16x64_bf16 v[52:55], v[40:43], v[176:183], v9\n\t"
"ds_read_b128 v[84:87], v0 offset:19456\n\t"
"v_smfmac_f32_16x16x64_bf16 v[48:51], v[44:47], v[152:159], v9\n\t"
"ds_read_b128 v[88:91], v0 offset:20480\n\t"
"v_smfmac_f32_16x16x64_bf16 v[52:55], v[44:47], v[184:191], v9\n\t"
"ds_read_b128 v[92:95], v0 offset:21504\n\t"
"v_smfmac_f32_16x16x64_bf16 v[56:59], v[16:19], a[0:7], v9\n\t"
"ds_read_b128 v[96:99], v0 offset:22528\n\t"
"v_smfmac_f32_16x16x64_bf16 v[60:63], v[16:19], a[64:71], v9\n\t"
"ds_read_b128 v[100:103], v0 offset:23552\n\t"
"v_smfmac_f32_16x16x64_bf16 v[56:59], v[24:27], a[16:23], v9\n\t"
"ds_read_b128 v[104:107], v0 offset:26624\n\t"
"v_smfmac_f32_16x16x64_bf16 v[60:63], v[24:27], a[80:87], v9\n\t"
"ds_read_b128 v[108:111], v0 offset:27648\n\t"
"v_smfmac_f32_16x16x64_bf16 v[56:59], v[28:31], a[24:31], v9\n\t"
"v_add_f32 v48, v48, v49\n\t"
"v_smfmac_f32_16x16x64_bf16 v[60:63], v[28:31], a[88:95], v9\n\t"
"v_add_f32 v50, v50, v51\n\t"
"v_smfmac_f32_16x16x64_bf16 v[56:59], v[32:35], a[32:39], v9\n\t"
"v_cvt_pk_bf16_f32 v124, v48, v50\n\t"
"v_smfmac_f32_16x16x64_bf16 v[60:63], v[32:35], a[96:103], v9\n\t"
"v_add_f32 v52, v52, v53\n\t"
"v_smfmac_f32_16x16x64_bf16 v[56:59], v[40:43], a[48:55], v9\n\t"
"v_add_f32 v54, v54, v55\n\t"
"v_smfmac_f32_16x16x64_bf16 v[60:63], v[40:43], a[112:119], v9\n\t"
"v_cvt_pk_bf16_f32 v125, v52, v54\n\t"
"v_smfmac_f32_16x16x64_bf16 v[56:59], v[44:47], a[56:63], v9\n\t"
"ds_write_b64 v4, v[124:125] offset:8192\n\t"
"v_smfmac_f32_16x16x64_bf16 v[60:63], v[44:47], a[120:127], v9\n\t"
"v_smfmac_f32_16x16x64_bf16 v[64:67], v[16:19], a[128:135], v9\n\t"
"s_nop 4\n\t"
"v_add_f32 v56, v56, v57\n\t"
"v_add_f32 v58, v58, v59\n\t"
"v_cvt_pk_bf16_f32 v126, v56, v58\n\t"
"v_add_f32 v60, v60, v61\n\t"
"v_add_f32 v62, v62, v63\n\t"
"v_cvt_pk_bf16_f32 v127, v60, v62\n\t"
"ds_write_b64 v4, v[126:127] offset:8704\n\t"
"v_smfmac_f32_16x16x64_bf16 v[68:71], v[16:19], a[192:199], v9\n\t"
"v_smfmac_f32_16x16x64_bf16 v[64:67], v[24:27], a[144:151], v9\n\t"
"v_smfmac_f32_16x16x64_bf16 v[68:71], v[24:27], a[208:215], v9\n\t"
"v_smfmac_f32_16x16x64_bf16 v[64:67], v[28:31], a[152:159], v9\n\t"
"s_waitcnt lgkmcnt(0)\n\t"
"s_barrier\n\t"
"ds_read_b64_tr_b16 v[48:49], v5 offset:8192\n\t"
"ds_read_b64_tr_b16 v[50:51], v5 offset:8704\n\t"
"ds_read_b64_tr_b16 v[52:53], v5 offset:9216\n\t"
"v_smfmac_f32_16x16x64_bf16 v[68:71], v[28:31], a[216:223], v9\n\t"
"ds_read_b64_tr_b16 v[54:55], v5 offset:9728\n\t"
"v_smfmac_f32_16x16x64_bf16 v[64:67], v[32:35], a[160:167], v9\n\t"
"ds_read_b64_tr_b16 v[56:57], v5 offset:10240\n\t"
"v_smfmac_f32_16x16x64_bf16 v[68:71], v[32:35], a[224:231], v9\n\t"
"ds_read_b64_tr_b16 v[58:59], v5 offset:10752\n\t"
"s_waitcnt lgkmcnt(4)\n\t"
"v_mfma_f32_16x16x32_bf16 v[112:115], v[72:75], v[48:51], 0\n\t"
"ds_read_b64_tr_b16 v[60:61], v5 offset:11264\n\t"
"v_mfma_f32_16x16x32_bf16 v[116:119], v[88:91], v[48:51], 0\n\t"
"ds_read_b64_tr_b16 v[62:63], v5 offset:11776\n\t"
"s_waitcnt lgkmcnt(4)\n\t"
"v_mfma_f32_16x16x32_bf16 v[112:115], v[76:79], v[52:55], v[112:115]\n\t"
"v_mov_b64 v[16:17], 0\n\t"
"v_mov_b64 v[18:19], 0\n\t"
"v_mfma_f32_16x16x32_bf16 v[116:119], v[92:95], v[52:55], v[116:119]\n\t"
"v_mov_b64 v[20:21], 0\n\t"
"s_waitcnt lgkmcnt(2)\n\t"
"v_mfma_f32_16x16x32_bf16 v[112:115], v[80:83], v[56:59], v[112:115]\n\t"
"v_mov_b64 v[22:23], 0\n\t"
"v_mov_b64 v[24:25], 0\n\t"
"v_mfma_f32_16x16x32_bf16 v[116:119], v[96:99], v[56:59], v[116:119]\n\t"
"v_mov_b64 v[26:27], 0\n\t"
"s_waitcnt lgkmcnt(0)\n\t"
"v_mfma_f32_16x16x32_bf16 v[112:115], v[84:87], v[60:63], v[112:115]\n\t"
"v_mov_b64 v[28:29], 0\n\t"
"v_mov_b64 v[30:31], 0\n\t"
"v_mfma_f32_16x16x32_bf16 v[116:119], v[100:103], v[60:63], v[116:119]\n\t"
"v_mov_b32 v81, 0\n\t"
"v_mov_b32 v82, 0\n\t"
"v_mfma_f32_16x16x32_bf16 v[120:123], v[48:51], v[48:51], 0\n\t"
"v_mov_b64 v[124:125], s[78:79]\n\t"
"v_mov_b64 v[126:127], s[78:79]\n\t"
"v_mfma_f32_16x16x32_bf16 v[120:123], v[52:55], v[52:55], v[120:123]\n\t"
"v_max3_f32 v72, v112, v113, v114\n\t"
"v_mfma_f32_16x16x32_bf16 v[120:123], v[56:59], v[56:59], v[120:123]\n\t"
"v_max3_f32 v72, v72, v115, v116\n\t"
"v_mfma_f32_16x16x32_bf16 v[120:123], v[60:63], v[60:63], v[120:123]\n\t"
"v_max3_f32 v72, v72, v117, v118\n\t"
"v_max_f32 v72, v72, v119\n\t"
"v_smfmac_f32_16x16x64_bf16 v[68:71], v[36:39], a[232:239], v9\n\t"
"s_nop 4\n\t"
"v_max3_f32 v72, v72, v122, v123\n\t"
"v_mov_b32 v73, v72\n\t"
"v_smfmac_f32_16x16x64_bf16 v[64:67], v[40:43], a[176:183], v9\n\t"
"s_nop 0\n\t"
"v_permlane32_swap_b32 v72, v73\n\t"
"v_max_f32 v72, v72, v73\n\t"
"v_mov_b32 v73, v72\n\t"
"v_smfmac_f32_16x16x64_bf16 v[68:71], v[40:43], a[240:247], v9\n\t"
"s_nop 0\n\t"
"v_permlane16_swap_b32 v72, v73\n\t"
"v_max_f32 v72, v72, v73\n\t"
"v_mul_f32 v74, s72, v72\n\t"
"v_pk_fma_f32 v[112:113], v[112:113], s[38:39], v[74:75] op_sel_hi:[1,1,0]\n\t"
"v_pk_fma_f32 v[114:115], v[114:115], s[38:39], v[74:75] op_sel_hi:[1,1,0]\n\t"
"v_pk_fma_f32 v[116:117], v[116:117], s[38:39], v[74:75] op_sel_hi:[1,1,0]\n\t"
"v_pk_fma_f32 v[118:119], v[118:119], s[38:39], v[74:75] op_sel_hi:[1,1,0]\n\t"
"v_pk_fma_f32 v[122:123], v[122:123], s[38:39], v[74:75] op_sel_hi:[1,1,0]\n\t"
"v_exp_f32 v112, v112\n\t"
"v_exp_f32 v113, v113\n\t"
"v_smfmac_f32_16x16x64_bf16 v[64:67], v[44:47], a[184:191], v9\n\t"
"v_exp_f32 v114, v114\n\t"
"v_exp_f32 v115, v115\n\t"
"v_exp_f32 v116, v116\n\t"
"v_smfmac_f32_16x16x64_bf16 v[68:71], v[44:47], a[248:255], v9\n\t"
"v_exp_f32 v117, v117\n\t"
"v_exp_f32 v118, v118\n\t"
"v_exp_f32 v119, v119\n\t"
"v_cvt_pk_bf16_f32 v76, v112, v113\n\t"
"v_cvt_pk_bf16_f32 v77, v114, v115\n\t"
"v_cvt_pk_bf16_f32 v78, v116, v117\n\t"
"v_cvt_pk_bf16_f32 v79, v118, v119\n\t"
"v_exp_f32 v122, v122\n\t"
"v_exp_f32 v123, v123\n\t"
"v_mfma_f32_16x16x32_bf16 v[72:75], v[124:127], v[76:79], 0\n\t"
"v_cvt_pk_bf16_f32 v80, v122, v123\n\t"
"v_cvt_pk_bf16_f32 v83, v122, v123\n\t"
"v_mfma_f32_16x16x32_bf16 v[48:51], v[104:107], v[76:79], 0\n\t"
"v_add_f32 v64, v64, v65\n\t"
"v_add_f32 v66, v66, v67\n\t"
"v_mfma_f32_16x16x16_bf16 v[72:75], v[124:125], v[80:81], v[72:75]\n\t"
"v_add_f32 v68, v68, v69\n\t"
"v_add_f32 v70, v70, v71\n\t"
"v_cvt_pk_bf16_f32 v84, v64, v66\n\t"
"v_cvt_pk_bf16_f32 v85, v68, v70\n\t"
"v_mfma_f32_16x16x32_bf16 v[52:55], v[108:111], v[76:79], 0\n\t"
"v_mov_b64 v[64:65], v[84:85]\n\t"
"v_mfma_f32_16x16x16_bf16 v[48:51], v[84:85], v[80:81], v[48:51]\n\t"
"v_mfma_f32_16x16x16_bf16 v[52:55], v[84:85], v[82:83], v[52:55]\n\t"
"v_rcp_f32 v124, v72\n\t"
"s_nop 5\n\t"
"v_pk_mul_f32 v[48:49], v[48:49], v[124:125] op_sel_hi:[1,0]\n\t"
"v_pk_mul_f32 v[50:51], v[50:51], v[124:125] op_sel_hi:[1,0]\n\t"
"v_pk_mul_f32 v[52:53], v[52:53], v[124:125] op_sel_hi:[1,0]\n\t"
"v_pk_mul_f32 v[54:55], v[54:55], v[124:125] op_sel_hi:[1,0]\n\t"
"v_cvt_pk_bf16_f32 v120, v48, v49\n\t"
"v_cvt_pk_bf16_f32 v121, v50, v51\n\t"
"v_cvt_pk_bf16_f32 v122, v52, v53\n\t"
"v_cvt_pk_bf16_f32 v123, v54, v55\n\t"
"ds_write_b128 v6, v[120:123] offset:12288\n\t"
"s_cmp_eq_u32 s64, 63\n\t"
"s_cbranch_scc1 CW1_fin8_%=\n\t"
"s_waitcnt lgkmcnt(0)\n\t"
"s_barrier\n\t"
"ds_read_b128 v[120:123], v0 offset:12288\n\t"
"ds_read_b128 v[124:127], v0 offset:13312\n\t"
"s_waitcnt lgkmcnt(1)\n\t"
"v_smfmac_f32_16x16x64_bf16 v[16:19], v[120:123], v[192:199], v9\n\t"
"v_smfmac_f32_16x16x64_bf16 v[20:23], v[120:123], v[200:207], v9\n\t"
"s_waitcnt lgkmcnt(0)\n\t"
"v_smfmac_f32_16x16x64_bf16 v[16:19], v[124:127], v[224:231], v9\n\t"
"s_nop 0\n\t"
"v_smfmac_f32_16x16x64_bf16 v[20:23], v[124:127], v[232:239], v9\n\t"
"s_nop 0\n\t"
"v_smfmac_f32_16x16x64_bf16 v[24:27], v[120:123], v[208:215], v9\n\t"
"s_nop 0\n\t"
"v_smfmac_f32_16x16x64_bf16 v[28:31], v[120:123], v[216:223], v9\n\t"
"s_nop 0\n\t"
"v_smfmac_f32_16x16x64_bf16 v[24:27], v[124:127], v[240:247], v9\n\t"
"v_add_f32 v16, v16, v17\n\t"
"v_smfmac_f32_16x16x64_bf16 v[28:31], v[124:127], v[248:255], v9\n\t"
"v_add_f32 v18, v18, v19\n\t"
"v_cvt_pk_bf16_f32 v68, v16, v18\n\t"
"v_add_f32 v20, v20, v21\n\t"
"v_add_f32 v22, v22, v23\n\t"
"v_cvt_pk_bf16_f32 v69, v20, v22\n\t"
"s_nop 0\n\t"
"v_add_f32 v24, v24, v25\n\t"
"v_add_f32 v26, v26, v27\n\t"
"v_cvt_pk_bf16_f32 v70, v24, v26\n\t"
"v_add_f32 v28, v28, v29\n\t"
"v_add_f32 v30, v30, v31\n\t"
"v_cvt_pk_bf16_f32 v71, v28, v30\n\t"
"buffer_store_dwordx4 v[68:71], v7, s[40:43], s65 offen sc1\n\t"
"s_memrealtime s[74:75]\n\t"
"s_waitcnt lgkmcnt(0)\n\t"
"s_mov_b32 s59, s74\n\t"
"ds_read_b64_tr_b16 v[68:69], v5 offset:9216\n\t"
"ds_read_b64_tr_b16 v[70:71], v5 offset:9728\n\t"
"s_and_b32 s74, s64, 3\n\t"
"s_lshl_b32 s74, s74, 16\n\t"
"s_add_u32 s69, s74, 1773568\n\t"
"s_add_u32 s70, s74, 1808384\n\t"
"buffer_load_dword v8, v10, s[40:43], s68 offen sc1\n\t"
"buffer_load_dwordx4 v0, s[44:47], s76 offen offset:0 sc1 lds\n\t"
"buffer_load_dwordx4 v0, s[44:47], s76 offen offset:1024 sc1 lds\n\t"
"buffer_load_dwordx4 v0, s[44:47], s76 offen offset:2048 sc1 lds\n\t"
"buffer_load_dwordx4 v0, s[44:47], s76 offen offset:3072 sc1 lds\n\t"
"ds_read_b128 v[56:59], v2 offset:16384\n\t"
"ds_read_b128 v[60:63], v2 offset:20480\n\t"
"ds_read_b128 v[72:75], v2 offset:24576\n\t"
"ds_read_b128 v[76:79], v2 offset:28672\n\t"
"ds_read_b128 v[80:83], v2 offset:0\n\t"
"ds_read_b128 v[84:87], v2 offset:4096\n\t"
"ds_read_b128 v[88:91], v2 offset:8192\n\t"
"ds_read_b128 v[92:95], v2 offset:12288\n\t"
"ds_read_b128 v[96:99], v2 offset:32768\n\t"
"ds_read_b128 v[100:103], v2 offset:36864\n\t"
"ds_read_b128 v[104:107], v2 offset:40960\n\t"
"ds_read_b128 v[108:111], v2 offset:45056\n\t"
"ds_read_b128 v[112:115], v2 offset:49152\n\t"
"ds_read_b128 v[116:119], v2 offset:53248\n\t"
"ds_read_b128 v[120:123], v2 offset:57344\n\t"
"ds_read_b128 v[124:127], v2 offset:61440\n\t"
"buffer_store_dwordx2 v[64:65], v12, s[52:55], s71 offen offset:2560 sc1\n\t"
"s_waitcnt lgkmcnt(15)\n\t"
"buffer_store_dwordx4 v[68:71], v11, s[52:55], s71 offen offset:512 sc1\n\t"
"v_mov_b64 v[48:49], 0\n\t"
"v_mov_b64 v[50:51], 0\n\t"
"v_mov_b64 v[52:53], 0\n\t"
"v_mov_b64 v[54:55], 0\n\t"
"v_mov_b64 v[64:65], 0\n\t"
"v_mov_b64 v[66:67], 0\n\t"
"v_mov_b64 v[68:69], 0\n\t"
"v_mov_b64 v[70:71], 0\n\t"
"s_add_u32 s64, s64, 1\n\t"
"s_add_u32 s65, s65, 524288\n\t"
"s_cmp_eq_u32 s65, 1572864\n\t"
"s_cselect_b32 s65, 0, s65\n\t"
"s_lshl_b32 s71, s64, 17\n\t"
"s_and_b32 s68, s64, 1\n\t"
"s_lshl_b32 s68, s68, 9\n\t"
"s_add_u32 s81, s81, 1048576\n\t"
"s_lshr_b32 s74, s81, 24\n\t"
"s_lshr_b32 s74, s80, s74\n\t"
"s_lshr_b32 s75, s81, 21\n\t"
"s_xor_b32 s74, s74, s75\n\t"
"s_and_b32 s74, s74, 1\n\t"
"s_lshl_b32 s74, s74, 13\n\t"
"s_or_b32 s76, s81, s74\n\t"
"s_add_u32 s61, s81, 1044480\n\t"
"s_lshr_b32 s74, s61, 24\n\t"
"s_lshr_b32 s74, s80, s74\n\t"
"s_lshr_b32 s75, s61, 21\n\t"
"s_xor_b32 s74, s74, s75\n\t"
"s_and_b32 s74, s74, 1\n\t"
"s_lshl_b32 s74, s74, 13\n\t"
"s_or_b32 s61, s61, s74\n\t"
"s_waitcnt vmcnt(6)\n\t"
"v_max_i32_dpp v8, v8, v8 row_shl:4 row_mask:0xf bank_mask:0xf\n\t"
"s_nop 0\n\t"
"v_readlane_b32 s74, v8, 0\n\t"
"v_readlane_b32 s75, v8, 1\n\t"
"v_readlane_b32 s50, v8, 2\n\t"
"v_readlane_b32 s51, v8, 3\n\t"
"s_max_i32 s74, s74, s75\n\t"
"s_max_i32 s50, s50, s51\n\t"
"s_max_i32 s74, s74, s50\n\t"
"s_add_u32 s58, s74, s48\n\t"
"s_sub_i32 s74, s58, s59\n\t"
"s_min_i32 s74, s74, 60\n\t"
"s_max_i32 s74, s74, -2\n\t"
"s_add_u32 s58, s59, s74\n\t"
"s_waitcnt lgkmcnt(0)\n\t"
"s_add_u32 s75, s58, %[dC]\n\t"
"CW1_sp12_%=:\n\t"
"s_memrealtime s[50:51]\n\t"
"s_waitcnt lgkmcnt(0)\n\t"
"s_sub_i32 s60, s75, s50\n\t"
"s_cmp_gt_i32 s60, 4\n\t"
"s_cbranch_scc1 CW1_sp12_%=\n\t"
"s_cmp_lt_i32 s60, 2\n\t"
"s_cbranch_scc1 CW1_sd13_%=\n\t"
"s_sub_u32 s60, s60, 2\n\t"
"CW1_fd14_%=:\n\t"
"s_sub_u32 s60, s60, 1\n\t"
"s_cbranch_scc0 CW1_fd14_%=\n\t"
"CW1_sd13_%=:\n\t"
"buffer_load_dwordx4 v[20:23], v0, s[40:43], s69 offen sc1\n\t"
"buffer_load_dwordx4 v[36:39], v0, s[40:43], s70 offen sc1\n\t"
"s_mov_b32 s60, 0\n\t"
"s_branch CW1_top7_%=\n\t"
"CW1_art9_%=:\n\t"
"v_cmp_eq_u32 vcc, s62, v15\n\t"
"s_cbranch_vccnz CW1_go15_%=\n\t"
"s_mov_b64 s[74:75], 0\n\t"
"v_cmp_eq_u32 vcc, s62, v20\n\t"
"s_nop 1\n\t"
"s_or_b64 s[74:75], s[74:75], vcc\n\t"
"v_cmp_eq_u32 vcc, s62, v21\n\t"
"s_nop 1\n\t"
"s_or_b64 s[74:75], s[74:75], vcc\n\t"
"v_cmp_eq_u32 vcc, s62, v22\n\t"
"s_nop 1\n\t"
"s_or_b64 s[74:75], s[74:75], vcc\n\t"
"v_cmp_eq_u32 vcc, s62, v23\n\t"
"s_nop 1\n\t"
"s_or_b64 s[74:75], s[74:75], vcc\n\t"
"v_cmp_eq_u32 vcc, s62, v36\n\t"
"s_nop 1\n\t"
"s_or_b64 s[74:75], s[74:75], vcc\n\t"
"v_cmp_eq_u32 vcc, s62, v37\n\t"
"s_nop 1\n\t"
"s_or_b64 s[74:75], s[74:75], vcc\n\t"
"v_cmp_eq_u32 vcc, s62, v38\n\t"
"s_nop 1\n\t"
"s_or_b64 s[74:75], s[74:75], vcc\n\t"
"v_cmp_eq_u32 vcc, s62, v39\n\t"
"s_nop 1\n\t"
"s_or_b64 s[74:75], s[74:75], vcc\n\t"
"s_cbranch_scc0 CW1_aok10_%=\n\t"
"CW1_go15_%=:\n\t"
"s_add_u32 s60, s60, 1\n\t"
"s_cmp_ge_u32 s60, 0x4000\n\t"
"s_cbranch_scc1 CW1_aok10_%=\n\t"
"s_waitcnt lgkmcnt(0)\n\t"
"buffer_load_dwordx4 v[20:23], v0, s[40:43], s69 offen sc1\n\t"
"buffer_load_dwordx4 v[36:39], v0, s[40:43], s70 offen sc1\n\t"
"v_mov_b64 v[48:49], 0\n\t"
"v_mov_b64 v[50:51], 0\n\t"
"v_mov_b64 v[52:53], 0\n\t"
"v_mov_b64 v[54:55], 0\n\t"
"v_mov_b64 v[64:65], 0\n\t"
"v_mov_b64 v[66:67], 0\n\t"
"v_mov_b64 v[68:69], 0\n\t"
"v_mov_b64 v[70:71], 0\n\t"
"s_branch CW1_top7_%=\n\t"
"CW1_fin8_%=:\n\t"
"s_waitcnt lgkmcnt(0)\n\t"
"s_nop 9\n\t"
"ds_read_b64_tr_b16 v[68:69], v5 offset:9216\n\t"
"ds_read_b64_tr_b16 v[70:71], v5 offset:9728\n\t"
"buffer_store_dwordx2 v[64:65], v12, s[52:55], s71 offen offset:2560 sc1\n\t"
"s_waitcnt lgkmcnt(0)\n\t"
"buffer_store_dwordx4 v[68:71], v11, s[52:55], s71 offen offset:512 sc1\n\t"
"s_waitcnt vmcnt(0)\n\t"
"s_barrier\n\t"
"s_branch CHAIN_END_%=\n\t"
"CHAIN_W2_%=:\n\t"
"v_mbcnt_lo_u32_b32 v127, -1, 0\n\t"
"v_mbcnt_hi_u32_b32 v127, -1, v127\n\t"
"v_lshlrev_b32 v127, 2, v127\n\t"
"v_add_u32 v127, 512, v127\n\t"
"ds_read_b32 v0, v127 offset:32768\n\t"
"ds_read_b32 v1, v127 offset:33792\n\t"
"ds_read_b32 v2, v127 offset:34816\n\t"
"ds_read_b32 v3, v127 offset:35840\n\t"
"ds_read_b32 v4, v127 offset:36864\n\t"
"ds_read_b32 v5, v127 offset:37888\n\t"
"ds_read_b32 v6, v127 offset:38912\n\t"
"ds_read_b32 v7, v127 offset:39936\n\t"
"ds_read_b32 v8, v127 offset:40960\n\t"
"ds_read_b32 v9, v127 offset:41984\n\t"
"ds_read_b32 v10, v127 offset:43008\n\t"
"ds_read_b32 v11, v127 offset:44032\n\t"
"ds_read_b32 v12, v127 offset:45056\n\t"
"ds_read_b32 v16, v127 offset:46080\n\t"
"ds_read_b32 v20, v127 offset:47104\n\t"
"s_waitcnt lgkmcnt(0)\n\t"
"ds_read_b32 v36, v127 offset:48128\n\t"
"ds_read_b32 v26, v127 offset:49152\n\t"
"ds_read_b32 v13, v127 offset:50176\n\t"
"s_waitcnt lgkmcnt(0)\n\t"
"s_barrier\n\t"
"v_xor_b32 v17, 64, v16\n\t"
"v_xor_b32 v21, 64, v20\n\t"
"v_xor_b32 v18, 128, v16\n\t"
"v_xor_b32 v22, 65536, v20\n\t"
"v_xor_b32 v19, 192, v16\n\t"
"v_xor_b32 v23, 65600, v20\n\t"
"v_xor_b32 v24, 128, v36\n\t"
"v_xor_b32 v25, 160, v36\n\t"
"v_xor_b32 v28, 0, v36\n\t"
"v_add_u32 v28, 32768, v28\n\t"
"v_xor_b32 v29, 32, v36\n\t"
"v_add_u32 v29, 32768, v29\n\t"
"v_xor_b32 v30, 64, v36\n\t"
"v_add_u32 v30, 32768, v30\n\t"
"v_xor_b32 v31, 96, v36\n\t"
"v_add_u32 v31, 32768, v31\n\t"
"v_xor_b32 v32, 128, v36\n\t"
"v_add_u32 v32, 32768, v32\n\t"
"v_xor_b32 v33, 160, v36\n\t"
"v_add_u32 v33, 32768, v33\n\t"
"v_xor_b32 v34, 192, v36\n\t"
"v_add_u32 v34, 32768, v34\n\t"
"v_xor_b32 v35, 224, v36\n\t"
"v_add_u32 v35, 32768, v35\n\t"
"s_mov_b32 s82, 0xff00ff00\n\t"
"s_mov_b32 s83, 0xff00ff00\n\t"
"s_mov_b32 s56, 0xffff\n\t"
"s_mov_b32 s57, 0\n\t"
"s_mov_b64 s[44:45], %[kv]\n\t"
"s_and_b32 s45, s45, 0xffff\n\t"
"s_mov_b32 s46, 67108864\n\t"
"s_mov_b32 s47, 0x20000\n\t"
"s_mov_b64 s[52:53], %[kmv]\n\t"
"s_and_b32 s53, s53, 0xffff\n\t"
"s_mov_b32 s54, %[kmsz]\n\t"
"s_mov_b32 s55, 0x20000\n\t"
"s_mov_b64 s[84:85], %[wq]\n\t"
"s_and_b32 s85, s85, 0xffff\n\t"
"s_mov_b32 s86, 4194304\n\t"
"s_mov_b32 s87, 0x20000\n\t"
"s_mov_b64 s[88:89], %[wk]\n\t"
"s_and_b32 s89, s89, 0xffff\n\t"
"s_mov_b32 s90, 4194304\n\t"
"s_mov_b32 s91, 0x20000\n\t"
"s_mov_b64 s[92:93], %[wv]\n\t"
"s_and_b32 s93, s93, 0xffff\n\t"
"s_mov_b32 s94, 4194304\n\t"
"s_mov_b32 s95, 0x20000\n\t"
"s_mov_b64 s[96:97], %[wo]\n\t"
"s_and_b32 s97, s97, 0xffff\n\t"
"s_mov_b32 s98, 130944\n\t"
"s_mov_b32 s99, 0x20000\n\t"
"s_mov_b64 s[60:61], %[mem0]\n\t"
"s_and_b32 s61, s61, 0xffff\n\t"
"s_mov_b32 s62, 8192\n\t"
"s_mov_b32 s63, 0x20000\n\t"
"buffer_load_dwordx2 v[48:49], v26, s[60:63], 0 offen offset:0\n\t"
"buffer_load_dwordx2 v[50:51], v26, s[60:63], 0 offen offset:32\n\t"
"buffer_load_dwordx2 v[52:53], v26, s[60:63], 0 offen offset:512\n\t"
"buffer_load_dwordx2 v[54:55], v26, s[60:63], 0 offen offset:544\n\t"
"v_mov_b32 v57, 0\n\t"
"global_load_dword v56, v57, %[ctl] offset:128 sc1\n\t"
"v_mov_b32 v60, 0xffbfffbf\n\t"
"v_mov_b32 v61, 0xffbfffbf\n\t"
"v_mov_b32 v62, 0xffbfffbf\n\t"
"v_mov_b32 v63, 0xffbfffbf\n\t"
"s_mov_b32 s74, 8126464\n\t"
"buffer_store_dwordx4 v[60:63], v1, s[52:55], s74 offen sc1\n\t"
"s_mov_b32 s74, 8257536\n\t"
"buffer_store_dwordx4 v[60:63], v1, s[52:55], s74 offen sc1\n\t"
"s_mov_b32 m0, 40960\n\t"
"s_mov_b32 s74, 2162688\n\t"
"buffer_load_dwordx4 v16, s[84:87], s74 offen lds\n\t"
"s_mov_b32 m0, 41984\n\t"
"s_mov_b32 s74, 2293760\n\t"
"buffer_load_dwordx4 v17, s[84:87], s74 offen lds\n\t"
"s_mov_b32 m0, 43008\n\t"
"s_mov_b32 s74, 2424832\n\t"
"buffer_load_dwordx4 v18, s[84:87], s74 offen lds\n\t"
"s_mov_b32 m0, 44032\n\t"
"s_mov_b32 s74, 2555904\n\t"
"buffer_load_dwordx4 v19, s[84:87], s74 offen lds\n\t"
"s_mov_b32 m0, 57344\n\t"
"s_mov_b32 s74, 2686976\n\t"
"buffer_load_dwordx4 v16, s[84:87], s74 offen lds\n\t"
"s_mov_b32 m0, 58368\n\t"
"s_mov_b32 s74, 2818048\n\t"
"buffer_load_dwordx4 v17, s[84:87], s74 offen lds\n\t"
"s_mov_b32 m0, 59392\n\t"
"s_mov_b32 s74, 2949120\n\t"
"buffer_load_dwordx4 v18, s[84:87], s74 offen lds\n\t"
"s_mov_b32 m0, 60416\n\t"
"s_mov_b32 s74, 3080192\n\t"
"buffer_load_dwordx4 v19, s[84:87], s74 offen lds\n\t"
"s_mov_b32 m0, 73728\n\t"
"s_mov_b32 s74, 3211264\n\t"
"buffer_load_dwordx4 v16, s[84:87], s74 offen lds\n\t"
"s_mov_b32 m0, 74752\n\t"
"s_mov_b32 s74, 3342336\n\t"
"buffer_load_dwordx4 v17, s[84:87], s74 offen lds\n\t"
"s_mov_b32 m0, 75776\n\t"
"s_mov_b32 s74, 3473408\n\t"
"buffer_load_dwordx4 v18, s[84:87], s74 offen lds\n\t"
"s_mov_b32 m0, 76800\n\t"
"s_mov_b32 s74, 3604480\n\t"
"buffer_load_dwordx4 v19, s[84:87], s74 offen lds\n\t"
"s_mov_b32 m0, 90112\n\t"
"s_mov_b32 s74, 3735552\n\t"
"buffer_load_dwordx4 v16, s[84:87], s74 offen lds\n\t"
"s_mov_b32 m0, 91136\n\t"
"s_mov_b32 s74, 3866624\n\t"
"buffer_load_dwordx4 v17, s[84:87], s74 offen lds\n\t"
"s_mov_b32 m0, 92160\n\t"
"s_mov_b32 s74, 3997696\n\t"
"buffer_load_dwordx4 v18, s[84:87], s74 offen lds\n\t"
"s_mov_b32 m0, 93184\n\t"
"s_mov_b32 s74, 4128768\n\t"
"buffer_load_dwordx4 v19, s[84:87], s74 offen lds\n\t"
"s_mov_b32 m0, 106496\n\t"
"s_mov_b32 s74, 2162688\n\t"
"buffer_load_dwordx4 v16, s[88:91], s74 offen lds\n\t"
"s_mov_b32 m0, 107520\n\t"
"s_mov_b32 s74, 2293760\n\t"
"buffer_load_dwordx4 v17, s[88:91], s74 offen lds\n\t"
"s_mov_b32 m0, 108544\n\t"
"s_mov_b32 s74, 2424832\n\t"
"buffer_load_dwordx4 v18, s[88:91], s74 offen lds\n\t"
"s_mov_b32 m0, 109568\n\t"
"s_mov_b32 s74, 2555904\n\t"
"buffer_load_dwordx4 v19, s[88:91], s74 offen lds\n\t"
"s_mov_b32 m0, 122880\n\t"
"s_mov_b32 s74, 2686976\n\t"
"buffer_load_dwordx4 v16, s[88:91], s74 offen lds\n\t"
"s_mov_b32 m0, 123904\n\t"
"s_mov_b32 s74, 2818048\n\t"
"buffer_load_dwordx4 v17, s[88:91], s74 offen lds\n\t"
"s_mov_b32 m0, 124928\n\t"
"s_mov_b32 s74, 2949120\n\t"
"buffer_load_dwordx4 v18, s[88:91], s74 offen lds\n\t"
"s_mov_b32 m0, 125952\n\t"
"s_mov_b32 s74, 3080192\n\t"
"buffer_load_dwordx4 v19, s[88:91], s74 offen lds\n\t"
"v_add_u32 v38, 32768, v24\n\t"
"v_add_u32 v39, 32768, v25\n\t"
"v_add_u32 v36, 98304, v24\n\t"
"v_add_u32 v37, 98304, v25\n\t"
"v_add_u32 v28, 32768, v28\n\t"
"v_add_u32 v29, 32768, v29\n\t"
"v_add_u32 v30, 32768, v30\n\t"
"v_add_u32 v31, 32768, v31\n\t"
"v_add_u32 v32, 32768, v32\n\t"
"v_add_u32 v33, 32768, v33\n\t"
"v_add_u32 v34, 32768, v34\n\t"
"v_add_u32 v35, 32768, v35\n\t"
"s_waitcnt vmcnt(16)\n\t"
"s_barrier\n\t"
"s_mov_b32 m0, 139264\n\t"
"s_mov_b32 s74, 3211264\n\t"
"buffer_load_dwordx4 v16, s[88:91], s74 offen lds\n\t"
"ds_read_b64_tr_b16 v[128:129], v38 offset:0\n\t"
"ds_read_b64_tr_b16 v[130:131], v38 offset:512\n\t"
"s_mov_b32 m0, 140288\n\t"
"s_mov_b32 s74, 3342336\n\t"
"buffer_load_dwordx4 v17, s[88:91], s74 offen lds\n\t"
"ds_read_b64_tr_b16 v[132:133], v38 offset:8192\n\t"
"ds_read_b64_tr_b16 v[134:135], v38 offset:8704\n\t"
"s_mov_b32 m0, 141312\n\t"
"s_mov_b32 s74, 3473408\n\t"
"buffer_load_dwordx4 v18, s[88:91], s74 offen lds\n\t"
"ds_read_b64_tr_b16 a[32:33], v39 offset:0\n\t"
"ds_read_b64_tr_b16 a[34:35], v39 offset:512\n\t"
"s_mov_b32 m0, 142336\n\t"
"s_mov_b32 s74, 3604480\n\t"
"buffer_load_dwordx4 v19, s[88:91], s74 offen lds\n\t"
"ds_read_b64_tr_b16 a[36:37], v39 offset:8192\n\t"
"ds_read_b64_tr_b16 a[38:39], v39 offset:8704\n\t"
"s_mov_b32 m0, 155648\n\t"
"s_mov_b32 s74, 3735552\n\t"
"buffer_load_dwordx4 v16, s[88:91], s74 offen lds\n\t"
"ds_read_b64_tr_b16 v[136:137], v38 offset:16384\n\t"
"ds_read_b64_tr_b16 v[138:139], v38 offset:16896\n\t"
"s_mov_b32 m0, 156672\n\t"
"s_mov_b32 s74, 3866624\n\t"
"buffer_load_dwordx4 v17, s[88:91], s74 offen lds\n\t"
"ds_read_b64_tr_b16 v[140:141], v38 offset:24576\n\t"
"ds_read_b64_tr_b16 v[142:143], v38 offset:25088\n\t"
"s_mov_b32 m0, 157696\n\t"
"s_mov_b32 s74, 3997696\n\t"
"buffer_load_dwordx4 v18, s[88:91], s74 offen lds\n\t"
"ds_read_b64_tr_b16 a[40:41], v39 offset:16384\n\t"
"ds_read_b64_tr_b16 a[42:43], v39 offset:16896\n\t"
"s_mov_b32 m0, 158720\n\t"
"s_mov_b32 s74, 4128768\n\t"
"buffer_load_dwordx4 v19, s[88:91], s74 offen lds\n\t"
"ds_read_b64_tr_b16 a[44:45], v39 offset:24576\n\t"
"ds_read_b64_tr_b16 a[46:47], v39 offset:25088\n\t"
"s_waitcnt lgkmcnt(0)\n\t"
"s_waitcnt vmcnt(16)\n\t"
"s_barrier\n\t"
"s_mov_b32 m0, 40960\n\t"
"s_mov_b32 s74, 65536\n\t"
"buffer_load_dwordx4 v16, s[92:95], s74 offen lds\n\t"
"ds_read_b64_tr_b16 v[144:145], v38 offset:32768\n\t"
"ds_read_b64_tr_b16 v[146:147], v38 offset:33280\n\t"
"s_mov_b32 m0, 41984\n\t"
"s_mov_b32 s74, 196608\n\t"
"buffer_load_dwordx4 v17, s[92:95], s74 offen lds\n\t"
"ds_read_b64_tr_b16 v[148:149], v38 offset:40960\n\t"
"ds_read_b64_tr_b16 v[150:151], v38 offset:41472\n\t"
"s_mov_b32 m0, 43008\n\t"
"s_mov_b32 s74, 327680\n\t"
"buffer_load_dwordx4 v18, s[92:95], s74 offen lds\n\t"
"ds_read_b64_tr_b16 a[48:49], v39 offset:32768\n\t"
"ds_read_b64_tr_b16 a[50:51], v39 offset:33280\n\t"
"s_mov_b32 m0, 44032\n\t"
"s_mov_b32 s74, 458752\n\t"
"buffer_load_dwordx4 v19, s[92:95], s74 offen lds\n\t"
"ds_read_b64_tr_b16 a[52:53], v39 offset:40960\n\t"
"ds_read_b64_tr_b16 a[54:55], v39 offset:41472\n\t"
"s_mov_b32 m0, 57344\n\t"
"s_mov_b32 s74, 589824\n\t"
"buffer_load_dwordx4 v16, s[92:95], s74 offen lds\n\t"
"ds_read_b64_tr_b16 v[152:153], v38 offset:49152\n\t"
"ds_read_b64_tr_b16 v[154:155], v38 offset:49664\n\t"
"s_mov_b32 m0, 58368\n\t"
"s_mov_b32 s74, 720896\n\t"
"buffer_load_dwordx4 v17, s[92:95], s74 offen lds\n\t"
"ds_read_b64_tr_b16 v[156:157], v38 offset:57344\n\t"
"ds_read_b64_tr_b16 v[158:159], v38 offset:57856\n\t"
"s_mov_b32 m0, 59392\n\t"
"s_mov_b32 s74, 851968\n\t"
"buffer_load_dwordx4 v18, s[92:95], s74 offen lds\n\t"
"ds_read_b64_tr_b16 a[56:57], v39 offset:49152\n\t"
"ds_read_b64_tr_b16 a[58:59], v39 offset:49664\n\t"
"s_mov_b32 m0, 60416\n\t"
"s_mov_b32 s74, 983040\n\t"
"buffer_load_dwordx4 v19, s[92:95], s74 offen lds\n\t"
"ds_read_b64_tr_b16 a[60:61], v39 offset:57344\n\t"
"ds_read_b64_tr_b16 a[62:63], v39 offset:57856\n\t"
"s_waitcnt lgkmcnt(0)\n\t"
"s_waitcnt vmcnt(16)\n\t"
"s_barrier\n\t"
"s_mov_b32 m0, 73728\n\t"
"s_mov_b32 s74, 1114112\n\t"
"buffer_load_dwordx4 v16, s[92:95], s74 offen lds\n\t"
"ds_read_b64_tr_b16 v[160:161], v36 offset:0\n\t"
"ds_read_b64_tr_b16 v[162:163], v36 offset:512\n\t"
"s_mov_b32 m0, 74752\n\t"
"s_mov_b32 s74, 1245184\n\t"
"buffer_load_dwordx4 v17, s[92:95], s74 offen lds\n\t"
"ds_read_b64_tr_b16 v[164:165], v36 offset:8192\n\t"
"ds_read_b64_tr_b16 v[166:167], v36 offset:8704\n\t"
"s_mov_b32 m0, 75776\n\t"
"s_mov_b32 s74, 1376256\n\t"
"buffer_load_dwordx4 v18, s[92:95], s74 offen lds\n\t"
"ds_read_b64_tr_b16 a[96:97], v37 offset:0\n\t"
"ds_read_b64_tr_b16 a[98:99], v37 offset:512\n\t"
"s_mov_b32 m0, 76800\n\t"
"s_mov_b32 s74, 1507328\n\t"
"buffer_load_dwordx4 v19, s[92:95], s74 offen lds\n\t"
"ds_read_b64_tr_b16 a[100:101], v37 offset:8192\n\t"
"ds_read_b64_tr_b16 a[102:103], v37 offset:8704\n\t"
"s_mov_b32 m0, 90112\n\t"
"s_mov_b32 s74, 1638400\n\t"
"buffer_load_dwordx4 v16, s[92:95], s74 offen lds\n\t"
"ds_read_b64_tr_b16 v[168:169], v36 offset:16384\n\t"
"ds_read_b64_tr_b16 v[170:171], v36 offset:16896\n\t"
"s_mov_b32 m0, 91136\n\t"
"s_mov_b32 s74, 1769472\n\t"
"buffer_load_dwordx4 v17, s[92:95], s74 offen lds\n\t"
"ds_read_b64_tr_b16 v[172:173], v36 offset:24576\n\t"
"ds_read_b64_tr_b16 v[174:175], v36 offset:25088\n\t"
"s_mov_b32 m0, 92160\n\t"
"s_mov_b32 s74, 1900544\n\t"
"buffer_load_dwordx4 v18, s[92:95], s74 offen lds\n\t"
"ds_read_b64_tr_b16 a[104:105], v37 offset:16384\n\t"
"ds_read_b64_tr_b16 a[106:107], v37 offset:16896\n\t"
"s_mov_b32 m0, 93184\n\t"
"s_mov_b32 s74, 2031616\n\t"
"buffer_load_dwordx4 v19, s[92:95], s74 offen lds\n\t"
"ds_read_b64_tr_b16 a[108:109], v37 offset:24576\n\t"
"ds_read_b64_tr_b16 a[110:111], v37 offset:25088\n\t"
"s_waitcnt lgkmcnt(0)\n\t"
"s_waitcnt vmcnt(16)\n\t"
"s_barrier\n\t"
"s_mov_b32 m0, 106496\n\t"
"s_mov_b32 s74, 2162688\n\t"
"buffer_load_dwordx4 v16, s[92:95], s74 offen lds\n\t"
"ds_read_b64_tr_b16 v[176:177], v36 offset:32768\n\t"
"ds_read_b64_tr_b16 v[178:179], v36 offset:33280\n\t"
"s_mov_b32 m0, 107520\n\t"
"s_mov_b32 s74, 2293760\n\t"
"buffer_load_dwordx4 v17, s[92:95], s74 offen lds\n\t"
"ds_read_b64_tr_b16 v[180:181], v36 offset:40960\n\t"
"ds_read_b64_tr_b16 v[182:183], v36 offset:41472\n\t"
"s_mov_b32 m0, 108544\n\t"
"s_mov_b32 s74, 2424832\n\t"
"buffer_load_dwordx4 v18, s[92:95], s74 offen lds\n\t"
"ds_read_b64_tr_b16 a[112:113], v37 offset:32768\n\t"
"ds_read_b64_tr_b16 a[114:115], v37 offset:33280\n\t"
"s_mov_b32 m0, 109568\n\t"
"s_mov_b32 s74, 2555904\n\t"
"buffer_load_dwordx4 v19, s[92:95], s74 offen lds\n\t"
"ds_read_b64_tr_b16 a[116:117], v37 offset:40960\n\t"
"ds_read_b64_tr_b16 a[118:119], v37 offset:41472\n\t"
"s_mov_b32 m0, 122880\n\t"
"s_mov_b32 s74, 2686976\n\t"
"buffer_load_dwordx4 v16, s[92:95], s74 offen lds\n\t"
"ds_read_b64_tr_b16 v[184:185], v36 offset:49152\n\t"
"ds_read_b64_tr_b16 v[186:187], v36 offset:49664\n\t"
"s_mov_b32 m0, 123904\n\t"
"s_mov_b32 s74, 2818048\n\t"
"buffer_load_dwordx4 v17, s[92:95], s74 offen lds\n\t"
"ds_read_b64_tr_b16 v[188:189], v36 offset:57344\n\t"
"ds_read_b64_tr_b16 v[190:191], v36 offset:57856\n\t"
"s_mov_b32 m0, 124928\n\t"
"s_mov_b32 s74, 2949120\n\t"
"buffer_load_dwordx4 v18, s[92:95], s74 offen lds\n\t"
"ds_read_b64_tr_b16 a[120:121], v37 offset:49152\n\t"
"ds_read_b64_tr_b16 a[122:123], v37 offset:49664\n\t"
"s_mov_b32 m0, 125952\n\t"
"s_mov_b32 s74, 3080192\n\t"
"buffer_load_dwordx4 v19, s[92:95], s74 offen lds\n\t"
"ds_read_b64_tr_b16 a[124:125], v37 offset:57344\n\t"
"ds_read_b64_tr_b16 a[126:127], v37 offset:57856\n\t"
"s_waitcnt lgkmcnt(0)\n\t"
"s_waitcnt vmcnt(16)\n\t"
"s_barrier\n\t"
"s_mov_b32 m0, 139264\n\t"
"s_mov_b32 s74, 3211264\n\t"
"buffer_load_dwordx4 v16, s[92:95], s74 offen lds\n\t"
"ds_read_b64_tr_b16 a[128:129], v38 offset:0\n\t"
"ds_read_b64_tr_b16 a[130:131], v38 offset:512\n\t"
"s_mov_b32 m0, 140288\n\t"
"s_mov_b32 s74, 3342336\n\t"
"buffer_load_dwordx4 v17, s[92:95], s74 offen lds\n\t"
"ds_read_b64_tr_b16 a[132:133], v38 offset:8192\n\t"
"ds_read_b64_tr_b16 a[134:135], v38 offset:8704\n\t"
"s_mov_b32 m0, 141312\n\t"
"s_mov_b32 s74, 3473408\n\t"
"buffer_load_dwordx4 v18, s[92:95], s74 offen lds\n\t"
"ds_read_b64_tr_b16 a[192:193], v39 offset:0\n\t"
"ds_read_b64_tr_b16 a[194:195], v39 offset:512\n\t"
"s_mov_b32 m0, 142336\n\t"
"s_mov_b32 s74, 3604480\n\t"
"buffer_load_dwordx4 v19, s[92:95], s74 offen lds\n\t"
"ds_read_b64_tr_b16 a[196:197], v39 offset:8192\n\t"
"ds_read_b64_tr_b16 a[198:199], v39 offset:8704\n\t"
"s_mov_b32 m0, 155648\n\t"
"s_mov_b32 s74, 3735552\n\t"
"buffer_load_dwordx4 v16, s[92:95], s74 offen lds\n\t"
"ds_read_b64_tr_b16 a[136:137], v38 offset:16384\n\t"
"ds_read_b64_tr_b16 a[138:139], v38 offset:16896\n\t"
"s_mov_b32 m0, 156672\n\t"
"s_mov_b32 s74, 3866624\n\t"
"buffer_load_dwordx4 v17, s[92:95], s74 offen lds\n\t"
"ds_read_b64_tr_b16 a[140:141], v38 offset:24576\n\t"
"ds_read_b64_tr_b16 a[142:143], v38 offset:25088\n\t"
"s_mov_b32 m0, 157696\n\t"
"s_mov_b32 s74, 3997696\n\t"
"buffer_load_dwordx4 v18, s[92:95], s74 offen lds\n\t"
"ds_read_b64_tr_b16 a[200:201], v39 offset:16384\n\t"
"ds_read_b64_tr_b16 a[202:203], v39 offset:16896\n\t"
"s_mov_b32 m0, 158720\n\t"
"s_mov_b32 s74, 4128768\n\t"
"buffer_load_dwordx4 v19, s[92:95], s74 offen lds\n\t"
"ds_read_b64_tr_b16 a[204:205], v39 offset:24576\n\t"
"ds_read_b64_tr_b16 a[206:207], v39 offset:25088\n\t"
"s_waitcnt lgkmcnt(0)\n\t"
"s_waitcnt vmcnt(16)\n\t"
"s_barrier\n\t"
"s_mov_b32 m0, 40960\n\t"
"s_mov_b32 s74, 32768\n\t"
"buffer_load_dwordx4 v20, s[96:99], s74 offen lds\n\t"
"ds_read_b64_tr_b16 a[144:145], v38 offset:32768\n\t"
"ds_read_b64_tr_b16 a[146:147], v38 offset:33280\n\t"
"s_mov_b32 m0, 41984\n\t"
"s_mov_b32 s74, 49152\n\t"
"buffer_load_dwordx4 v21, s[96:99], s74 offen lds\n\t"
"ds_read_b64_tr_b16 a[148:149], v38 offset:40960\n\t"
"ds_read_b64_tr_b16 a[150:151], v38 offset:41472\n\t"
"s_mov_b32 m0, 43008\n\t"
"s_mov_b32 s74, 36864\n\t"
"buffer_load_dwordx4 v22, s[96:99], s74 offen lds\n\t"
"ds_read_b64_tr_b16 a[208:209], v39 offset:32768\n\t"
"ds_read_b64_tr_b16 a[210:211], v39 offset:33280\n\t"
"s_mov_b32 m0, 44032\n\t"
"s_mov_b32 s74, 53248\n\t"
"buffer_load_dwordx4 v23, s[96:99], s74 offen lds\n\t"
"ds_read_b64_tr_b16 a[212:213], v39 offset:40960\n\t"
"ds_read_b64_tr_b16 a[214:215], v39 offset:41472\n\t"
"s_mov_b32 m0, 57344\n\t"
"s_mov_b32 s74, 33024\n\t"
"buffer_load_dwordx4 v20, s[96:99], s74 offen lds\n\t"
"ds_read_b64_tr_b16 a[152:153], v38 offset:49152\n\t"
"ds_read_b64_tr_b16 a[154:155], v38 offset:49664\n\t"
"s_mov_b32 m0, 58368\n\t"
"s_mov_b32 s74, 49408\n\t"
"buffer_load_dwordx4 v21, s[96:99], s74 offen lds\n\t"
"ds_read_b64_tr_b16 a[156:157], v38 offset:57344\n\t"
"ds_read_b64_tr_b16 a[158:159], v38 offset:57856\n\t"
"s_mov_b32 m0, 59392\n\t"
"s_mov_b32 s74, 37120\n\t"
"buffer_load_dwordx4 v22, s[96:99], s74 offen lds\n\t"
"ds_read_b64_tr_b16 a[216:217], v39 offset:49152\n\t"
"ds_read_b64_tr_b16 a[218:219], v39 offset:49664\n\t"
"s_mov_b32 m0, 60416\n\t"
"s_mov_b32 s74, 53504\n\t"
"buffer_load_dwordx4 v23, s[96:99], s74 offen lds\n\t"
"ds_read_b64_tr_b16 a[220:221], v39 offset:57344\n\t"
"ds_read_b64_tr_b16 a[222:223], v39 offset:57856\n\t"
"s_waitcnt lgkmcnt(0)\n\t"
"s_waitcnt vmcnt(16)\n\t"
"s_barrier\n\t"
"s_mov_b32 m0, 73728\n\t"
"s_mov_b32 s74, 33280\n\t"
"buffer_load_dwordx4 v20, s[96:99], s74 offen lds\n\t"
"ds_read_b64_tr_b16 a[160:161], v36 offset:0\n\t"
"ds_read_b64_tr_b16 a[162:163], v36 offset:512\n\t"
"s_mov_b32 m0, 74752\n\t"
"s_mov_b32 s74, 49664\n\t"
"buffer_load_dwordx4 v21, s[96:99], s74 offen lds\n\t"
"ds_read_b64_tr_b16 a[164:165], v36 offset:8192\n\t"
"ds_read_b64_tr_b16 a[166:167], v36 offset:8704\n\t"
"s_mov_b32 m0, 75776\n\t"
"s_mov_b32 s74, 37376\n\t"
"buffer_load_dwordx4 v22, s[96:99], s74 offen lds\n\t"
"ds_read_b64_tr_b16 a[224:225], v37 offset:0\n\t"
"ds_read_b64_tr_b16 a[226:227], v37 offset:512\n\t"
"s_mov_b32 m0, 76800\n\t"
"s_mov_b32 s74, 53760\n\t"
"buffer_load_dwordx4 v23, s[96:99], s74 offen lds\n\t"
"ds_read_b64_tr_b16 a[228:229], v37 offset:8192\n\t"
"ds_read_b64_tr_b16 a[230:231], v37 offset:8704\n\t"
"s_mov_b32 m0, 90112\n\t"
"s_mov_b32 s74, 33536\n\t"
"buffer_load_dwordx4 v20, s[96:99], s74 offen lds\n\t"
"ds_read_b64_tr_b16 a[168:169], v36 offset:16384\n\t"
"ds_read_b64_tr_b16 a[170:171], v36 offset:16896\n\t"
"s_mov_b32 m0, 91136\n\t"
"s_mov_b32 s74, 49920\n\t"
"buffer_load_dwordx4 v21, s[96:99], s74 offen lds\n\t"
"ds_read_b64_tr_b16 a[172:173], v36 offset:24576\n\t"
"ds_read_b64_tr_b16 a[174:175], v36 offset:25088\n\t"
"s_mov_b32 m0, 92160\n\t"
"s_mov_b32 s74, 37632\n\t"
"buffer_load_dwordx4 v22, s[96:99], s74 offen lds\n\t"
"ds_read_b64_tr_b16 a[232:233], v37 offset:16384\n\t"
"ds_read_b64_tr_b16 a[234:235], v37 offset:16896\n\t"
"s_mov_b32 m0, 93184\n\t"
"s_mov_b32 s74, 54016\n\t"
"buffer_load_dwordx4 v23, s[96:99], s74 offen lds\n\t"
"ds_read_b64_tr_b16 a[236:237], v37 offset:24576\n\t"
"ds_read_b64_tr_b16 a[238:239], v37 offset:25088\n\t"
"s_waitcnt lgkmcnt(0)\n\t"
"s_waitcnt vmcnt(16)\n\t"
"s_barrier\n\t"
"s_mov_b32 m0, 106496\n\t"
"s_mov_b32 s74, 65536\n\t"
"buffer_load_dwordx4 v16, s[84:87], s74 offen lds\n\t"
"ds_read_b64_tr_b16 a[176:177], v36 offset:32768\n\t"
"ds_read_b64_tr_b16 a[178:179], v36 offset:33280\n\t"
"s_mov_b32 m0, 107520\n\t"
"s_mov_b32 s74, 196608\n\t"
"buffer_load_dwordx4 v17, s[84:87], s74 offen lds\n\t"
"ds_read_b64_tr_b16 a[180:181], v36 offset:40960\n\t"
"ds_read_b64_tr_b16 a[182:183], v36 offset:41472\n\t"
"s_mov_b32 m0, 108544\n\t"
"s_mov_b32 s74, 327680\n\t"
"buffer_load_dwordx4 v18, s[84:87], s74 offen lds\n\t"
"ds_read_b64_tr_b16 a[240:241], v37 offset:32768\n\t"
"ds_read_b64_tr_b16 a[242:243], v37 offset:33280\n\t"
"s_mov_b32 m0, 109568\n\t"
"s_mov_b32 s74, 458752\n\t"
"buffer_load_dwordx4 v19, s[84:87], s74 offen lds\n\t"
"ds_read_b64_tr_b16 a[244:245], v37 offset:40960\n\t"
"ds_read_b64_tr_b16 a[246:247], v37 offset:41472\n\t"
"s_mov_b32 m0, 122880\n\t"
"s_mov_b32 s74, 589824\n\t"
"buffer_load_dwordx4 v16, s[84:87], s74 offen lds\n\t"
"ds_read_b64_tr_b16 a[184:185], v36 offset:49152\n\t"
"ds_read_b64_tr_b16 a[186:187], v36 offset:49664\n\t"
"s_mov_b32 m0, 123904\n\t"
"s_mov_b32 s74, 720896\n\t"
"buffer_load_dwordx4 v17, s[84:87], s74 offen lds\n\t"
"ds_read_b64_tr_b16 a[188:189], v36 offset:57344\n\t"
"ds_read_b64_tr_b16 a[190:191], v36 offset:57856\n\t"
"s_mov_b32 m0, 124928\n\t"
"s_mov_b32 s74, 851968\n\t"
"buffer_load_dwordx4 v18, s[84:87], s74 offen lds\n\t"
"ds_read_b64_tr_b16 a[248:249], v37 offset:49152\n\t"
"ds_read_b64_tr_b16 a[250:251], v37 offset:49664\n\t"
"s_mov_b32 m0, 125952\n\t"
"s_mov_b32 s74, 983040\n\t"
"buffer_load_dwordx4 v19, s[84:87], s74 offen lds\n\t"
"ds_read_b64_tr_b16 a[252:253], v37 offset:57344\n\t"
"ds_read_b64_tr_b16 a[254:255], v37 offset:57856\n\t"
"s_waitcnt lgkmcnt(0)\n\t"
"s_waitcnt vmcnt(16)\n\t"
"s_barrier\n\t"
"s_mov_b32 m0, 139264\n\t"
"s_mov_b32 s74, 1114112\n\t"
"buffer_load_dwordx4 v16, s[84:87], s74 offen lds\n\t"
"s_mov_b32 m0, 140288\n\t"
"s_mov_b32 s74, 1245184\n\t"
"buffer_load_dwordx4 v17, s[84:87], s74 offen lds\n\t"
"s_mov_b32 m0, 141312\n\t"
"s_mov_b32 s74, 1376256\n\t"
"buffer_load_dwordx4 v18, s[84:87], s74 offen lds\n\t"
"s_mov_b32 m0, 142336\n\t"
"s_mov_b32 s74, 1507328\n\t"
"buffer_load_dwordx4 v19, s[84:87], s74 offen lds\n\t"
"s_mov_b32 m0, 155648\n\t"
"s_mov_b32 s74, 1638400\n\t"
"buffer_load_dwordx4 v16, s[84:87], s74 offen lds\n\t"
"s_mov_b32 m0, 156672\n\t"
"s_mov_b32 s74, 1769472\n\t"
"buffer_load_dwordx4 v17, s[84:87], s74 offen lds\n\t"
"s_mov_b32 m0, 157696\n\t"
"s_mov_b32 s74, 1900544\n\t"
"buffer_load_dwordx4 v18, s[84:87], s74 offen lds\n\t"
"s_mov_b32 m0, 158720\n\t"
"s_mov_b32 s74, 2031616\n\t"
"buffer_load_dwordx4 v19, s[84:87], s74 offen lds\n\t"
"s_waitcnt vmcnt(16)\n\t"
"s_barrier\n\t"
"s_mov_b32 m0, 40960\n\t"
"s_mov_b32 s74, 65536\n\t"
"buffer_load_dwordx4 v16, s[88:91], s74 offen lds\n\t"
"ds_read_b64_tr_b16 v[192:193], v28 offset:0\n\t"
"ds_read_b64_tr_b16 v[194:195], v28 offset:512\n\t"
"ds_read_b64_tr_b16 v[196:197], v28 offset:8192\n\t"
"ds_read_b64_tr_b16 v[198:199], v28 offset:8704\n\t"
"s_mov_b32 m0, 41984\n\t"
"s_mov_b32 s74, 196608\n\t"
"buffer_load_dwordx4 v17, s[88:91], s74 offen lds\n\t"
"ds_read_b64_tr_b16 v[200:201], v29 offset:0\n\t"
"ds_read_b64_tr_b16 v[202:203], v29 offset:512\n\t"
"ds_read_b64_tr_b16 v[204:205], v29 offset:8192\n\t"
"ds_read_b64_tr_b16 v[206:207], v29 offset:8704\n\t"
"s_mov_b32 m0, 43008\n\t"
"s_mov_b32 s74, 327680\n\t"
"buffer_load_dwordx4 v18, s[88:91], s74 offen lds\n\t"
"ds_read_b64_tr_b16 v[208:209], v30 offset:0\n\t"
"ds_read_b64_tr_b16 v[210:211], v30 offset:512\n\t"
"ds_read_b64_tr_b16 v[212:213], v30 offset:8192\n\t"
"ds_read_b64_tr_b16 v[214:215], v30 offset:8704\n\t"
"s_mov_b32 m0, 44032\n\t"
"s_mov_b32 s74, 458752\n\t"
"buffer_load_dwordx4 v19, s[88:91], s74 offen lds\n\t"
"ds_read_b64_tr_b16 v[216:217], v31 offset:0\n\t"
"ds_read_b64_tr_b16 v[218:219], v31 offset:512\n\t"
"ds_read_b64_tr_b16 v[220:221], v31 offset:8192\n\t"
"ds_read_b64_tr_b16 v[222:223], v31 offset:8704\n\t"
"s_mov_b32 m0, 57344\n\t"
"s_mov_b32 s74, 589824\n\t"
"buffer_load_dwordx4 v16, s[88:91], s74 offen lds\n\t"
"ds_read_b64_tr_b16 v[224:225], v32 offset:0\n\t"
"ds_read_b64_tr_b16 v[226:227], v32 offset:512\n\t"
"ds_read_b64_tr_b16 v[228:229], v32 offset:8192\n\t"
"ds_read_b64_tr_b16 v[230:231], v32 offset:8704\n\t"
"s_mov_b32 m0, 58368\n\t"
"s_mov_b32 s74, 720896\n\t"
"buffer_load_dwordx4 v17, s[88:91], s74 offen lds\n\t"
"ds_read_b64_tr_b16 v[232:233], v33 offset:0\n\t"
"ds_read_b64_tr_b16 v[234:235], v33 offset:512\n\t"
"ds_read_b64_tr_b16 v[236:237], v33 offset:8192\n\t"
"ds_read_b64_tr_b16 v[238:239], v33 offset:8704\n\t"
"s_mov_b32 m0, 59392\n\t"
"s_mov_b32 s74, 851968\n\t"
"buffer_load_dwordx4 v18, s[88:91], s74 offen lds\n\t"
"ds_read_b64_tr_b16 v[240:241], v34 offset:0\n\t"
"ds_read_b64_tr_b16 v[242:243], v34 offset:512\n\t"
"ds_read_b64_tr_b16 v[244:245], v34 offset:8192\n\t"
"ds_read_b64_tr_b16 v[246:247], v34 offset:8704\n\t"
"s_mov_b32 m0, 60416\n\t"
"s_mov_b32 s74, 983040\n\t"
"buffer_load_dwordx4 v19, s[88:91], s74 offen lds\n\t"
"ds_read_b64_tr_b16 v[248:249], v35 offset:0\n\t"
"ds_read_b64_tr_b16 v[250:251], v35 offset:512\n\t"
"ds_read_b64_tr_b16 v[252:253], v35 offset:8192\n\t"
"ds_read_b64_tr_b16 v[254:255], v35 offset:8704\n\t"
"global_load_dwordx2 v[58:59], v57, %[ctl] sc1\n\t"
"s_waitcnt lgkmcnt(0)\n\t"
"s_waitcnt vmcnt(16)\n\t"
"s_barrier\n\t"
"s_mov_b32 m0, 73728\n\t"
"s_mov_b32 s74, 1114112\n\t"
"buffer_load_dwordx4 v16, s[88:91], s74 offen lds\n\t"
"ds_read_b64_tr_b16 v[64:65], v36 offset:0\n\t"
"ds_read_b64_tr_b16 v[66:67], v36 offset:512\n\t"
"s_mov_b32 m0, 74752\n\t"
"s_mov_b32 s74, 1245184\n\t"
"buffer_load_dwordx4 v17, s[88:91], s74 offen lds\n\t"
"ds_read_b64_tr_b16 v[68:69], v36 offset:8192\n\t"
"ds_read_b64_tr_b16 v[70:71], v36 offset:8704\n\t"
"s_mov_b32 m0, 75776\n\t"
"s_mov_b32 s74, 1376256\n\t"
"buffer_load_dwordx4 v18, s[88:91], s74 offen lds\n\t"
"ds_read_b64_tr_b16 a[0:1], v37 offset:0\n\t"
"ds_read_b64_tr_b16 a[2:3], v37 offset:512\n\t"
"s_mov_b32 m0, 76800\n\t"
"s_mov_b32 s74, 1507328\n\t"
"buffer_load_dwordx4 v19, s[88:91], s74 offen lds\n\t"
"ds_read_b64_tr_b16 a[4:5], v37 offset:8192\n\t"
"ds_read_b64_tr_b16 a[6:7], v37 offset:8704\n\t"
"s_mov_b32 m0, 90112\n\t"
"s_mov_b32 s74, 1638400\n\t"
"buffer_load_dwordx4 v16, s[88:91], s74 offen lds\n\t"
"ds_read_b64_tr_b16 v[80:81], v36 offset:16384\n\t"
"ds_read_b64_tr_b16 v[82:83], v36 offset:16896\n\t"
"s_mov_b32 m0, 91136\n\t"
"s_mov_b32 s74, 1769472\n\t"
"buffer_load_dwordx4 v17, s[88:91], s74 offen lds\n\t"
"ds_read_b64_tr_b16 v[84:85], v36 offset:24576\n\t"
"ds_read_b64_tr_b16 v[86:87], v36 offset:25088\n\t"
"s_mov_b32 m0, 92160\n\t"
"s_mov_b32 s74, 1900544\n\t"
"buffer_load_dwordx4 v18, s[88:91], s74 offen lds\n\t"
"ds_read_b64_tr_b16 a[8:9], v37 offset:16384\n\t"
"ds_read_b64_tr_b16 a[10:11], v37 offset:16896\n\t"
"s_mov_b32 m0, 93184\n\t"
"s_mov_b32 s74, 2031616\n\t"
"buffer_load_dwordx4 v19, s[88:91], s74 offen lds\n\t"
"ds_read_b64_tr_b16 a[12:13], v37 offset:24576\n\t"
"ds_read_b64_tr_b16 a[14:15], v37 offset:25088\n\t"
"s_waitcnt lgkmcnt(0)\n\t"
"s_waitcnt vmcnt(16)\n\t"
"s_barrier\n\t"
"ds_read_b64_tr_b16 v[96:97], v36 offset:32768\n\t"
"ds_read_b64_tr_b16 v[98:99], v36 offset:33280\n\t"
"ds_read_b64_tr_b16 v[100:101], v36 offset:40960\n\t"
"ds_read_b64_tr_b16 v[102:103], v36 offset:41472\n\t"
"ds_read_b64_tr_b16 a[16:17], v37 offset:32768\n\t"
"ds_read_b64_tr_b16 a[18:19], v37 offset:33280\n\t"
"ds_read_b64_tr_b16 a[20:21], v37 offset:40960\n\t"
"ds_read_b64_tr_b16 a[22:23], v37 offset:41472\n\t"
"ds_read_b64_tr_b16 v[112:113], v36 offset:49152\n\t"
"ds_read_b64_tr_b16 v[114:115], v36 offset:49664\n\t"
"ds_read_b64_tr_b16 v[116:117], v36 offset:57344\n\t"
"ds_read_b64_tr_b16 v[118:119], v36 offset:57856\n\t"
"ds_read_b64_tr_b16 a[24:25], v37 offset:49152\n\t"
"ds_read_b64_tr_b16 a[26:27], v37 offset:49664\n\t"
"ds_read_b64_tr_b16 a[28:29], v37 offset:57344\n\t"
"ds_read_b64_tr_b16 a[30:31], v37 offset:57856\n\t"
"s_waitcnt lgkmcnt(0)\n\t"
"s_waitcnt vmcnt(8)\n\t"
"v_readfirstlane_b32 s76, v56\n\t"
"s_nop 3\n\t"
"s_add_u32 s76, s76, 1\n\t"
"s_mov_b32 s60, 0\n\t"
"v_mov_b32 v56, 0\n\t"
"s_branch CW2_polc3_%=\n\t"
"CW2_pol1_%=:\n\t"
"global_load_dwordx2 v[58:59], v56, %[ctl] sc1\n\t"
"s_waitcnt vmcnt(0)\n\t"
"CW2_polc3_%=:\n\t"
"v_readfirstlane_b32 s74, v58\n\t"
"v_readfirstlane_b32 s75, v59\n\t"
"s_nop 3\n\t"
"s_cmp_eq_u32 s75, s76\n\t"
"s_cbranch_scc1 CW2_pold2_%=\n\t"
"s_sleep 2\n\t"
"s_add_u32 s60, s60, 1\n\t"
"s_cmp_lt_u32 s60, 0x10000\n\t"
"s_cbranch_scc1 CW2_pol1_%=\n\t"
"CW2_pold2_%=:\n\t"
"s_andn2_b32 s48, s74, 0xfff\n\t"
"s_lshr_b32 s80, s74, 1\n\t"
"s_and_b32 s74, s74, 1\n\t"
"s_lshl_b32 s74, s74, 13\n\t"
"s_mov_b64 s[40:41], %[comm]\n\t"
"s_add_u32 s40, s40, s74\n\t"
"s_addc_u32 s41, s41, 0\n\t"
"s_and_b32 s41, s41, 0xffff\n\t"
"s_mov_b32 s42, 0x1fe000\n\t"
"s_mov_b32 s43, 0x20000\n\t"
"s_add_u32 s75, %[me7], 4096\n\t"
"v_mov_b32 v56, s75\n\t"
"buffer_load_dword v8, v10, s[40:43], 0 offen sc1\n\t"
"s_lshl_b32 s81, %[me14], 1\n\t"
"s_add_u32 s81, s81, 16384\n\t"
"s_lshr_b32 s74, s81, 24\n\t"
"s_lshr_b32 s74, s80, s74\n\t"
"s_lshr_b32 s75, s81, 21\n\t"
"s_xor_b32 s74, s74, s75\n\t"
"s_and_b32 s74, s74, 1\n\t"
"s_lshl_b32 s74, s74, 13\n\t"
"s_or_b32 s76, s81, s74\n\t"
"s_add_u32 s61, s81, 1048576\n\t"
"s_lshr_b32 s74, s61, 24\n\t"
"s_lshr_b32 s74, s80, s74\n\t"
"s_lshr_b32 s75, s61, 21\n\t"
"s_xor_b32 s74, s74, s75\n\t"
"s_and_b32 s74, s74, 1\n\t"
"s_lshl_b32 s74, s74, 13\n\t"
"s_or_b32 s61, s61, s74\n\t"
"s_barrier\n\t"
"ds_read_b64_tr_b16 v[72:73], v38 offset:0\n\t"
"ds_read_b64_tr_b16 v[74:75], v38 offset:512\n\t"
"ds_read_b64_tr_b16 v[76:77], v38 offset:8192\n\t"
"ds_read_b64_tr_b16 v[78:79], v38 offset:8704\n\t"
"ds_read_b64_tr_b16 a[64:65], v39 offset:0\n\t"
"ds_read_b64_tr_b16 a[66:67], v39 offset:512\n\t"
"ds_read_b64_tr_b16 a[68:69], v39 offset:8192\n\t"
"ds_read_b64_tr_b16 a[70:71], v39 offset:8704\n\t"
"ds_read_b64_tr_b16 v[88:89], v38 offset:16384\n\t"
"ds_read_b64_tr_b16 v[90:91], v38 offset:16896\n\t"
"ds_read_b64_tr_b16 v[92:93], v38 offset:24576\n\t"
"ds_read_b64_tr_b16 v[94:95], v38 offset:25088\n\t"
"ds_read_b64_tr_b16 a[72:73], v39 offset:16384\n\t"
"ds_read_b64_tr_b16 a[74:75], v39 offset:16896\n\t"
"ds_read_b64_tr_b16 a[76:77], v39 offset:24576\n\t"
"ds_read_b64_tr_b16 a[78:79], v39 offset:25088\n\t"
"s_waitcnt lgkmcnt(0)\n\t"
"s_waitcnt vmcnt(0)\n\t"
"global_load_dword v57, v56, %[ctl] sc1\n\t"
"s_barrier\n\t"
"ds_read_b64_tr_b16 v[104:105], v38 offset:32768\n\t"
"ds_read_b64_tr_b16 v[106:107], v38 offset:33280\n\t"
"ds_read_b64_tr_b16 v[108:109], v38 offset:40960\n\t"
"ds_read_b64_tr_b16 v[110:111], v38 offset:41472\n\t"
"ds_read_b64_tr_b16 a[80:81], v39 offset:32768\n\t"
"ds_read_b64_tr_b16 a[82:83], v39 offset:33280\n\t"
"ds_read_b64_tr_b16 a[84:85], v39 offset:40960\n\t"
"ds_read_b64_tr_b16 a[86:87], v39 offset:41472\n\t"
"ds_read_b64_tr_b16 v[120:121], v38 offset:49152\n\t"
"ds_read_b64_tr_b16 v[122:123], v38 offset:49664\n\t"
"ds_read_b64_tr_b16 v[124:125], v38 offset:57344\n\t"
"ds_read_b64_tr_b16 v[126:127], v38 offset:57856\n\t"
"ds_read_b64_tr_b16 a[88:89], v39 offset:49152\n\t"
"ds_read_b64_tr_b16 a[90:91], v39 offset:49664\n\t"
"ds_read_b64_tr_b16 a[92:93], v39 offset:57344\n\t"
"ds_read_b64_tr_b16 a[94:95], v39 offset:57856\n\t"
"s_waitcnt lgkmcnt(0)\n\t"
"s_waitcnt vmcnt(0)\n\t"
"s_mov_b32 s60, 0\n\t"
"s_branch CW2_kc6_%=\n\t"
"CW2_kw4_%=:\n\t"
"global_load_dword v57, v56, %[ctl] sc1\n\t"
"s_waitcnt vmcnt(0)\n\t"
"CW2_kc6_%=:\n\t"
"v_readfirstlane_b32 s75, v57\n\t"
"s_nop 3\n\t"
"s_lshr_b32 s75, s75, 24\n\t"
"s_and_b32 s75, s75, 0xff\n\t"
"s_cmp_ge_u32 s75, 16\n\t"
"s_cbranch_scc1 CW2_kd5_%=\n\t"
"s_sleep 2\n\t"
"s_add_u32 s60, s60, 1\n\t"
"s_cmp_lt_u32 s60, 0x8000\n\t"
"s_cbranch_scc1 CW2_kw4_%=\n\t"
"CW2_kd5_%=:\n\t"
"s_barrier\n\t"
"s_mov_b32 m0, 24576\n\t"
"s_nop 0\n\t"
"buffer_load_dwordx4 v0, s[44:47], s76 offen offset:0 sc1 lds\n\t"
"buffer_load_dwordx4 v0, s[44:47], s76 offen offset:1024 sc1 lds\n\t"
"buffer_load_dwordx4 v0, s[44:47], s76 offen offset:2048 sc1 lds\n\t"
"buffer_load_dwordx4 v0, s[44:47], s76 offen offset:3072 sc1 lds\n\t"
"buffer_load_dword v14, v13, s[44:47], s61 offen sc1\n\t"
"ds_write_b128 v2, v[64:67] offset:0\n\t"
"ds_write_b128 v2, v[68:71] offset:4096\n\t"
"ds_write_b128 v2, v[72:75] offset:8192\n\t"
"ds_write_b128 v2, v[76:79] offset:12288\n\t"
"ds_write_b128 v2, v[80:83] offset:16384\n\t"
"ds_write_b128 v2, v[84:87] offset:20480\n\t"
"ds_write_b128 v2, v[88:91] offset:24576\n\t"
"ds_write_b128 v2, v[92:95] offset:28672\n\t"
"ds_write_b128 v2, v[96:99] offset:32768\n\t"
"ds_write_b128 v2, v[100:103] offset:36864\n\t"
"ds_write_b128 v2, v[104:107] offset:40960\n\t"
"ds_write_b128 v2, v[108:111] offset:45056\n\t"
"ds_write_b128 v2, v[112:115] offset:49152\n\t"
"ds_write_b128 v2, v[116:119] offset:53248\n\t"
"ds_write_b128 v2, v[120:123] offset:57344\n\t"
"ds_write_b128 v2, v[124:127] offset:61440\n\t"
"s_waitcnt lgkmcnt(0)\n\t"
"v_mov_b64 v[24:25], v[48:49]\n\t"
"v_mov_b64 v[26:27], v[50:51]\n\t"
"v_mov_b64 v[40:41], v[52:53]\n\t"
"v_mov_b64 v[42:43], v[54:55]\n\t"
"s_mov_b32 s62, 0xffbfffbf\n\t"
"s_mov_b32 s63, 0xffbfffbf\n\t"
"s_mov_b32 s78, 0x3f803f80\n\t"
"s_mov_b32 s79, 0x3f803f80\n\t"
"s_mov_b32 s38, 0x3e0293ee\n\t"
"s_mov_b32 s39, 0x3e0293ee\n\t"
"s_mov_b32 s72, 0xbe0293ee\n\t"
"s_sub_u32 s77, 100, s48\n\t"
"s_mov_b32 s64, 0\n\t"
"ds_read_b128 v[56:59], v2 offset:32768\n\t"
"ds_read_b128 v[60:63], v2 offset:36864\n\t"
"ds_read_b128 v[72:75], v2 offset:40960\n\t"
"ds_read_b128 v[76:79], v2 offset:45056\n\t"
"ds_read_b128 v[80:83], v2 offset:0\n\t"
"ds_read_b128 v[84:87], v2 offset:4096\n\t"
"ds_read_b128 v[88:91], v2 offset:8192\n\t"
"ds_read_b128 v[92:95], v2 offset:12288\n\t"
"ds_read_b128 v[96:99], v2 offset:16384\n\t"
"ds_read_b128 v[100:103], v2 offset:20480\n\t"
"ds_read_b128 v[104:107], v2 offset:24576\n\t"
"ds_read_b128 v[108:111], v2 offset:28672\n\t"
"ds_read_b128 v[112:115], v2 offset:49152\n\t"
"ds_read_b128 v[116:119], v2 offset:53248\n\t"
"ds_read_b128 v[120:123], v2 offset:57344\n\t"
"ds_read_b128 v[124:127], v2 offset:61440\n\t"
"v_mov_b64 v[48:49], 0\n\t"
"v_mov_b64 v[50:51], 0\n\t"
"v_mov_b64 v[52:53], 0\n\t"
"v_mov_b64 v[54:55], 0\n\t"
"v_mov_b64 v[64:65], 0\n\t"
"v_mov_b64 v[66:67], 0\n\t"
"v_mov_b64 v[68:69], 0\n\t"
"v_mov_b64 v[70:71], 0\n\t"
"s_mov_b32 s65, 0\n\t"
"s_mov_b32 s66, 112\n\t"
"s_mov_b32 s67, s48\n\t"
"s_add_u32 s65, s65, 524288\n\t"
"s_cmp_eq_u32 s65, 1572864\n\t"
"s_cselect_b32 s65, 0, s65\n\t"
"s_lshl_b32 s71, s64, 17\n\t"
"s_and_b32 s68, s64, 1\n\t"
"s_lshl_b32 s68, s68, 9\n\t"
"s_add_u32 s81, s81, 1048576\n\t"
"s_lshr_b32 s74, s81, 24\n\t"
"s_lshr_b32 s74, s80, s74\n\t"
"s_lshr_b32 s75, s81, 21\n\t"
"s_xor_b32 s74, s74, s75\n\t"
"s_and_b32 s74, s74, 1\n\t"
"s_lshl_b32 s74, s74, 13\n\t"
"s_or_b32 s76, s81, s74\n\t"
"s_add_u32 s61, s81, 1048576\n\t"
"s_lshr_b32 s74, s61, 24\n\t"
"s_lshr_b32 s74, s80, s74\n\t"
"s_lshr_b32 s75, s61, 21\n\t"
"s_xor_b32 s74, s74, s75\n\t"
"s_and_b32 s74, s74, 1\n\t"
"s_lshl_b32 s74, s74, 13\n\t"
"s_or_b32 s61, s61, s74\n\t"
"s_waitcnt lgkmcnt(0)\n\t"
"s_waitcnt vmcnt(0)\n\t"
"s_mov_b32 s60, 0\n\t"
"CW2_top7_%=:\n\t"
"s_waitcnt vmcnt(1)\n\t"
"CW2_top211_%=:\n\t"
"ds_write_b128 v1, v[24:27] offset:0\n\t"
"v_smfmac_f32_16x16x64_bf16 v[48:51], v[24:27], v[56:63], v9\n\t"
"v_max3_u32 v15, v24, v25, v26\n\t"
"v_smfmac_f32_16x16x64_bf16 v[52:55], v[24:27], v[72:79], v9\n\t"
"v_smfmac_f32_16x16x64_bf16 v[64:67], v[24:27], a[144:151], v9\n\t"
"v_smfmac_f32_16x16x64_bf16 v[68:71], v[24:27], a[208:215], v9\n\t"
"s_waitcnt vmcnt(0)\n\t"
"ds_write_b128 v1, v[40:43] offset:4096\n\t"
"v_max3_u32 v15, v15, v27, v40\n\t"
"v_max3_u32 v15, v15, v41, v42\n\t"
"v_max_u32 v15, v15, v43\n\t"
"v_cmp_le_u32 vcc, s62, v15\n\t"
"s_cbranch_vccnz CW2_art9_%=\n\t"
"CW2_aok10_%=:\n\t"
"v_smfmac_f32_16x16x64_bf16 v[48:51], v[40:43], v[144:151], v9\n\t"
"v_mov_b64 v[56:57], 0\n\t"
"v_smfmac_f32_16x16x64_bf16 v[52:55], v[40:43], v[176:183], v9\n\t"
"v_mov_b64 v[58:59], 0\n\t"
"v_mov_b64 v[60:61], 0\n\t"
"v_mov_b64 v[62:63], 0\n\t"
"s_waitcnt lgkmcnt(0)\n\t"
"s_barrier\n\t"
"ds_read_b128 v[16:19], v0 offset:0\n\t"
"ds_read_b128 v[20:23], v0 offset:1024\n\t"
"v_smfmac_f32_16x16x64_bf16 v[56:59], v[24:27], a[16:23], v9\n\t"
"ds_read_b128 v[28:31], v0 offset:3072\n\t"
"v_smfmac_f32_16x16x64_bf16 v[56:59], v[40:43], a[48:55], v9\n\t"
"ds_read_b128 v[32:35], v0 offset:4096\n\t"
"v_smfmac_f32_16x16x64_bf16 v[60:63], v[24:27], a[80:87], v9\n\t"
"ds_read_b128 v[36:39], v0 offset:5120\n\t"
"v_smfmac_f32_16x16x64_bf16 v[60:63], v[40:43], a[112:119], v9\n\t"
"ds_read_b128 v[44:47], v0 offset:7168\n\t"
"v_smfmac_f32_16x16x64_bf16 v[64:67], v[40:43], a[176:183], v9\n\t"
"s_waitcnt lgkmcnt(4)\n\t"
"v_smfmac_f32_16x16x64_bf16 v[48:51], v[16:19], v[80:87], v9\n\t"
"v_smfmac_f32_16x16x64_bf16 v[52:55], v[16:19], v[88:95], v9\n\t"
"buffer_load_dword v14, v13, s[44:47], s61 offen sc1\n\t"
"v_smfmac_f32_16x16x64_bf16 v[48:51], v[20:23], v[96:103], v9\n\t"
"v_smfmac_f32_16x16x64_bf16 v[52:55], v[20:23], v[104:111], v9\n\t"
"s_waitcnt lgkmcnt(0)\n\t"
"v_smfmac_f32_16x16x64_bf16 v[48:51], v[28:31], v[112:119], v9\n\t"
"v_smfmac_f32_16x16x64_bf16 v[52:55], v[28:31], v[120:127], v9\n\t"
"v_smfmac_f32_16x16x64_bf16 v[48:51], v[32:35], v[128:135], v9\n\t"
"ds_read_b128 v[72:75], v0 offset:16384\n\t"
"v_smfmac_f32_16x16x64_bf16 v[52:55], v[32:35], v[160:167], v9\n\t"
"ds_read_b128 v[76:79], v0 offset:17408\n\t"
"v_smfmac_f32_16x16x64_bf16 v[48:51], v[36:39], v[136:143], v9\n\t"
"ds_read_b128 v[80:83], v0 offset:18432\n\t"
"v_smfmac_f32_16x16x64_bf16 v[52:55], v[36:39], v[168:175], v9\n\t"
"ds_read_b128 v[84:87], v0 offset:19456\n\t"
"v_smfmac_f32_16x16x64_bf16 v[48:51], v[44:47], v[152:159], v9\n\t"
"ds_read_b128 v[88:91], v0 offset:20480\n\t"
"v_smfmac_f32_16x16x64_bf16 v[52:55], v[44:47], v[184:191], v9\n\t"
"ds_read_b128 v[92:95], v0 offset:21504\n\t"
"v_smfmac_f32_16x16x64_bf16 v[56:59], v[16:19], a[0:7], v9\n\t"
"ds_read_b128 v[96:99], v0 offset:22528\n\t"
"v_smfmac_f32_16x16x64_bf16 v[60:63], v[16:19], a[64:71], v9\n\t"
"ds_read_b128 v[100:103], v0 offset:23552\n\t"
"v_smfmac_f32_16x16x64_bf16 v[56:59], v[20:23], a[8:15], v9\n\t"
"ds_read_b128 v[104:107], v0 offset:28672\n\t"
"v_smfmac_f32_16x16x64_bf16 v[60:63], v[20:23], a[72:79], v9\n\t"
"ds_read_b128 v[108:111], v0 offset:29696\n\t"
"v_smfmac_f32_16x16x64_bf16 v[56:59], v[28:31], a[24:31], v9\n\t"
"v_add_f32 v48, v48, v49\n\t"
"v_smfmac_f32_16x16x64_bf16 v[60:63], v[28:31], a[88:95], v9\n\t"
"v_add_f32 v50, v50, v51\n\t"
"v_smfmac_f32_16x16x64_bf16 v[56:59], v[32:35], a[32:39], v9\n\t"
"v_cvt_pk_bf16_f32 v124, v48, v50\n\t"
"v_smfmac_f32_16x16x64_bf16 v[60:63], v[32:35], a[96:103], v9\n\t"
"v_add_f32 v52, v52, v53\n\t"
"v_smfmac_f32_16x16x64_bf16 v[56:59], v[36:39], a[40:47], v9\n\t"
"v_add_f32 v54, v54, v55\n\t"
"v_smfmac_f32_16x16x64_bf16 v[60:63], v[36:39], a[104:111], v9\n\t"
"v_cvt_pk_bf16_f32 v125, v52, v54\n\t"
"v_smfmac_f32_16x16x64_bf16 v[56:59], v[44:47], a[56:63], v9\n\t"
"ds_write_b64 v4, v[124:125] offset:8192\n\t"
"v_smfmac_f32_16x16x64_bf16 v[60:63], v[44:47], a[120:127], v9\n\t"
"v_smfmac_f32_16x16x64_bf16 v[64:67], v[16:19], a[128:135], v9\n\t"
"s_nop 4\n\t"
"v_add_f32 v56, v56, v57\n\t"
"v_add_f32 v58, v58, v59\n\t"
"v_cvt_pk_bf16_f32 v126, v56, v58\n\t"
"v_add_f32 v60, v60, v61\n\t"
"v_add_f32 v62, v62, v63\n\t"
"v_cvt_pk_bf16_f32 v127, v60, v62\n\t"
"ds_write_b64 v4, v[126:127] offset:8704\n\t"
"v_smfmac_f32_16x16x64_bf16 v[68:71], v[16:19], a[192:199], v9\n\t"
"v_smfmac_f32_16x16x64_bf16 v[64:67], v[20:23], a[136:143], v9\n\t"
"v_smfmac_f32_16x16x64_bf16 v[68:71], v[20:23], a[200:207], v9\n\t"
"v_smfmac_f32_16x16x64_bf16 v[64:67], v[28:31], a[152:159], v9\n\t"
"s_waitcnt lgkmcnt(0)\n\t"
"s_barrier\n\t"
"ds_read_b64_tr_b16 v[48:49], v5 offset:8192\n\t"
"ds_read_b64_tr_b16 v[50:51], v5 offset:8704\n\t"
"ds_read_b64_tr_b16 v[52:53], v5 offset:9216\n\t"
"v_smfmac_f32_16x16x64_bf16 v[68:71], v[28:31], a[216:223], v9\n\t"
"ds_read_b64_tr_b16 v[54:55], v5 offset:9728\n\t"
"v_smfmac_f32_16x16x64_bf16 v[64:67], v[32:35], a[160:167], v9\n\t"
"ds_read_b64_tr_b16 v[56:57], v5 offset:10240\n\t"
"v_smfmac_f32_16x16x64_bf16 v[68:71], v[32:35], a[224:231], v9\n\t"
"ds_read_b64_tr_b16 v[58:59], v5 offset:10752\n\t"
"s_waitcnt lgkmcnt(4)\n\t"
"v_mfma_f32_16x16x32_bf16 v[112:115], v[72:75], v[48:51], 0\n\t"
"ds_read_b64_tr_b16 v[60:61], v5 offset:11264\n\t"
"v_mfma_f32_16x16x32_bf16 v[116:119], v[88:91], v[48:51], 0\n\t"
"ds_read_b64_tr_b16 v[62:63], v5 offset:11776\n\t"
"s_waitcnt lgkmcnt(4)\n\t"
"v_mfma_f32_16x16x32_bf16 v[112:115], v[76:79], v[52:55], v[112:115]\n\t"
"v_mov_b64 v[16:17], 0\n\t"
"v_mov_b64 v[18:19], 0\n\t"
"v_mfma_f32_16x16x32_bf16 v[116:119], v[92:95], v[52:55], v[116:119]\n\t"
"v_mov_b64 v[20:21], 0\n\t"
"s_waitcnt lgkmcnt(2)\n\t"
"v_mfma_f32_16x16x32_bf16 v[112:115], v[80:83], v[56:59], v[112:115]\n\t"
"v_mov_b64 v[22:23], 0\n\t"
"v_mov_b64 v[24:25], 0\n\t"
"v_mfma_f32_16x16x32_bf16 v[116:119], v[96:99], v[56:59], v[116:119]\n\t"
"v_mov_b64 v[26:27], 0\n\t"
"s_waitcnt lgkmcnt(0)\n\t"
"v_mfma_f32_16x16x32_bf16 v[112:115], v[84:87], v[60:63], v[112:115]\n\t"
"v_mov_b64 v[28:29], 0\n\t"
"v_mov_b64 v[30:31], 0\n\t"
"v_mfma_f32_16x16x32_bf16 v[116:119], v[100:103], v[60:63], v[116:119]\n\t"
"v_mov_b32 v81, 0\n\t"
"v_mov_b32 v82, 0\n\t"
"v_mfma_f32_16x16x32_bf16 v[120:123], v[48:51], v[48:51], 0\n\t"
"v_mov_b64 v[124:125], s[78:79]\n\t"
"v_mov_b64 v[126:127], s[78:79]\n\t"
"v_mfma_f32_16x16x32_bf16 v[120:123], v[52:55], v[52:55], v[120:123]\n\t"
"v_max3_f32 v72, v112, v113, v114\n\t"
"v_mfma_f32_16x16x32_bf16 v[120:123], v[56:59], v[56:59], v[120:123]\n\t"
"v_max3_f32 v72, v72, v115, v116\n\t"
"v_mfma_f32_16x16x32_bf16 v[120:123], v[60:63], v[60:63], v[120:123]\n\t"
"v_max3_f32 v72, v72, v117, v118\n\t"
"v_max_f32 v72, v72, v119\n\t"
"v_smfmac_f32_16x16x64_bf16 v[64:67], v[36:39], a[168:175], v9\n\t"
"s_nop 4\n\t"
"v_max3_f32 v72, v72, v122, v123\n\t"
"v_mov_b32 v73, v72\n\t"
"v_smfmac_f32_16x16x64_bf16 v[68:71], v[36:39], a[232:239], v9\n\t"
"s_nop 0\n\t"
"v_permlane32_swap_b32 v72, v73\n\t"
"v_max_f32 v72, v72, v73\n\t"
"v_mov_b32 v73, v72\n\t"
"v_smfmac_f32_16x16x64_bf16 v[68:71], v[40:43], a[240:247], v9\n\t"
"s_nop 0\n\t"
"v_permlane16_swap_b32 v72, v73\n\t"
"v_max_f32 v72, v72, v73\n\t"
"v_mul_f32 v74, s72, v72\n\t"
"v_pk_fma_f32 v[112:113], v[112:113], s[38:39], v[74:75] op_sel_hi:[1,1,0]\n\t"
"v_pk_fma_f32 v[114:115], v[114:115], s[38:39], v[74:75] op_sel_hi:[1,1,0]\n\t"
"v_pk_fma_f32 v[116:117], v[116:117], s[38:39], v[74:75] op_sel_hi:[1,1,0]\n\t"
"v_pk_fma_f32 v[118:119], v[118:119], s[38:39], v[74:75] op_sel_hi:[1,1,0]\n\t"
"v_pk_fma_f32 v[122:123], v[122:123], s[38:39], v[74:75] op_sel_hi:[1,1,0]\n\t"
"v_exp_f32 v112, v112\n\t"
"v_exp_f32 v113, v113\n\t"
"v_smfmac_f32_16x16x64_bf16 v[64:67], v[44:47], a[184:191], v9\n\t"
"v_exp_f32 v114, v114\n\t"
"v_exp_f32 v115, v115\n\t"
"v_exp_f32 v116, v116\n\t"
"v_smfmac_f32_16x16x64_bf16 v[68:71], v[44:47], a[248:255], v9\n\t"
"v_exp_f32 v117, v117\n\t"
"v_exp_f32 v118, v118\n\t"
"v_exp_f32 v119, v119\n\t"
"v_cvt_pk_bf16_f32 v76, v112, v113\n\t"
"v_cvt_pk_bf16_f32 v77, v114, v115\n\t"
"v_cvt_pk_bf16_f32 v78, v116, v117\n\t"
"v_cvt_pk_bf16_f32 v79, v118, v119\n\t"
"v_exp_f32 v122, v122\n\t"
"v_exp_f32 v123, v123\n\t"
"v_mfma_f32_16x16x32_bf16 v[72:75], v[124:127], v[76:79], 0\n\t"
"v_cvt_pk_bf16_f32 v80, v122, v123\n\t"
"v_cvt_pk_bf16_f32 v83, v122, v123\n\t"
"v_mfma_f32_16x16x32_bf16 v[48:51], v[104:107], v[76:79], 0\n\t"
"v_add_f32 v64, v64, v65\n\t"
"v_add_f32 v66, v66, v67\n\t"
"v_mfma_f32_16x16x16_bf16 v[72:75], v[124:125], v[80:81], v[72:75]\n\t"
"v_add_f32 v68, v68, v69\n\t"
"v_add_f32 v70, v70, v71\n\t"
"v_cvt_pk_bf16_f32 v84, v64, v66\n\t"
"v_cvt_pk_bf16_f32 v85, v68, v70\n\t"
"v_mfma_f32_16x16x32_bf16 v[52:55], v[108:111], v[76:79], 0\n\t"
"v_mov_b64 v[64:65], v[84:85]\n\t"
"v_mfma_f32_16x16x16_bf16 v[48:51], v[84:85], v[80:81], v[48:51]\n\t"
"v_mfma_f32_16x16x16_bf16 v[52:55], v[84:85], v[82:83], v[52:55]\n\t"
"v_rcp_f32 v124, v72\n\t"
"s_nop 5\n\t"
"v_pk_mul_f32 v[48:49], v[48:49], v[124:125] op_sel_hi:[1,0]\n\t"
"v_pk_mul_f32 v[50:51], v[50:51], v[124:125] op_sel_hi:[1,0]\n\t"
"v_pk_mul_f32 v[52:53], v[52:53], v[124:125] op_sel_hi:[1,0]\n\t"
"v_pk_mul_f32 v[54:55], v[54:55], v[124:125] op_sel_hi:[1,0]\n\t"
"v_cvt_pk_bf16_f32 v120, v48, v49\n\t"
"v_cvt_pk_bf16_f32 v121, v50, v51\n\t"
"v_cvt_pk_bf16_f32 v122, v52, v53\n\t"
"v_cvt_pk_bf16_f32 v123, v54, v55\n\t"
"ds_write_b128 v6, v[120:123] offset:12288\n\t"
"s_cmp_eq_u32 s64, 63\n\t"
"s_cbranch_scc1 CW2_fin8_%=\n\t"
"s_waitcnt lgkmcnt(0)\n\t"
"s_barrier\n\t"
"ds_read_b128 v[120:123], v0 offset:12288\n\t"
"ds_read_b128 v[124:127], v0 offset:13312\n\t"
"s_waitcnt lgkmcnt(1)\n\t"
"v_smfmac_f32_16x16x64_bf16 v[16:19], v[120:123], v[192:199], v9\n\t"
"v_smfmac_f32_16x16x64_bf16 v[20:23], v[120:123], v[200:207], v9\n\t"
"s_waitcnt lgkmcnt(0)\n\t"
"v_smfmac_f32_16x16x64_bf16 v[16:19], v[124:127], v[224:231], v9\n\t"
"s_nop 0\n\t"
"v_smfmac_f32_16x16x64_bf16 v[20:23], v[124:127], v[232:239], v9\n\t"
"s_nop 0\n\t"
"v_smfmac_f32_16x16x64_bf16 v[24:27], v[120:123], v[208:215], v9\n\t"
"s_nop 0\n\t"
"v_smfmac_f32_16x16x64_bf16 v[28:31], v[120:123], v[216:223], v9\n\t"
"s_nop 0\n\t"
"v_smfmac_f32_16x16x64_bf16 v[24:27], v[124:127], v[240:247], v9\n\t"
"v_add_f32 v16, v16, v17\n\t"
"v_smfmac_f32_16x16x64_bf16 v[28:31], v[124:127], v[248:255], v9\n\t"
"v_add_f32 v18, v18, v19\n\t"
"v_cvt_pk_bf16_f32 v68, v16, v18\n\t"
"v_add_f32 v20, v20, v21\n\t"
"v_add_f32 v22, v22, v23\n\t"
"v_cvt_pk_bf16_f32 v69, v20, v22\n\t"
"s_nop 0\n\t"
"v_add_f32 v24, v24, v25\n\t"
"v_add_f32 v26, v26, v27\n\t"
"v_cvt_pk_bf16_f32 v70, v24, v26\n\t"
"v_add_f32 v28, v28, v29\n\t"
"v_add_f32 v30, v30, v31\n\t"
"v_cvt_pk_bf16_f32 v71, v28, v30\n\t"
"buffer_store_dwordx4 v[68:71], v7, s[40:43], s65 offen sc1\n\t"
"s_memrealtime s[74:75]\n\t"
"s_waitcnt lgkmcnt(0)\n\t"
"s_mov_b32 s59, s74\n\t"
"ds_read_b64_tr_b16 v[68:69], v5 offset:10240\n\t"
"ds_read_b64_tr_b16 v[70:71], v5 offset:10752\n\t"
"s_and_b32 s74, s64, 3\n\t"
"s_lshl_b32 s74, s74, 16\n\t"
"s_add_u32 s69, s74, 1786880\n\t"
"s_add_u32 s70, s74, 1821696\n\t"
"buffer_load_dword v8, v10, s[40:43], s68 offen sc1\n\t"
"buffer_load_dwordx4 v0, s[44:47], s76 offen offset:0 sc1 lds\n\t"
"buffer_load_dwordx4 v0, s[44:47], s76 offen offset:1024 sc1 lds\n\t"
"buffer_load_dwordx4 v0, s[44:47], s76 offen offset:2048 sc1 lds\n\t"
"buffer_load_dwordx4 v0, s[44:47], s76 offen offset:3072 sc1 lds\n\t"
"ds_read_b128 v[56:59], v2 offset:32768\n\t"
"ds_read_b128 v[60:63], v2 offset:36864\n\t"
"ds_read_b128 v[72:75], v2 offset:40960\n\t"
"ds_read_b128 v[76:79], v2 offset:45056\n\t"
"ds_read_b128 v[80:83], v2 offset:0\n\t"
"ds_read_b128 v[84:87], v2 offset:4096\n\t"
"ds_read_b128 v[88:91], v2 offset:8192\n\t"
"ds_read_b128 v[92:95], v2 offset:12288\n\t"
"ds_read_b128 v[96:99], v2 offset:16384\n\t"
"ds_read_b128 v[100:103], v2 offset:20480\n\t"
"ds_read_b128 v[104:107], v2 offset:24576\n\t"
"ds_read_b128 v[108:111], v2 offset:28672\n\t"
"ds_read_b128 v[112:115], v2 offset:49152\n\t"
"ds_read_b128 v[116:119], v2 offset:53248\n\t"
"ds_read_b128 v[120:123], v2 offset:57344\n\t"
"ds_read_b128 v[124:127], v2 offset:61440\n\t"
"buffer_store_dwordx2 v[64:65], v12, s[52:55], s71 offen offset:3072 sc1\n\t"
"s_waitcnt lgkmcnt(15)\n\t"
"buffer_store_dwordx4 v[68:71], v11, s[52:55], s71 offen offset:1024 sc1\n\t"
"v_mov_b64 v[48:49], 0\n\t"
"v_mov_b64 v[50:51], 0\n\t"
"v_mov_b64 v[52:53], 0\n\t"
"v_mov_b64 v[54:55], 0\n\t"
"v_mov_b64 v[64:65], 0\n\t"
"v_mov_b64 v[66:67], 0\n\t"
"v_mov_b64 v[68:69], 0\n\t"
"v_mov_b64 v[70:71], 0\n\t"
"s_add_u32 s64, s64, 1\n\t"
"s_add_u32 s65, s65, 524288\n\t"
"s_cmp_eq_u32 s65, 1572864\n\t"
"s_cselect_b32 s65, 0, s65\n\t"
"s_lshl_b32 s71, s64, 17\n\t"
"s_and_b32 s68, s64, 1\n\t"
"s_lshl_b32 s68, s68, 9\n\t"
"s_add_u32 s81, s81, 1048576\n\t"
"s_lshr_b32 s74, s81, 24\n\t"
"s_lshr_b32 s74, s80, s74\n\t"
"s_lshr_b32 s75, s81, 21\n\t"
"s_xor_b32 s74, s74, s75\n\t"
"s_and_b32 s74, s74, 1\n\t"
"s_lshl_b32 s74, s74, 13\n\t"
"s_or_b32 s76, s81, s74\n\t"
"s_add_u32 s61, s81, 1048576\n\t"
"s_lshr_b32 s74, s61, 24\n\t"
"s_lshr_b32 s74, s80, s74\n\t"
"s_lshr_b32 s75, s61, 21\n\t"
"s_xor_b32 s74, s74, s75\n\t"
"s_and_b32 s74, s74, 1\n\t"
"s_lshl_b32 s74, s74, 13\n\t"
"s_or_b32 s61, s61, s74\n\t"
"s_waitcnt vmcnt(6)\n\t"
"v_max_i32_dpp v8, v8, v8 row_shl:4 row_mask:0xf bank_mask:0xf\n\t"
"s_nop 0\n\t"
"v_readlane_b32 s74, v8, 0\n\t"
"v_readlane_b32 s75, v8, 1\n\t"
"v_readlane_b32 s50, v8, 2\n\t"
"v_readlane_b32 s51, v8, 3\n\t"
"s_max_i32 s74, s74, s75\n\t"
"s_max_i32 s50, s50, s51\n\t"
"s_max_i32 s74, s74, s50\n\t"
"s_add_u32 s58, s74, s48\n\t"
"s_sub_i32 s74, s58, s59\n\t"
"s_min_i32 s74, s74, 60\n\t"
"s_max_i32 s74, s74, -2\n\t"
"s_add_u32 s58, s59, s74\n\t"
"s_waitcnt lgkmcnt(0)\n\t"
"s_add_u32 s75, s58, %[dC]\n\t"
"CW2_sp12_%=:\n\t"
"s_memrealtime s[50:51]\n\t"
"s_waitcnt lgkmcnt(0)\n\t"
"s_sub_i32 s60, s75, s50\n\t"
"s_cmp_gt_i32 s60, 4\n\t"
"s_cbranch_scc1 CW2_sp12_%=\n\t"
"s_cmp_lt_i32 s60, 2\n\t"
"s_cbranch_scc1 CW2_sd13_%=\n\t"
"s_sub_u32 s60, s60, 2\n\t"
"CW2_fd14_%=:\n\t"
"s_sub_u32 s60, s60, 1\n\t"
"s_cbranch_scc0 CW2_fd14_%=\n\t"
"CW2_sd13_%=:\n\t"
"buffer_load_dwordx4 v[24:27], v0, s[40:43], s69 offen sc1\n\t"
"buffer_load_dwordx4 v[40:43], v0, s[40:43], s70 offen sc1\n\t"
"s_mov_b32 s60, 0\n\t"
"s_branch CW2_top7_%=\n\t"
"CW2_art9_%=:\n\t"
"v_cmp_eq_u32 vcc, s62, v15\n\t"
"s_cbranch_vccnz CW2_go15_%=\n\t"
"s_mov_b64 s[74:75], 0\n\t"
"v_cmp_eq_u32 vcc, s62, v24\n\t"
"s_nop 1\n\t"
"s_or_b64 s[74:75], s[74:75], vcc\n\t"
"v_cmp_eq_u32 vcc, s62, v25\n\t"
"s_nop 1\n\t"
"s_or_b64 s[74:75], s[74:75], vcc\n\t"
"v_cmp_eq_u32 vcc, s62, v26\n\t"
"s_nop 1\n\t"
"s_or_b64 s[74:75], s[74:75], vcc\n\t"
"v_cmp_eq_u32 vcc, s62, v27\n\t"
"s_nop 1\n\t"
"s_or_b64 s[74:75], s[74:75], vcc\n\t"
"v_cmp_eq_u32 vcc, s62, v40\n\t"
"s_nop 1\n\t"
"s_or_b64 s[74:75], s[74:75], vcc\n\t"
"v_cmp_eq_u32 vcc, s62, v41\n\t"
"s_nop 1\n\t"
"s_or_b64 s[74:75], s[74:75], vcc\n\t"
"v_cmp_eq_u32 vcc, s62, v42\n\t"
"s_nop 1\n\t"
"s_or_b64 s[74:75], s[74:75], vcc\n\t"
"v_cmp_eq_u32 vcc, s62, v43\n\t"
"s_nop 1\n\t"
"s_or_b64 s[74:75], s[74:75], vcc\n\t"
"s_cbranch_scc0 CW2_aok10_%=\n\t"
"CW2_go15_%=:\n\t"
"s_add_u32 s60, s60, 1\n\t"
"s_cmp_ge_u32 s60, 0x4000\n\t"
"s_cbranch_scc1 CW2_aok10_%=\n\t"
"s_waitcnt lgkmcnt(0)\n\t"
"buffer_load_dwordx4 v[24:27], v0, s[40:43], s69 offen sc1\n\t"
"buffer_load_dwordx4 v[40:43], v0, s[40:43], s70 offen sc1\n\t"
"v_mov_b64 v[48:49], 0\n\t"
"v_mov_b64 v[50:51], 0\n\t"
"v_mov_b64 v[52:53], 0\n\t"
"v_mov_b64 v[54:55], 0\n\t"
"v_mov_b64 v[64:65], 0\n\t"
"v_mov_b64 v[66:67], 0\n\t"
"v_mov_b64 v[68:69], 0\n\t"
"v_mov_b64 v[70:71], 0\n\t"
"s_branch CW2_top7_%=\n\t"
"CW2_fin8_%=:\n\t"
"s_waitcnt lgkmcnt(0)\n\t"
"s_nop 9\n\t"
"ds_read_b64_tr_b16 v[68:69], v5 offset:10240\n\t"
"ds_read_b64_tr_b16 v[70:71], v5 offset:10752\n\t"
"buffer_store_dwordx2 v[64:65], v12, s[52:55], s71 offen offset:3072 sc1\n\t"
"s_waitcnt lgkmcnt(0)\n\t"
"buffer_store_dwordx4 v[68:71], v11, s[52:55], s71 offen offset:1024 sc1\n\t"
"s_waitcnt vmcnt(0)\n\t"
"s_barrier\n\t"
"s_branch CHAIN_END_%=\n\t"
"CHAIN_W3_%=:\n\t"
"v_mbcnt_lo_u32_b32 v127, -1, 0\n\t"
"v_mbcnt_hi_u32_b32 v127, -1, v127\n\t"
"v_lshlrev_b32 v127, 2, v127\n\t"
"v_add_u32 v127, 768, v127\n\t"
"ds_read_b32 v0, v127 offset:32768\n\t"
"ds_read_b32 v1, v127 offset:33792\n\t"
"ds_read_b32 v2, v127 offset:34816\n\t"
"ds_read_b32 v3, v127 offset:35840\n\t"
"ds_read_b32 v4, v127 offset:36864\n\t"
"ds_read_b32 v5, v127 offset:37888\n\t"
"ds_read_b32 v6, v127 offset:38912\n\t"
"ds_read_b32 v7, v127 offset:39936\n\t"
"ds_read_b32 v8, v127 offset:40960\n\t"
"ds_read_b32 v9, v127 offset:41984\n\t"
"ds_read_b32 v10, v127 offset:43008\n\t"
"ds_read_b32 v11, v127 offset:44032\n\t"
"ds_read_b32 v12, v127 offset:45056\n\t"
"ds_read_b32 v16, v127 offset:46080\n\t"
"ds_read_b32 v20, v127 offset:47104\n\t"
"s_waitcnt lgkmcnt(0)\n\t"
"ds_read_b32 v36, v127 offset:48128\n\t"
"ds_read_b32 v26, v127 offset:49152\n\t"
"ds_read_b32 v13, v127 offset:50176\n\t"
"s_waitcnt lgkmcnt(0)\n\t"
"s_barrier\n\t"
"v_xor_b32 v17, 64, v16\n\t"
"v_xor_b32 v21, 64, v20\n\t"
"v_xor_b32 v18, 128, v16\n\t"
"v_xor_b32 v22, 65536, v20\n\t"
"v_xor_b32 v19, 192, v16\n\t"
"v_xor_b32 v23, 65600, v20\n\t"
"v_xor_b32 v24, 192, v36\n\t"
"v_xor_b32 v25, 224, v36\n\t"
"v_xor_b32 v28, 0, v36\n\t"
"v_add_u32 v28, 49152, v28\n\t"
"v_xor_b32 v29, 32, v36\n\t"
"v_add_u32 v29, 49152, v29\n\t"
"v_xor_b32 v30, 64, v36\n\t"
"v_add_u32 v30, 49152, v30\n\t"
"v_xor_b32 v31, 96, v36\n\t"
"v_add_u32 v31, 49152, v31\n\t"
"v_xor_b32 v32, 128, v36\n\t"
"v_add_u32 v32, 49152, v32\n\t"
"v_xor_b32 v33, 160, v36\n\t"
"v_add_u32 v33, 49152, v33\n\t"
"v_xor_b32 v34, 192, v36\n\t"
"v_add_u32 v34, 49152, v34\n\t"
"v_xor_b32 v35, 224, v36\n\t"
"v_add_u32 v35, 49152, v35\n\t"
"s_mov_b32 s82, 0xff00ff00\n\t"
"s_mov_b32 s83, 0xff00ff00\n\t"
"s_mov_b32 s56, 0xffff\n\t"
"s_mov_b32 s57, 0\n\t"
"s_mov_b64 s[44:45], %[kv]\n\t"
"s_and_b32 s45, s45, 0xffff\n\t"
"s_mov_b32 s46, 67108864\n\t"
"s_mov_b32 s47, 0x20000\n\t"
"s_mov_b64 s[52:53], %[kmv]\n\t"
"s_and_b32 s53, s53, 0xffff\n\t"
"s_mov_b32 s54, %[kmsz]\n\t"
"s_mov_b32 s55, 0x20000\n\t"
"s_mov_b64 s[84:85], %[wq]\n\t"
"s_and_b32 s85, s85, 0xffff\n\t"
"s_mov_b32 s86, 4194304\n\t"
"s_mov_b32 s87, 0x20000\n\t"
"s_mov_b64 s[88:89], %[wk]\n\t"
"s_and_b32 s89, s89, 0xffff\n\t"
"s_mov_b32 s90, 4194304\n\t"
"s_mov_b32 s91, 0x20000\n\t"
"s_mov_b64 s[92:93], %[wv]\n\t"
"s_and_b32 s93, s93, 0xffff\n\t"
"s_mov_b32 s94, 4194304\n\t"
"s_mov_b32 s95, 0x20000\n\t"
"s_mov_b64 s[96:97], %[wo]\n\t"
"s_and_b32 s97, s97, 0xffff\n\t"
"s_mov_b32 s98, 130944\n\t"
"s_mov_b32 s99, 0x20000\n\t"
"s_mov_b64 s[60:61], %[mem0]\n\t"
"s_and_b32 s61, s61, 0xffff\n\t"
"s_mov_b32 s62, 8192\n\t"
"s_mov_b32 s63, 0x20000\n\t"
"buffer_load_dwordx2 v[48:49], v26, s[60:63], 0 offen offset:0\n\t"
"buffer_load_dwordx2 v[50:51], v26, s[60:63], 0 offen offset:32\n\t"
"buffer_load_dwordx2 v[52:53], v26, s[60:63], 0 offen offset:512\n\t"
"buffer_load_dwordx2 v[54:55], v26, s[60:63], 0 offen offset:544\n\t"
"v_mov_b32 v57, 0\n\t"
"global_load_dword v56, v57, %[ctl] offset:128 sc1\n\t"
"v_mov_b32 v60, 0xffbfffbf\n\t"
"v_mov_b32 v61, 0xffbfffbf\n\t"
"v_mov_b32 v62, 0xffbfffbf\n\t"
"v_mov_b32 v63, 0xffbfffbf\n\t"
"s_mov_b32 s74, 8126464\n\t"
"buffer_store_dwordx4 v[60:63], v1, s[52:55], s74 offen sc1\n\t"
"s_mov_b32 s74, 8257536\n\t"
"buffer_store_dwordx4 v[60:63], v1, s[52:55], s74 offen sc1\n\t"
"s_mov_b32 m0, 45056\n\t"
"s_mov_b32 s74, 2195456\n\t"
"buffer_load_dwordx4 v16, s[84:87], s74 offen lds\n\t"
"s_mov_b32 m0, 46080\n\t"
"s_mov_b32 s74, 2326528\n\t"
"buffer_load_dwordx4 v17, s[84:87], s74 offen lds\n\t"
"s_mov_b32 m0, 47104\n\t"
"s_mov_b32 s74, 2457600\n\t"
"buffer_load_dwordx4 v18, s[84:87], s74 offen lds\n\t"
"s_mov_b32 m0, 48128\n\t"
"s_mov_b32 s74, 2588672\n\t"
"buffer_load_dwordx4 v19, s[84:87], s74 offen lds\n\t"
"s_mov_b32 m0, 61440\n\t"
"s_mov_b32 s74, 2719744\n\t"
"buffer_load_dwordx4 v16, s[84:87], s74 offen lds\n\t"
"s_mov_b32 m0, 62464\n\t"
"s_mov_b32 s74, 2850816\n\t"
"buffer_load_dwordx4 v17, s[84:87], s74 offen lds\n\t"
"s_mov_b32 m0, 63488\n\t"
"s_mov_b32 s74, 2981888\n\t"
"buffer_load_dwordx4 v18, s[84:87], s74 offen lds\n\t"
"s_mov_b32 m0, 64512\n\t"
"s_mov_b32 s74, 3112960\n\t"
"buffer_load_dwordx4 v19, s[84:87], s74 offen lds\n\t"
"s_mov_b32 m0, 77824\n\t"
"s_mov_b32 s74, 3244032\n\t"
"buffer_load_dwordx4 v16, s[84:87], s74 offen lds\n\t"
"s_mov_b32 m0, 78848\n\t"
"s_mov_b32 s74, 3375104\n\t"
"buffer_load_dwordx4 v17, s[84:87], s74 offen lds\n\t"
"s_mov_b32 m0, 79872\n\t"
"s_mov_b32 s74, 3506176\n\t"
"buffer_load_dwordx4 v18, s[84:87], s74 offen lds\n\t"
"s_mov_b32 m0, 80896\n\t"
"s_mov_b32 s74, 3637248\n\t"
"buffer_load_dwordx4 v19, s[84:87], s74 offen lds\n\t"
"s_mov_b32 m0, 94208\n\t"
"s_mov_b32 s74, 3768320\n\t"
"buffer_load_dwordx4 v16, s[84:87], s74 offen lds\n\t"
"s_mov_b32 m0, 95232\n\t"
"s_mov_b32 s74, 3899392\n\t"
"buffer_load_dwordx4 v17, s[84:87], s74 offen lds\n\t"
"s_mov_b32 m0, 96256\n\t"
"s_mov_b32 s74, 4030464\n\t"
"buffer_load_dwordx4 v18, s[84:87], s74 offen lds\n\t"
"s_mov_b32 m0, 97280\n\t"
"s_mov_b32 s74, 4161536\n\t"
"buffer_load_dwordx4 v19, s[84:87], s74 offen lds\n\t"
"s_mov_b32 m0, 110592\n\t"
"s_mov_b32 s74, 2195456\n\t"
"buffer_load_dwordx4 v16, s[88:91], s74 offen lds\n\t"
"s_mov_b32 m0, 111616\n\t"
"s_mov_b32 s74, 2326528\n\t"
"buffer_load_dwordx4 v17, s[88:91], s74 offen lds\n\t"
"s_mov_b32 m0, 112640\n\t"
"s_mov_b32 s74, 2457600\n\t"
"buffer_load_dwordx4 v18, s[88:91], s74 offen lds\n\t"
"s_mov_b32 m0, 113664\n\t"
"s_mov_b32 s74, 2588672\n\t"
"buffer_load_dwordx4 v19, s[88:91], s74 offen lds\n\t"
"s_mov_b32 m0, 126976\n\t"
"s_mov_b32 s74, 2719744\n\t"
"buffer_load_dwordx4 v16, s[88:91], s74 offen lds\n\t"
"s_mov_b32 m0, 128000\n\t"
"s_mov_b32 s74, 2850816\n\t"
"buffer_load_dwordx4 v17, s[88:91], s74 offen lds\n\t"
"s_mov_b32 m0, 129024\n\t"
"s_mov_b32 s74, 2981888\n\t"
"buffer_load_dwordx4 v18, s[88:91], s74 offen lds\n\t"
"s_mov_b32 m0, 130048\n\t"
"s_mov_b32 s74, 3112960\n\t"
"buffer_load_dwordx4 v19, s[88:91], s74 offen lds\n\t"
"v_add_u32 v38, 32768, v24\n\t"
"v_add_u32 v39, 32768, v25\n\t"
"v_add_u32 v36, 98304, v24\n\t"
"v_add_u32 v37, 98304, v25\n\t"
"v_add_u32 v28, 32768, v28\n\t"
"v_add_u32 v29, 32768, v29\n\t"
"v_add_u32 v30, 32768, v30\n\t"
"v_add_u32 v31, 32768, v31\n\t"
"v_add_u32 v32, 32768, v32\n\t"
"v_add_u32 v33, 32768, v33\n\t"
"v_add_u32 v34, 32768, v34\n\t"
"v_add_u32 v35, 32768, v35\n\t"
"s_waitcnt vmcnt(16)\n\t"
"s_barrier\n\t"
"s_mov_b32 m0, 143360\n\t"
"s_mov_b32 s74, 3244032\n\t"
"buffer_load_dwordx4 v16, s[88:91], s74 offen lds\n\t"
"ds_read_b64_tr_b16 v[128:129], v38 offset:0\n\t"
"ds_read_b64_tr_b16 v[130:131], v38 offset:512\n\t"
"s_mov_b32 m0, 144384\n\t"
"s_mov_b32 s74, 3375104\n\t"
"buffer_load_dwordx4 v17, s[88:91], s74 offen lds\n\t"
"ds_read_b64_tr_b16 v[132:133], v38 offset:8192\n\t"
"ds_read_b64_tr_b16 v[134:135], v38 offset:8704\n\t"
"s_mov_b32 m0, 145408\n\t"
"s_mov_b32 s74, 3506176\n\t"
"buffer_load_dwordx4 v18, s[88:91], s74 offen lds\n\t"
"ds_read_b64_tr_b16 a[32:33], v39 offset:0\n\t"
"ds_read_b64_tr_b16 a[34:35], v39 offset:512\n\t"
"s_mov_b32 m0, 146432\n\t"
"s_mov_b32 s74, 3637248\n\t"
"buffer_load_dwordx4 v19, s[88:91], s74 offen lds\n\t"
"ds_read_b64_tr_b16 a[36:37], v39 offset:8192\n\t"
"ds_read_b64_tr_b16 a[38:39], v39 offset:8704\n\t"
"s_mov_b32 m0, 159744\n\t"
"s_mov_b32 s74, 3768320\n\t"
"buffer_load_dwordx4 v16, s[88:91], s74 offen lds\n\t"
"ds_read_b64_tr_b16 v[136:137], v38 offset:16384\n\t"
"ds_read_b64_tr_b16 v[138:139], v38 offset:16896\n\t"
"s_mov_b32 m0, 160768\n\t"
"s_mov_b32 s74, 3899392\n\t"
"buffer_load_dwordx4 v17, s[88:91], s74 offen lds\n\t"
"ds_read_b64_tr_b16 v[140:141], v38 offset:24576\n\t"
"ds_read_b64_tr_b16 v[142:143], v38 offset:25088\n\t"
"s_mov_b32 m0, 161792\n\t"
"s_mov_b32 s74, 4030464\n\t"
"buffer_load_dwordx4 v18, s[88:91], s74 offen lds\n\t"
"ds_read_b64_tr_b16 a[40:41], v39 offset:16384\n\t"
"ds_read_b64_tr_b16 a[42:43], v39 offset:16896\n\t"
"s_mov_b32 m0, 162816\n\t"
"s_mov_b32 s74, 4161536\n\t"
"buffer_load_dwordx4 v19, s[88:91], s74 offen lds\n\t"
"ds_read_b64_tr_b16 a[44:45], v39 offset:24576\n\t"
"ds_read_b64_tr_b16 a[46:47], v39 offset:25088\n\t"
"s_waitcnt lgkmcnt(0)\n\t"
"s_waitcnt vmcnt(16)\n\t"
"s_barrier\n\t"
"s_mov_b32 m0, 45056\n\t"
"s_mov_b32 s74, 98304\n\t"
"buffer_load_dwordx4 v16, s[92:95], s74 offen lds\n\t"
"ds_read_b64_tr_b16 v[144:145], v38 offset:32768\n\t"
"ds_read_b64_tr_b16 v[146:147], v38 offset:33280\n\t"
"s_mov_b32 m0, 46080\n\t"
"s_mov_b32 s74, 229376\n\t"
"buffer_load_dwordx4 v17, s[92:95], s74 offen lds\n\t"
"ds_read_b64_tr_b16 v[148:149], v38 offset:40960\n\t"
"ds_read_b64_tr_b16 v[150:151], v38 offset:41472\n\t"
"s_mov_b32 m0, 47104\n\t"
"s_mov_b32 s74, 360448\n\t"
"buffer_load_dwordx4 v18, s[92:95], s74 offen lds\n\t"
"ds_read_b64_tr_b16 a[48:49], v39 offset:32768\n\t"
"ds_read_b64_tr_b16 a[50:51], v39 offset:33280\n\t"
"s_mov_b32 m0, 48128\n\t"
"s_mov_b32 s74, 491520\n\t"
"buffer_load_dwordx4 v19, s[92:95], s74 offen lds\n\t"
"ds_read_b64_tr_b16 a[52:53], v39 offset:40960\n\t"
"ds_read_b64_tr_b16 a[54:55], v39 offset:41472\n\t"
"s_mov_b32 m0, 61440\n\t"
"s_mov_b32 s74, 622592\n\t"
"buffer_load_dwordx4 v16, s[92:95], s74 offen lds\n\t"
"ds_read_b64_tr_b16 v[152:153], v38 offset:49152\n\t"
"ds_read_b64_tr_b16 v[154:155], v38 offset:49664\n\t"
"s_mov_b32 m0, 62464\n\t"
"s_mov_b32 s74, 753664\n\t"
"buffer_load_dwordx4 v17, s[92:95], s74 offen lds\n\t"
"ds_read_b64_tr_b16 v[156:157], v38 offset:57344\n\t"
"ds_read_b64_tr_b16 v[158:159], v38 offset:57856\n\t"
"s_mov_b32 m0, 63488\n\t"
"s_mov_b32 s74, 884736\n\t"
"buffer_load_dwordx4 v18, s[92:95], s74 offen lds\n\t"
"ds_read_b64_tr_b16 a[56:57], v39 offset:49152\n\t"
"ds_read_b64_tr_b16 a[58:59], v39 offset:49664\n\t"
"s_mov_b32 m0, 64512\n\t"
"s_mov_b32 s74, 1015808\n\t"
"buffer_load_dwordx4 v19, s[92:95], s74 offen lds\n\t"
"ds_read_b64_tr_b16 a[60:61], v39 offset:57344\n\t"
"ds_read_b64_tr_b16 a[62:63], v39 offset:57856\n\t"
"s_waitcnt lgkmcnt(0)\n\t"
"s_waitcnt vmcnt(16)\n\t"
"s_barrier\n\t"
"s_mov_b32 m0, 77824\n\t"
"s_mov_b32 s74, 1146880\n\t"
"buffer_load_dwordx4 v16, s[92:95], s74 offen lds\n\t"
"ds_read_b64_tr_b16 v[160:161], v36 offset:0\n\t"
"ds_read_b64_tr_b16 v[162:163], v36 offset:512\n\t"
"s_mov_b32 m0, 78848\n\t"
"s_mov_b32 s74, 1277952\n\t"
"buffer_load_dwordx4 v17, s[92:95], s74 offen lds\n\t"
"ds_read_b64_tr_b16 v[164:165], v36 offset:8192\n\t"
"ds_read_b64_tr_b16 v[166:167], v36 offset:8704\n\t"
"s_mov_b32 m0, 79872\n\t"
"s_mov_b32 s74, 1409024\n\t"
"buffer_load_dwordx4 v18, s[92:95], s74 offen lds\n\t"
"ds_read_b64_tr_b16 a[96:97], v37 offset:0\n\t"
"ds_read_b64_tr_b16 a[98:99], v37 offset:512\n\t"
"s_mov_b32 m0, 80896\n\t"
"s_mov_b32 s74, 1540096\n\t"
"buffer_load_dwordx4 v19, s[92:95], s74 offen lds\n\t"
"ds_read_b64_tr_b16 a[100:101], v37 offset:8192\n\t"
"ds_read_b64_tr_b16 a[102:103], v37 offset:8704\n\t"
"s_mov_b32 m0, 94208\n\t"
"s_mov_b32 s74, 1671168\n\t"
"buffer_load_dwordx4 v16, s[92:95], s74 offen lds\n\t"
"ds_read_b64_tr_b16 v[168:169], v36 offset:16384\n\t"
"ds_read_b64_tr_b16 v[170:171], v36 offset:16896\n\t"
"s_mov_b32 m0, 95232\n\t"
"s_mov_b32 s74, 1802240\n\t"
"buffer_load_dwordx4 v17, s[92:95], s74 offen lds\n\t"
"ds_read_b64_tr_b16 v[172:173], v36 offset:24576\n\t"
"ds_read_b64_tr_b16 v[174:175], v36 offset:25088\n\t"
"s_mov_b32 m0, 96256\n\t"
"s_mov_b32 s74, 1933312\n\t"
"buffer_load_dwordx4 v18, s[92:95], s74 offen lds\n\t"
"ds_read_b64_tr_b16 a[104:105], v37 offset:16384\n\t"
"ds_read_b64_tr_b16 a[106:107], v37 offset:16896\n\t"
"s_mov_b32 m0, 97280\n\t"
"s_mov_b32 s74, 2064384\n\t"
"buffer_load_dwordx4 v19, s[92:95], s74 offen lds\n\t"
"ds_read_b64_tr_b16 a[108:109], v37 offset:24576\n\t"
"ds_read_b64_tr_b16 a[110:111], v37 offset:25088\n\t"
"s_waitcnt lgkmcnt(0)\n\t"
"s_waitcnt vmcnt(16)\n\t"
"s_barrier\n\t"
"s_mov_b32 m0, 110592\n\t"
"s_mov_b32 s74, 2195456\n\t"
"buffer_load_dwordx4 v16, s[92:95], s74 offen lds\n\t"
"ds_read_b64_tr_b16 v[176:177], v36 offset:32768\n\t"
"ds_read_b64_tr_b16 v[178:179], v36 offset:33280\n\t"
"s_mov_b32 m0, 111616\n\t"
"s_mov_b32 s74, 2326528\n\t"
"buffer_load_dwordx4 v17, s[92:95], s74 offen lds\n\t"
"ds_read_b64_tr_b16 v[180:181], v36 offset:40960\n\t"
"ds_read_b64_tr_b16 v[182:183], v36 offset:41472\n\t"
"s_mov_b32 m0, 112640\n\t"
"s_mov_b32 s74, 2457600\n\t"
"buffer_load_dwordx4 v18, s[92:95], s74 offen lds\n\t"
"ds_read_b64_tr_b16 a[112:113], v37 offset:32768\n\t"
"ds_read_b64_tr_b16 a[114:115], v37 offset:33280\n\t"
"s_mov_b32 m0, 113664\n\t"
"s_mov_b32 s74, 2588672\n\t"
"buffer_load_dwordx4 v19, s[92:95], s74 offen lds\n\t"
"ds_read_b64_tr_b16 a[116:117], v37 offset:40960\n\t"
"ds_read_b64_tr_b16 a[118:119], v37 offset:41472\n\t"
"s_mov_b32 m0, 126976\n\t"
"s_mov_b32 s74, 2719744\n\t"
"buffer_load_dwordx4 v16, s[92:95], s74 offen lds\n\t"
"ds_read_b64_tr_b16 v[184:185], v36 offset:49152\n\t"
"ds_read_b64_tr_b16 v[186:187], v36 offset:49664\n\t"
"s_mov_b32 m0, 128000\n\t"
"s_mov_b32 s74, 2850816\n\t"
"buffer_load_dwordx4 v17, s[92:95], s74 offen lds\n\t"
"ds_read_b64_tr_b16 v[188:189], v36 offset:57344\n\t"
"ds_read_b64_tr_b16 v[190:191], v36 offset:57856\n\t"
"s_mov_b32 m0, 129024\n\t"
"s_mov_b32 s74, 2981888\n\t"
"buffer_load_dwordx4 v18, s[92:95], s74 offen lds\n\t"
"ds_read_b64_tr_b16 a[120:121], v37 offset:49152\n\t"
"ds_read_b64_tr_b16 a[122:123], v37 offset:49664\n\t"
"s_mov_b32 m0, 130048\n\t"
"s_mov_b32 s74, 3112960\n\t"
"buffer_load_dwordx4 v19, s[92:95], s74 offen lds\n\t"
"ds_read_b64_tr_b16 a[124:125], v37 offset:57344\n\t"
"ds_read_b64_tr_b16 a[126:127], v37 offset:57856\n\t"
"s_waitcnt lgkmcnt(0)\n\t"
"s_waitcnt vmcnt(16)\n\t"
"s_barrier\n\t"
"s_mov_b32 m0, 143360\n\t"
"s_mov_b32 s74, 3244032\n\t"
"buffer_load_dwordx4 v16, s[92:95], s74 offen lds\n\t"
"ds_read_b64_tr_b16 a[128:129], v38 offset:0\n\t"
"ds_read_b64_tr_b16 a[130:131], v38 offset:512\n\t"
"s_mov_b32 m0, 144384\n\t"
"s_mov_b32 s74, 3375104\n\t"
"buffer_load_dwordx4 v17, s[92:95], s74 offen lds\n\t"
"ds_read_b64_tr_b16 a[132:133], v38 offset:8192\n\t"
"ds_read_b64_tr_b16 a[134:135], v38 offset:8704\n\t"
"s_mov_b32 m0, 145408\n\t"
"s_mov_b32 s74, 3506176\n\t"
"buffer_load_dwordx4 v18, s[92:95], s74 offen lds\n\t"
"ds_read_b64_tr_b16 a[192:193], v39 offset:0\n\t"
"ds_read_b64_tr_b16 a[194:195], v39 offset:512\n\t"
"s_mov_b32 m0, 146432\n\t"
"s_mov_b32 s74, 3637248\n\t"
"buffer_load_dwordx4 v19, s[92:95], s74 offen lds\n\t"
"ds_read_b64_tr_b16 a[196:197], v39 offset:8192\n\t"
"ds_read_b64_tr_b16 a[198:199], v39 offset:8704\n\t"
"s_mov_b32 m0, 159744\n\t"
"s_mov_b32 s74, 3768320\n\t"
"buffer_load_dwordx4 v16, s[92:95], s74 offen lds\n\t"
"ds_read_b64_tr_b16 a[136:137], v38 offset:16384\n\t"
"ds_read_b64_tr_b16 a[138:139], v38 offset:16896\n\t"
"s_mov_b32 m0, 160768\n\t"
"s_mov_b32 s74, 3899392\n\t"
"buffer_load_dwordx4 v17, s[92:95], s74 offen lds\n\t"
"ds_read_b64_tr_b16 a[140:141], v38 offset:24576\n\t"
"ds_read_b64_tr_b16 a[142:143], v38 offset:25088\n\t"
"s_mov_b32 m0, 161792\n\t"
"s_mov_b32 s74, 4030464\n\t"
"buffer_load_dwordx4 v18, s[92:95], s74 offen lds\n\t"
"ds_read_b64_tr_b16 a[200:201], v39 offset:16384\n\t"
"ds_read_b64_tr_b16 a[202:203], v39 offset:16896\n\t"
"s_mov_b32 m0, 162816\n\t"
"s_mov_b32 s74, 4161536\n\t"
"buffer_load_dwordx4 v19, s[92:95], s74 offen lds\n\t"
"ds_read_b64_tr_b16 a[204:205], v39 offset:24576\n\t"
"ds_read_b64_tr_b16 a[206:207], v39 offset:25088\n\t"
"s_waitcnt lgkmcnt(0)\n\t"
"s_waitcnt vmcnt(16)\n\t"
"s_barrier\n\t"
"s_mov_b32 m0, 45056\n\t"
"s_mov_b32 s74, 40960\n\t"
"buffer_load_dwordx4 v20, s[96:99], s74 offen lds\n\t"
"ds_read_b64_tr_b16 a[144:145], v38 offset:32768\n\t"
"ds_read_b64_tr_b16 a[146:147], v38 offset:33280\n\t"
"s_mov_b32 m0, 46080\n\t"
"s_mov_b32 s74, 57344\n\t"
"buffer_load_dwordx4 v21, s[96:99], s74 offen lds\n\t"
"ds_read_b64_tr_b16 a[148:149], v38 offset:40960\n\t"
"ds_read_b64_tr_b16 a[150:151], v38 offset:41472\n\t"
"s_mov_b32 m0, 47104\n\t"
"s_mov_b32 s74, 45056\n\t"
"buffer_load_dwordx4 v22, s[96:99], s74 offen lds\n\t"
"ds_read_b64_tr_b16 a[208:209], v39 offset:32768\n\t"
"ds_read_b64_tr_b16 a[210:211], v39 offset:33280\n\t"
"s_mov_b32 m0, 48128\n\t"
"s_mov_b32 s74, 61440\n\t"
"buffer_load_dwordx4 v23, s[96:99], s74 offen lds\n\t"
"ds_read_b64_tr_b16 a[212:213], v39 offset:40960\n\t"
"ds_read_b64_tr_b16 a[214:215], v39 offset:41472\n\t"
"s_mov_b32 m0, 61440\n\t"
"s_mov_b32 s74, 41216\n\t"
"buffer_load_dwordx4 v20, s[96:99], s74 offen lds\n\t"
"ds_read_b64_tr_b16 a[152:153], v38 offset:49152\n\t"
"ds_read_b64_tr_b16 a[154:155], v38 offset:49664\n\t"
"s_mov_b32 m0, 62464\n\t"
"s_mov_b32 s74, 57600\n\t"
"buffer_load_dwordx4 v21, s[96:99], s74 offen lds\n\t"
"ds_read_b64_tr_b16 a[156:157], v38 offset:57344\n\t"
"ds_read_b64_tr_b16 a[158:159], v38 offset:57856\n\t"
"s_mov_b32 m0, 63488\n\t"
"s_mov_b32 s74, 45312\n\t"
"buffer_load_dwordx4 v22, s[96:99], s74 offen lds\n\t"
"ds_read_b64_tr_b16 a[216:217], v39 offset:49152\n\t"
"ds_read_b64_tr_b16 a[218:219], v39 offset:49664\n\t"
"s_mov_b32 m0, 64512\n\t"
"s_mov_b32 s74, 61696\n\t"
"buffer_load_dwordx4 v23, s[96:99], s74 offen lds\n\t"
"ds_read_b64_tr_b16 a[220:221], v39 offset:57344\n\t"
"ds_read_b64_tr_b16 a[222:223], v39 offset:57856\n\t"
"s_waitcnt lgkmcnt(0)\n\t"
"s_waitcnt vmcnt(16)\n\t"
"s_barrier\n\t"
"s_mov_b32 m0, 77824\n\t"
"s_mov_b32 s74, 41472\n\t"
"buffer_load_dwordx4 v20, s[96:99], s74 offen lds\n\t"
"ds_read_b64_tr_b16 a[160:161], v36 offset:0\n\t"
"ds_read_b64_tr_b16 a[162:163], v36 offset:512\n\t"
"s_mov_b32 m0, 78848\n\t"
"s_mov_b32 s74, 57856\n\t"
"buffer_load_dwordx4 v21, s[96:99], s74 offen lds\n\t"
"ds_read_b64_tr_b16 a[164:165], v36 offset:8192\n\t"
"ds_read_b64_tr_b16 a[166:167], v36 offset:8704\n\t"
"s_mov_b32 m0, 79872\n\t"
"s_mov_b32 s74, 45568\n\t"
"buffer_load_dwordx4 v22, s[96:99], s74 offen lds\n\t"
"ds_read_b64_tr_b16 a[224:225], v37 offset:0\n\t"
"ds_read_b64_tr_b16 a[226:227], v37 offset:512\n\t"
"s_mov_b32 m0, 80896\n\t"
"s_mov_b32 s74, 61952\n\t"
"buffer_load_dwordx4 v23, s[96:99], s74 offen lds\n\t"
"ds_read_b64_tr_b16 a[228:229], v37 offset:8192\n\t"
"ds_read_b64_tr_b16 a[230:231], v37 offset:8704\n\t"
"s_mov_b32 m0, 94208\n\t"
"s_mov_b32 s74, 41728\n\t"
"buffer_load_dwordx4 v20, s[96:99], s74 offen lds\n\t"
"ds_read_b64_tr_b16 a[168:169], v36 offset:16384\n\t"
"ds_read_b64_tr_b16 a[170:171], v36 offset:16896\n\t"
"s_mov_b32 m0, 95232\n\t"
"s_mov_b32 s74, 58112\n\t"
"buffer_load_dwordx4 v21, s[96:99], s74 offen lds\n\t"
"ds_read_b64_tr_b16 a[172:173], v36 offset:24576\n\t"
"ds_read_b64_tr_b16 a[174:175], v36 offset:25088\n\t"
"s_mov_b32 m0, 96256\n\t"
"s_mov_b32 s74, 45824\n\t"
"buffer_load_dwordx4 v22, s[96:99], s74 offen lds\n\t"
"ds_read_b64_tr_b16 a[232:233], v37 offset:16384\n\t"
"ds_read_b64_tr_b16 a[234:235], v37 offset:16896\n\t"
"s_mov_b32 m0, 97280\n\t"
"s_mov_b32 s74, 62208\n\t"
"buffer_load_dwordx4 v23, s[96:99], s74 offen lds\n\t"
"ds_read_b64_tr_b16 a[236:237], v37 offset:24576\n\t"
"ds_read_b64_tr_b16 a[238:239], v37 offset:25088\n\t"
"s_waitcnt lgkmcnt(0)\n\t"
"s_waitcnt vmcnt(16)\n\t"
"s_barrier\n\t"
"s_mov_b32 m0, 110592\n\t"
"s_mov_b32 s74, 98304\n\t"
"buffer_load_dwordx4 v16, s[84:87], s74 offen lds\n\t"
"ds_read_b64_tr_b16 a[176:177], v36 offset:32768\n\t"
"ds_read_b64_tr_b16 a[178:179], v36 offset:33280\n\t"
"s_mov_b32 m0, 111616\n\t"
"s_mov_b32 s74, 229376\n\t"
"buffer_load_dwordx4 v17, s[84:87], s74 offen lds\n\t"
"ds_read_b64_tr_b16 a[180:181], v36 offset:40960\n\t"
"ds_read_b64_tr_b16 a[182:183], v36 offset:41472\n\t"
"s_mov_b32 m0, 112640\n\t"
"s_mov_b32 s74, 360448\n\t"
"buffer_load_dwordx4 v18, s[84:87], s74 offen lds\n\t"
"ds_read_b64_tr_b16 a[240:241], v37 offset:32768\n\t"
"ds_read_b64_tr_b16 a[242:243], v37 offset:33280\n\t"
"s_mov_b32 m0, 113664\n\t"
"s_mov_b32 s74, 491520\n\t"
"buffer_load_dwordx4 v19, s[84:87], s74 offen lds\n\t"
"ds_read_b64_tr_b16 a[244:245], v37 offset:40960\n\t"
"ds_read_b64_tr_b16 a[246:247], v37 offset:41472\n\t"
"s_mov_b32 m0, 126976\n\t"
"s_mov_b32 s74, 622592\n\t"
"buffer_load_dwordx4 v16, s[84:87], s74 offen lds\n\t"
"ds_read_b64_tr_b16 a[184:185], v36 offset:49152\n\t"
"ds_read_b64_tr_b16 a[186:187], v36 offset:49664\n\t"
"s_mov_b32 m0, 128000\n\t"
"s_mov_b32 s74, 753664\n\t"
"buffer_load_dwordx4 v17, s[84:87], s74 offen lds\n\t"
"ds_read_b64_tr_b16 a[188:189], v36 offset:57344\n\t"
"ds_read_b64_tr_b16 a[190:191], v36 offset:57856\n\t"
"s_mov_b32 m0, 129024\n\t"
"s_mov_b32 s74, 884736\n\t"
"buffer_load_dwordx4 v18, s[84:87], s74 offen lds\n\t"
"ds_read_b64_tr_b16 a[248:249], v37 offset:49152\n\t"
"ds_read_b64_tr_b16 a[250:251], v37 offset:49664\n\t"
"s_mov_b32 m0, 130048\n\t"
"s_mov_b32 s74, 1015808\n\t"
"buffer_load_dwordx4 v19, s[84:87], s74 offen lds\n\t"
"ds_read_b64_tr_b16 a[252:253], v37 offset:57344\n\t"
"ds_read_b64_tr_b16 a[254:255], v37 offset:57856\n\t"
"s_waitcnt lgkmcnt(0)\n\t"
"s_waitcnt vmcnt(16)\n\t"
"s_barrier\n\t"
"s_mov_b32 m0, 143360\n\t"
"s_mov_b32 s74, 1146880\n\t"
"buffer_load_dwordx4 v16, s[84:87], s74 offen lds\n\t"
"s_mov_b32 m0, 144384\n\t"
"s_mov_b32 s74, 1277952\n\t"
"buffer_load_dwordx4 v17, s[84:87], s74 offen lds\n\t"
"s_mov_b32 m0, 145408\n\t"
"s_mov_b32 s74, 1409024\n\t"
"buffer_load_dwordx4 v18, s[84:87], s74 offen lds\n\t"
"s_mov_b32 m0, 146432\n\t"
"s_mov_b32 s74, 1540096\n\t"
"buffer_load_dwordx4 v19, s[84:87], s74 offen lds\n\t"
"s_mov_b32 m0, 159744\n\t"
"s_mov_b32 s74, 1671168\n\t"
"buffer_load_dwordx4 v16, s[84:87], s74 offen lds\n\t"
"s_mov_b32 m0, 160768\n\t"
"s_mov_b32 s74, 1802240\n\t"
"buffer_load_dwordx4 v17, s[84:87], s74 offen lds\n\t"
"s_mov_b32 m0, 161792\n\t"
"s_mov_b32 s74, 1933312\n\t"
"buffer_load_dwordx4 v18, s[84:87], s74 offen lds\n\t"
"s_mov_b32 m0, 162816\n\t"
"s_mov_b32 s74, 2064384\n\t"
"buffer_load_dwordx4 v19, s[84:87], s74 offen lds\n\t"
"s_waitcnt vmcnt(16)\n\t"
"s_barrier\n\t"
"s_mov_b32 m0, 45056\n\t"
"s_mov_b32 s74, 98304\n\t"
"buffer_load_dwordx4 v16, s[88:91], s74 offen lds\n\t"
"ds_read_b64_tr_b16 v[192:193], v28 offset:0\n\t"
"ds_read_b64_tr_b16 v[194:195], v28 offset:512\n\t"
"ds_read_b64_tr_b16 v[196:197], v28 offset:8192\n\t"
"ds_read_b64_tr_b16 v[198:199], v28 offset:8704\n\t"
"s_mov_b32 m0, 46080\n\t"
"s_mov_b32 s74, 229376\n\t"
"buffer_load_dwordx4 v17, s[88:91], s74 offen lds\n\t"
"ds_read_b64_tr_b16 v[200:201], v29 offset:0\n\t"
"ds_read_b64_tr_b16 v[202:203], v29 offset:512\n\t"
"ds_read_b64_tr_b16 v[204:205], v29 offset:8192\n\t"
"ds_read_b64_tr_b16 v[206:207], v29 offset:8704\n\t"
"s_mov_b32 m0, 47104\n\t"
"s_mov_b32 s74, 360448\n\t"
"buffer_load_dwordx4 v18, s[88:91], s74 offen lds\n\t"
"ds_read_b64_tr_b16 v[208:209], v30 offset:0\n\t"
"ds_read_b64_tr_b16 v[210:211], v30 offset:512\n\t"
"ds_read_b64_tr_b16 v[212:213], v30 offset:8192\n\t"
"ds_read_b64_tr_b16 v[214:215], v30 offset:8704\n\t"
"s_mov_b32 m0, 48128\n\t"
"s_mov_b32 s74, 491520\n\t"
"buffer_load_dwordx4 v19, s[88:91], s74 offen lds\n\t"
"ds_read_b64_tr_b16 v[216:217], v31 offset:0\n\t"
"ds_read_b64_tr_b16 v[218:219], v31 offset:512\n\t"
"ds_read_b64_tr_b16 v[220:221], v31 offset:8192\n\t"
"ds_read_b64_tr_b16 v[222:223], v31 offset:8704\n\t"
"s_mov_b32 m0, 61440\n\t"
"s_mov_b32 s74, 622592\n\t"
"buffer_load_dwordx4 v16, s[88:91], s74 offen lds\n\t"
"ds_read_b64_tr_b16 v[224:225], v32 offset:0\n\t"
"ds_read_b64_tr_b16 v[226:227], v32 offset:512\n\t"
"ds_read_b64_tr_b16 v[228:229], v32 offset:8192\n\t"
"ds_read_b64_tr_b16 v[230:231], v32 offset:8704\n\t"
"s_mov_b32 m0, 62464\n\t"
"s_mov_b32 s74, 753664\n\t"
"buffer_load_dwordx4 v17, s[88:91], s74 offen lds\n\t"
"ds_read_b64_tr_b16 v[232:233], v33 offset:0\n\t"
"ds_read_b64_tr_b16 v[234:235], v33 offset:512\n\t"
"ds_read_b64_tr_b16 v[236:237], v33 offset:8192\n\t"
"ds_read_b64_tr_b16 v[238:239], v33 offset:8704\n\t"
"s_mov_b32 m0, 63488\n\t"
"s_mov_b32 s74, 884736\n\t"
"buffer_load_dwordx4 v18, s[88:91], s74 offen lds\n\t"
"ds_read_b64_tr_b16 v[240:241], v34 offset:0\n\t"
"ds_read_b64_tr_b16 v[242:243], v34 offset:512\n\t"
"ds_read_b64_tr_b16 v[244:245], v34 offset:8192\n\t"
"ds_read_b64_tr_b16 v[246:247], v34 offset:8704\n\t"
"s_mov_b32 m0, 64512\n\t"
"s_mov_b32 s74, 1015808\n\t"
"buffer_load_dwordx4 v19, s[88:91], s74 offen lds\n\t"
"ds_read_b64_tr_b16 v[248:249], v35 offset:0\n\t"
"ds_read_b64_tr_b16 v[250:251], v35 offset:512\n\t"
"ds_read_b64_tr_b16 v[252:253], v35 offset:8192\n\t"
"ds_read_b64_tr_b16 v[254:255], v35 offset:8704\n\t"
"global_load_dwordx2 v[58:59], v57, %[ctl] sc1\n\t"
"s_waitcnt lgkmcnt(0)\n\t"
"s_waitcnt vmcnt(16)\n\t"
"s_barrier\n\t"
"s_mov_b32 m0, 77824\n\t"
"s_mov_b32 s74, 1146880\n\t"
"buffer_load_dwordx4 v16, s[88:91], s74 offen lds\n\t"
"ds_read_b64_tr_b16 v[64:65], v36 offset:0\n\t"
"ds_read_b64_tr_b16 v[66:67], v36 offset:512\n\t"
"s_mov_b32 m0, 78848\n\t"
"s_mov_b32 s74, 1277952\n\t"
"buffer_load_dwordx4 v17, s[88:91], s74 offen lds\n\t"
"ds_read_b64_tr_b16 v[68:69], v36 offset:8192\n\t"
"ds_read_b64_tr_b16 v[70:71], v36 offset:8704\n\t"
"s_mov_b32 m0, 79872\n\t"
"s_mov_b32 s74, 1409024\n\t"
"buffer_load_dwordx4 v18, s[88:91], s74 offen lds\n\t"
"ds_read_b64_tr_b16 a[0:1], v37 offset:0\n\t"
"ds_read_b64_tr_b16 a[2:3], v37 offset:512\n\t"
"s_mov_b32 m0, 80896\n\t"
"s_mov_b32 s74, 1540096\n\t"
"buffer_load_dwordx4 v19, s[88:91], s74 offen lds\n\t"
"ds_read_b64_tr_b16 a[4:5], v37 offset:8192\n\t"
"ds_read_b64_tr_b16 a[6:7], v37 offset:8704\n\t"
"s_mov_b32 m0, 94208\n\t"
"s_mov_b32 s74, 1671168\n\t"
"buffer_load_dwordx4 v16, s[88:91], s74 offen lds\n\t"
"ds_read_b64_tr_b16 v[80:81], v36 offset:16384\n\t"
"ds_read_b64_tr_b16 v[82:83], v36 offset:16896\n\t"
"s_mov_b32 m0, 95232\n\t"
"s_mov_b32 s74, 1802240\n\t"
"buffer_load_dwordx4 v17, s[88:91], s74 offen lds\n\t"
"ds_read_b64_tr_b16 v[84:85], v36 offset:24576\n\t"
"ds_read_b64_tr_b16 v[86:87], v36 offset:25088\n\t"
"s_mov_b32 m0, 96256\n\t"
"s_mov_b32 s74, 1933312\n\t"
"buffer_load_dwordx4 v18, s[88:91], s74 offen lds\n\t"
"ds_read_b64_tr_b16 a[8:9], v37 offset:16384\n\t"
"ds_read_b64_tr_b16 a[10:11], v37 offset:16896\n\t"
"s_mov_b32 m0, 97280\n\t"
"s_mov_b32 s74, 2064384\n\t"
"buffer_load_dwordx4 v19, s[88:91], s74 offen lds\n\t"
"ds_read_b64_tr_b16 a[12:13], v37 offset:24576\n\t"
"ds_read_b64_tr_b16 a[14:15], v37 offset:25088\n\t"
"s_waitcnt lgkmcnt(0)\n\t"
"s_waitcnt vmcnt(16)\n\t"
"s_barrier\n\t"
"ds_read_b64_tr_b16 v[96:97], v36 offset:32768\n\t"
"ds_read_b64_tr_b16 v[98:99], v36 offset:33280\n\t"
"ds_read_b64_tr_b16 v[100:101], v36 offset:40960\n\t"
"ds_read_b64_tr_b16 v[102:103], v36 offset:41472\n\t"
"ds_read_b64_tr_b16 a[16:17], v37 offset:32768\n\t"
"ds_read_b64_tr_b16 a[18:19], v37 offset:33280\n\t"
"ds_read_b64_tr_b16 a[20:21], v37 offset:40960\n\t"
"ds_read_b64_tr_b16 a[22:23], v37 offset:41472\n\t"
"ds_read_b64_tr_b16 v[112:113], v36 offset:49152\n\t"
"ds_read_b64_tr_b16 v[114:115], v36 offset:49664\n\t"
"ds_read_b64_tr_b16 v[116:117], v36 offset:57344\n\t"
"ds_read_b64_tr_b16 v[118:119], v36 offset:57856\n\t"
"ds_read_b64_tr_b16 a[24:25], v37 offset:49152\n\t"
"ds_read_b64_tr_b16 a[26:27], v37 offset:49664\n\t"
"ds_read_b64_tr_b16 a[28:29], v37 offset:57344\n\t"
"ds_read_b64_tr_b16 a[30:31], v37 offset:57856\n\t"
"s_waitcnt lgkmcnt(0)\n\t"
"s_waitcnt vmcnt(8)\n\t"
"v_readfirstlane_b32 s76, v56\n\t"
"s_nop 3\n\t"
"s_add_u32 s76, s76, 1\n\t"
"s_mov_b32 s60, 0\n\t"
"v_mov_b32 v56, 0\n\t"
"s_branch CW3_polc3_%=\n\t"
"CW3_pol1_%=:\n\t"
"global_load_dwordx2 v[58:59], v56, %[ctl] sc1\n\t"
"s_waitcnt vmcnt(0)\n\t"
"CW3_polc3_%=:\n\t"
"v_readfirstlane_b32 s74, v58\n\t"
"v_readfirstlane_b32 s75, v59\n\t"
"s_nop 3\n\t"
"s_cmp_eq_u32 s75, s76\n\t"
"s_cbranch_scc1 CW3_pold2_%=\n\t"
"s_sleep 2\n\t"
"s_add_u32 s60, s60, 1\n\t"
"s_cmp_lt_u32 s60, 0x10000\n\t"
"s_cbranch_scc1 CW3_pol1_%=\n\t"
"CW3_pold2_%=:\n\t"
"s_andn2_b32 s48, s74, 0xfff\n\t"
"s_lshr_b32 s80, s74, 1\n\t"
"s_and_b32 s74, s74, 1\n\t"
"s_lshl_b32 s74, s74, 13\n\t"
"s_mov_b64 s[40:41], %[comm]\n\t"
"s_add_u32 s40, s40, s74\n\t"
"s_addc_u32 s41, s41, 0\n\t"
"s_and_b32 s41, s41, 0xffff\n\t"
"s_mov_b32 s42, 0x1fe000\n\t"
"s_mov_b32 s43, 0x20000\n\t"
"s_add_u32 s75, %[me7], 4096\n\t"
"v_mov_b32 v56, s75\n\t"
"buffer_load_dword v8, v10, s[40:43], 0 offen sc1\n\t"
"s_lshl_b32 s81, %[me14], 1\n\t"
"s_add_u32 s81, s81, 20480\n\t"
"s_lshr_b32 s74, s81, 24\n\t"
"s_lshr_b32 s74, s80, s74\n\t"
"s_lshr_b32 s75, s81, 21\n\t"
"s_xor_b32 s74, s74, s75\n\t"
"s_and_b32 s74, s74, 1\n\t"
"s_lshl_b32 s74, s74, 13\n\t"
"s_or_b32 s76, s81, s74\n\t"
"s_add_u32 s61, s81, 1044480\n\t"
"s_lshr_b32 s74, s61, 24\n\t"
"s_lshr_b32 s74, s80, s74\n\t"
"s_lshr_b32 s75, s61, 21\n\t"
"s_xor_b32 s74, s74, s75\n\t"
"s_and_b32 s74, s74, 1\n\t"
"s_lshl_b32 s74, s74, 13\n\t"
"s_or_b32 s61, s61, s74\n\t"
"s_barrier\n\t"
"ds_read_b64_tr_b16 v[72:73], v38 offset:0\n\t"
"ds_read_b64_tr_b16 v[74:75], v38 offset:512\n\t"
"ds_read_b64_tr_b16 v[76:77], v38 offset:8192\n\t"
"ds_read_b64_tr_b16 v[78:79], v38 offset:8704\n\t"
"ds_read_b64_tr_b16 a[64:65], v39 offset:0\n\t"
"ds_read_b64_tr_b16 a[66:67], v39 offset:512\n\t"
"ds_read_b64_tr_b16 a[68:69], v39 offset:8192\n\t"
"ds_read_b64_tr_b16 a[70:71], v39 offset:8704\n\t"
"ds_read_b64_tr_b16 v[88:89], v38 offset:16384\n\t"
"ds_read_b64_tr_b16 v[90:91], v38 offset:16896\n\t"
"ds_read_b64_tr_b16 v[92:93], v38 offset:24576\n\t"
"ds_read_b64_tr_b16 v[94:95], v38 offset:25088\n\t"
"ds_read_b64_tr_b16 a[72:73], v39 offset:16384\n\t"
"ds_read_b64_tr_b16 a[74:75], v39 offset:16896\n\t"
"ds_read_b64_tr_b16 a[76:77], v39 offset:24576\n\t"
"ds_read_b64_tr_b16 a[78:79], v39 offset:25088\n\t"
"s_waitcnt lgkmcnt(0)\n\t"
"s_waitcnt vmcnt(0)\n\t"
"global_load_dword v57, v56, %[ctl] sc1\n\t"
"s_barrier\n\t"
"ds_read_b64_tr_b16 v[104:105], v38 offset:32768\n\t"
"ds_read_b64_tr_b16 v[106:107], v38 offset:33280\n\t"
"ds_read_b64_tr_b16 v[108:109], v38 offset:40960\n\t"
"ds_read_b64_tr_b16 v[110:111], v38 offset:41472\n\t"
"ds_read_b64_tr_b16 a[80:81], v39 offset:32768\n\t"
"ds_read_b64_tr_b16 a[82:83], v39 offset:33280\n\t"
"ds_read_b64_tr_b16 a[84:85], v39 offset:40960\n\t"
"ds_read_b64_tr_b16 a[86:87], v39 offset:41472\n\t"
"ds_read_b64_tr_b16 v[120:121], v38 offset:49152\n\t"
"ds_read_b64_tr_b16 v[122:123], v38 offset:49664\n\t"
"ds_read_b64_tr_b16 v[124:125], v38 offset:57344\n\t"
"ds_read_b64_tr_b16 v[126:127], v38 offset:57856\n\t"
"ds_read_b64_tr_b16 a[88:89], v39 offset:49152\n\t"
"ds_read_b64_tr_b16 a[90:91], v39 offset:49664\n\t"
"ds_read_b64_tr_b16 a[92:93], v39 offset:57344\n\t"
"ds_read_b64_tr_b16 a[94:95], v39 offset:57856\n\t"
"s_waitcnt lgkmcnt(0)\n\t"
"s_waitcnt vmcnt(0)\n\t"
"s_mov_b32 s60, 0\n\t"
"s_branch CW3_kc6_%=\n\t"
"CW3_kw4_%=:\n\t"
"global_load_dword v57, v56, %[ctl] sc1\n\t"
"s_waitcnt vmcnt(0)\n\t"
"CW3_kc6_%=:\n\t"
"v_readfirstlane_b32 s75, v57\n\t"
"s_nop 3\n\t"
"s_lshr_b32 s75, s75, 24\n\t"
"s_and_b32 s75, s75, 0xff\n\t"
"s_cmp_ge_u32 s75, 16\n\t"
"s_cbranch_scc1 CW3_kd5_%=\n\t"
"s_sleep 2\n\t"
"s_add_u32 s60, s60, 1\n\t"
"s_cmp_lt_u32 s60, 0x8000\n\t"
"s_cbranch_scc1 CW3_kw4_%=\n\t"
"CW3_kd5_%=:\n\t"
"s_barrier\n\t"
"s_mov_b32 m0, 28672\n\t"
"s_nop 0\n\t"
"buffer_load_dwordx4 v0, s[44:47], s76 offen offset:0 sc1 lds\n\t"
"buffer_load_dwordx4 v0, s[44:47], s76 offen offset:1024 sc1 lds\n\t"
"buffer_load_dwordx4 v0, s[44:47], s76 offen offset:2048 sc1 lds\n\t"
"buffer_load_dwordx4 v0, s[44:47], s76 offen offset:3072 sc1 lds\n\t"
"ds_write_b128 v2, v[64:67] offset:0\n\t"
"ds_write_b128 v2, v[68:71] offset:4096\n\t"
"ds_write_b128 v2, v[72:75] offset:8192\n\t"
"ds_write_b128 v2, v[76:79] offset:12288\n\t"
"ds_write_b128 v2, v[80:83] offset:16384\n\t"
"ds_write_b128 v2, v[84:87] offset:20480\n\t"
"ds_write_b128 v2, v[88:91] offset:24576\n\t"
"ds_write_b128 v2, v[92:95] offset:28672\n\t"
"ds_write_b128 v2, v[96:99] offset:32768\n\t"
"ds_write_b128 v2, v[100:103] offset:36864\n\t"
"ds_write_b128 v2, v[104:107] offset:40960\n\t"
"ds_write_b128 v2, v[108:111] offset:45056\n\t"
"ds_write_b128 v2, v[112:115] offset:49152\n\t"
"ds_write_b128 v2, v[116:119] offset:53248\n\t"
"ds_write_b128 v2, v[120:123] offset:57344\n\t"
"ds_write_b128 v2, v[124:127] offset:61440\n\t"
"s_waitcnt lgkmcnt(0)\n\t"
"v_mov_b64 v[28:29], v[48:49]\n\t"
"v_mov_b64 v[30:31], v[50:51]\n\t"
"v_mov_b64 v[44:45], v[52:53]\n\t"
"v_mov_b64 v[46:47], v[54:55]\n\t"
"s_mov_b32 s62, 0xffbfffbf\n\t"
"s_mov_b32 s63, 0xffbfffbf\n\t"
"s_mov_b32 s78, 0x3f803f80\n\t"
"s_mov_b32 s79, 0x3f803f80\n\t"
"s_mov_b32 s38, 0x3e0293ee\n\t"
"s_mov_b32 s39, 0x3e0293ee\n\t"
"s_mov_b32 s72, 0xbe0293ee\n\t"
"s_sub_u32 s77, 100, s48\n\t"
"s_mov_b32 s64, 0\n\t"
"ds_read_b128 v[56:59], v2 offset:49152\n\t"
"ds_read_b128 v[60:63], v2 offset:53248\n\t"
"ds_read_b128 v[72:75], v2 offset:57344\n\t"
"ds_read_b128 v[76:79], v2 offset:61440\n\t"
"ds_read_b128 v[80:83], v2 offset:0\n\t"
"ds_read_b128 v[84:87], v2 offset:4096\n\t"
"ds_read_b128 v[88:91], v2 offset:8192\n\t"
"ds_read_b128 v[92:95], v2 offset:12288\n\t"
"ds_read_b128 v[96:99], v2 offset:16384\n\t"
"ds_read_b128 v[100:103], v2 offset:20480\n\t"
"ds_read_b128 v[104:107], v2 offset:24576\n\t"
"ds_read_b128 v[108:111], v2 offset:28672\n\t"
"ds_read_b128 v[112:115], v2 offset:32768\n\t"
"ds_read_b128 v[116:119], v2 offset:36864\n\t"
"ds_read_b128 v[120:123], v2 offset:40960\n\t"
"ds_read_b128 v[124:127], v2 offset:45056\n\t"
"v_mov_b64 v[48:49], 0\n\t"
"v_mov_b64 v[50:51], 0\n\t"
"v_mov_b64 v[52:53], 0\n\t"
"v_mov_b64 v[54:55], 0\n\t"
"v_mov_b64 v[64:65], 0\n\t"
"v_mov_b64 v[66:67], 0\n\t"
"v_mov_b64 v[68:69], 0\n\t"
"v_mov_b64 v[70:71], 0\n\t"
"s_mov_b32 s65, 0\n\t"
"s_mov_b32 s66, 112\n\t"
"s_mov_b32 s67, s48\n\t"
"s_add_u32 s65, s65, 524288\n\t"
"s_cmp_eq_u32 s65, 1572864\n\t"
"s_cselect_b32 s65, 0, s65\n\t"
"s_lshl_b32 s71, s64, 17\n\t"
"s_and_b32 s68, s64, 1\n\t"
"s_lshl_b32 s68, s68, 9\n\t"
"s_add_u32 s81, s81, 1048576\n\t"
"s_lshr_b32 s74, s81, 24\n\t"
"s_lshr_b32 s74, s80, s74\n\t"
"s_lshr_b32 s75, s81, 21\n\t"
"s_xor_b32 s74, s74, s75\n\t"
"s_and_b32 s74, s74, 1\n\t"
"s_lshl_b32 s74, s74, 13\n\t"
"s_or_b32 s76, s81, s74\n\t"
"s_waitcnt lgkmcnt(0)\n\t"
"s_waitcnt vmcnt(0)\n\t"
"s_mov_b32 s60, 0\n\t"
"CW3_top7_%=:\n\t"
"s_waitcnt vmcnt(1)\n\t"
"CW3_top211_%=:\n\t"
"ds_write_b128 v1, v[28:31] offset:0\n\t"
"v_smfmac_f32_16x16x64_bf16 v[48:51], v[28:31], v[56:63], v9\n\t"
"v_max3_u32 v15, v28, v29, v30\n\t"
"v_smfmac_f32_16x16x64_bf16 v[52:55], v[28:31], v[72:79], v9\n\t"
"v_smfmac_f32_16x16x64_bf16 v[64:67], v[28:31], a[152:159], v9\n\t"
"v_smfmac_f32_16x16x64_bf16 v[68:71], v[28:31], a[216:223], v9\n\t"
"s_waitcnt vmcnt(0)\n\t"
"ds_write_b128 v1, v[44:47] offset:4096\n\t"
"v_max3_u32 v15, v15, v31, v44\n\t"
"v_max3_u32 v15, v15, v45, v46\n\t"
"v_max_u32 v15, v15, v47\n\t"
"v_cmp_le_u32 vcc, s62, v15\n\t"
"s_cbranch_vccnz CW3_art9_%=\n\t"
"CW3_aok10_%=:\n\t"
"v_smfmac_f32_16x16x64_bf16 v[48:51], v[44:47], v[152:159], v9\n\t"
"v_mov_b64 v[56:57], 0\n\t"
"v_smfmac_f32_16x16x64_bf16 v[52:55], v[44:47], v[184:191], v9\n\t"
"v_mov_b64 v[58:59], 0\n\t"
"v_mov_b64 v[60:61], 0\n\t"
"v_mov_b64 v[62:63], 0\n\t"
"s_waitcnt lgkmcnt(0)\n\t"
"s_barrier\n\t"
"ds_read_b128 v[16:19], v0 offset:0\n\t"
"ds_read_b128 v[20:23], v0 offset:1024\n\t"
"v_smfmac_f32_16x16x64_bf16 v[56:59], v[28:31], a[24:31], v9\n\t"
"ds_read_b128 v[24:27], v0 offset:2048\n\t"
"v_smfmac_f32_16x16x64_bf16 v[56:59], v[44:47], a[56:63], v9\n\t"
"ds_read_b128 v[32:35], v0 offset:4096\n\t"
"v_smfmac_f32_16x16x64_bf16 v[60:63], v[28:31], a[88:95], v9\n\t"
"ds_read_b128 v[36:39], v0 offset:5120\n\t"
"v_smfmac_f32_16x16x64_bf16 v[60:63], v[44:47], a[120:127], v9\n\t"
"ds_read_b128 v[40:43], v0 offset:6144\n\t"
"v_smfmac_f32_16x16x64_bf16 v[64:67], v[44:47], a[184:191], v9\n\t"
"s_waitcnt lgkmcnt(4)\n\t"
"v_smfmac_f32_16x16x64_bf16 v[48:51], v[16:19], v[80:87], v9\n\t"
"v_smfmac_f32_16x16x64_bf16 v[52:55], v[16:19], v[88:95], v9\n\t"
"v_smfmac_f32_16x16x64_bf16 v[48:51], v[20:23], v[96:103], v9\n\t"
"v_smfmac_f32_16x16x64_bf16 v[52:55], v[20:23], v[104:111], v9\n\t"
"s_waitcnt lgkmcnt(0)\n\t"
"v_smfmac_f32_16x16x64_bf16 v[48:51], v[24:27], v[112:119], v9\n\t"
"v_smfmac_f32_16x16x64_bf16 v[52:55], v[24:27], v[120:127], v9\n\t"
"v_smfmac_f32_16x16x64_bf16 v[48:51], v[32:35], v[128:135], v9\n\t"
"ds_read_b128 v[72:75], v0 offset:16384\n\t"
"v_smfmac_f32_16x16x64_bf16 v[52:55], v[32:35], v[160:167], v9\n\t"
"ds_read_b128 v[76:79], v0 offset:17408\n\t"
"v_smfmac_f32_16x16x64_bf16 v[48:51], v[36:39], v[136:143], v9\n\t"
"ds_read_b128 v[80:83], v0 offset:18432\n\t"
"v_smfmac_f32_16x16x64_bf16 v[52:55], v[36:39], v[168:175], v9\n\t"
"ds_read_b128 v[84:87], v0 offset:19456\n\t"
"v_smfmac_f32_16x16x64_bf16 v[48:51], v[40:43], v[144:151], v9\n\t"
"ds_read_b128 v[88:91], v0 offset:20480\n\t"
"v_smfmac_f32_16x16x64_bf16 v[52:55], v[40:43], v[176:183], v9\n\t"
"ds_read_b128 v[92:95], v0 offset:21504\n\t"
"v_smfmac_f32_16x16x64_bf16 v[56:59], v[16:19], a[0:7], v9\n\t"
"ds_read_b128 v[96:99], v0 offset:22528\n\t"
"v_smfmac_f32_16x16x64_bf16 v[60:63], v[16:19], a[64:71], v9\n\t"
"ds_read_b128 v[100:103], v0 offset:23552\n\t"
"v_smfmac_f32_16x16x64_bf16 v[56:59], v[20:23], a[8:15], v9\n\t"
"ds_read_b128 v[104:107], v0 offset:30720\n\t"
"v_smfmac_f32_16x16x64_bf16 v[60:63], v[20:23], a[72:79], v9\n\t"
"ds_read_b128 v[108:111], v0 offset:31744\n\t"
"v_smfmac_f32_16x16x64_bf16 v[56:59], v[24:27], a[16:23], v9\n\t"
"v_add_f32 v48, v48, v49\n\t"
"v_smfmac_f32_16x16x64_bf16 v[60:63], v[24:27], a[80:87], v9\n\t"
"v_add_f32 v50, v50, v51\n\t"
"v_smfmac_f32_16x16x64_bf16 v[56:59], v[32:35], a[32:39], v9\n\t"
"v_cvt_pk_bf16_f32 v124, v48, v50\n\t"
"v_smfmac_f32_16x16x64_bf16 v[60:63], v[32:35], a[96:103], v9\n\t"
"v_add_f32 v52, v52, v53\n\t"
"v_smfmac_f32_16x16x64_bf16 v[56:59], v[36:39], a[40:47], v9\n\t"
"v_add_f32 v54, v54, v55\n\t"
"v_smfmac_f32_16x16x64_bf16 v[60:63], v[36:39], a[104:111], v9\n\t"
"v_cvt_pk_bf16_f32 v125, v52, v54\n\t"
"v_smfmac_f32_16x16x64_bf16 v[56:59], v[40:43], a[48:55], v9\n\t"
"ds_write_b64 v4, v[124:125] offset:8192\n\t"
"v_smfmac_f32_16x16x64_bf16 v[60:63], v[40:43], a[112:119], v9\n\t"
"v_smfmac_f32_16x16x64_bf16 v[64:67], v[16:19], a[128:135], v9\n\t"
"s_nop 4\n\t"
"v_add_f32 v56, v56, v57\n\t"
"v_add_f32 v58, v58, v59\n\t"
"v_cvt_pk_bf16_f32 v126, v56, v58\n\t"
"v_add_f32 v60, v60, v61\n\t"
"v_add_f32 v62, v62, v63\n\t"
"v_cvt_pk_bf16_f32 v127, v60, v62\n\t"
"ds_write_b64 v4, v[126:127] offset:8704\n\t"
"v_smfmac_f32_16x16x64_bf16 v[68:71], v[16:19], a[192:199], v9\n\t"
"v_smfmac_f32_16x16x64_bf16 v[64:67], v[20:23], a[136:143], v9\n\t"
"v_smfmac_f32_16x16x64_bf16 v[68:71], v[20:23], a[200:207], v9\n\t"
"v_smfmac_f32_16x16x64_bf16 v[64:67], v[24:27], a[144:151], v9\n\t"
"s_waitcnt lgkmcnt(0)\n\t"
"s_barrier\n\t"
"ds_read_b64_tr_b16 v[48:49], v5 offset:8192\n\t"
"ds_read_b64_tr_b16 v[50:51], v5 offset:8704\n\t"
"ds_read_b64_tr_b16 v[52:53], v5 offset:9216\n\t"
"v_smfmac_f32_16x16x64_bf16 v[68:71], v[24:27], a[208:215], v9\n\t"
"ds_read_b64_tr_b16 v[54:55], v5 offset:9728\n\t"
"v_smfmac_f32_16x16x64_bf16 v[64:67], v[32:35], a[160:167], v9\n\t"
"ds_read_b64_tr_b16 v[56:57], v5 offset:10240\n\t"
"v_smfmac_f32_16x16x64_bf16 v[68:71], v[32:35], a[224:231], v9\n\t"
"ds_read_b64_tr_b16 v[58:59], v5 offset:10752\n\t"
"s_waitcnt lgkmcnt(4)\n\t"
"v_mfma_f32_16x16x32_bf16 v[112:115], v[72:75], v[48:51], 0\n\t"
"ds_read_b64_tr_b16 v[60:61], v5 offset:11264\n\t"
"v_mfma_f32_16x16x32_bf16 v[116:119], v[88:91], v[48:51], 0\n\t"
"ds_read_b64_tr_b16 v[62:63], v5 offset:11776\n\t"
"s_waitcnt lgkmcnt(4)\n\t"
"v_mfma_f32_16x16x32_bf16 v[112:115], v[76:79], v[52:55], v[112:115]\n\t"
"v_mov_b64 v[16:17], 0\n\t"
"v_mov_b64 v[18:19], 0\n\t"
"v_mfma_f32_16x16x32_bf16 v[116:119], v[92:95], v[52:55], v[116:119]\n\t"
"v_mov_b64 v[20:21], 0\n\t"
"s_waitcnt lgkmcnt(2)\n\t"
"v_mfma_f32_16x16x32_bf16 v[112:115], v[80:83], v[56:59], v[112:115]\n\t"
"v_mov_b64 v[22:23], 0\n\t"
"v_mov_b64 v[24:25], 0\n\t"
"v_mfma_f32_16x16x32_bf16 v[116:119], v[96:99], v[56:59], v[116:119]\n\t"
"v_mov_b64 v[26:27], 0\n\t"
"s_waitcnt lgkmcnt(0)\n\t"
"v_mfma_f32_16x16x32_bf16 v[112:115], v[84:87], v[60:63], v[112:115]\n\t"
"v_mov_b64 v[28:29], 0\n\t"
"v_mov_b64 v[30:31], 0\n\t"
"v_mfma_f32_16x16x32_bf16 v[116:119], v[100:103], v[60:63], v[116:119]\n\t"
"v_mov_b32 v81, 0\n\t"
"v_mov_b32 v82, 0\n\t"
"v_mfma_f32_16x16x32_bf16 v[120:123], v[48:51], v[48:51], 0\n\t"
"v_mov_b64 v[124:125], s[78:79]\n\t"
"v_mov_b64 v[126:127], s[78:79]\n\t"
"v_mfma_f32_16x16x32_bf16 v[120:123], v[52:55], v[52:55], v[120:123]\n\t"
"v_max3_f32 v72, v112, v113, v114\n\t"
"v_mfma_f32_16x16x32_bf16 v[120:123], v[56:59], v[56:59], v[120:123]\n\t"
"v_max3_f32 v72, v72, v115, v116\n\t"
"v_mfma_f32_16x16x32_bf16 v[120:123], v[60:63], v[60:63], v[120:123]\n\t"
"v_max3_f32 v72, v72, v117, v118\n\t"
"v_max_f32 v72, v72, v119\n\t"
"v_smfmac_f32_16x16x64_bf16 v[64:67], v[36:39], a[168:175], v9\n\t"
"s_nop 4\n\t"
"v_max3_f32 v72, v72, v122, v123\n\t"
"v_mov_b32 v73, v72\n\t"
"v_smfmac_f32_16x16x64_bf16 v[68:71], v[36:39], a[232:239], v9\n\t"
"s_nop 0\n\t"
"v_permlane32_swap_b32 v72, v73\n\t"
"v_max_f32 v72, v72, v73\n\t"
"v_mov_b32 v73, v72\n\t"
"v_smfmac_f32_16x16x64_bf16 v[64:67], v[40:43], a[176:183], v9\n\t"
"s_nop 0\n\t"
"v_permlane16_swap_b32 v72, v73\n\t"
"v_max_f32 v72, v72, v73\n\t"
"v_mul_f32 v74, s72, v72\n\t"
"v_pk_fma_f32 v[112:113], v[112:113], s[38:39], v[74:75] op_sel_hi:[1,1,0]\n\t"
"v_pk_fma_f32 v[114:115], v[114:115], s[38:39], v[74:75] op_sel_hi:[1,1,0]\n\t"
"v_pk_fma_f32 v[116:117], v[116:117], s[38:39], v[74:75] op_sel_hi:[1,1,0]\n\t"
"v_pk_fma_f32 v[118:119], v[118:119], s[38:39], v[74:75] op_sel_hi:[1,1,0]\n\t"
"v_pk_fma_f32 v[122:123], v[122:123], s[38:39], v[74:75] op_sel_hi:[1,1,0]\n\t"
"v_exp_f32 v112, v112\n\t"
"v_exp_f32 v113, v113\n\t"
"v_smfmac_f32_16x16x64_bf16 v[68:71], v[40:43], a[240:247], v9\n\t"
"v_exp_f32 v114, v114\n\t"
"v_exp_f32 v115, v115\n\t"
"v_exp_f32 v116, v116\n\t"
"v_smfmac_f32_16x16x64_bf16 v[68:71], v[44:47], a[248:255], v9\n\t"
"v_exp_f32 v117, v117\n\t"
"v_exp_f32 v118, v118\n\t"
"v_exp_f32 v119, v119\n\t"
"v_cvt_pk_bf16_f32 v76, v112, v113\n\t"
"v_cvt_pk_bf16_f32 v77, v114, v115\n\t"
"v_cvt_pk_bf16_f32 v78, v116, v117\n\t"
"v_cvt_pk_bf16_f32 v79, v118, v119\n\t"
"v_exp_f32 v122, v122\n\t"
"v_exp_f32 v123, v123\n\t"
"v_mfma_f32_16x16x32_bf16 v[72:75], v[124:127], v[76:79], 0\n\t"
"v_cvt_pk_bf16_f32 v80, v122, v123\n\t"
"v_cvt_pk_bf16_f32 v83, v122, v123\n\t"
"v_mfma_f32_16x16x32_bf16 v[48:51], v[104:107], v[76:79], 0\n\t"
"v_add_f32 v64, v64, v65\n\t"
"v_add_f32 v66, v66, v67\n\t"
"v_mfma_f32_16x16x16_bf16 v[72:75], v[124:125], v[80:81], v[72:75]\n\t"
"v_add_f32 v68, v68, v69\n\t"
"v_add_f32 v70, v70, v71\n\t"
"v_cvt_pk_bf16_f32 v84, v64, v66\n\t"
"v_cvt_pk_bf16_f32 v85, v68, v70\n\t"
"v_mfma_f32_16x16x32_bf16 v[52:55], v[108:111], v[76:79], 0\n\t"
"v_mov_b64 v[64:65], v[84:85]\n\t"
"v_mfma_f32_16x16x16_bf16 v[48:51], v[84:85], v[80:81], v[48:51]\n\t"
"v_mfma_f32_16x16x16_bf16 v[52:55], v[84:85], v[82:83], v[52:55]\n\t"
"v_rcp_f32 v124, v72\n\t"
"s_nop 5\n\t"
"v_pk_mul_f32 v[48:49], v[48:49], v[124:125] op_sel_hi:[1,0]\n\t"
"v_pk_mul_f32 v[50:51], v[50:51], v[124:125] op_sel_hi:[1,0]\n\t"
"v_pk_mul_f32 v[52:53], v[52:53], v[124:125] op_sel_hi:[1,0]\n\t"
"v_pk_mul_f32 v[54:55], v[54:55], v[124:125] op_sel_hi:[1,0]\n\t"
"v_cvt_pk_bf16_f32 v120, v48, v49\n\t"
"v_cvt_pk_bf16_f32 v121, v50, v51\n\t"
"v_cvt_pk_bf16_f32 v122, v52, v53\n\t"
"v_cvt_pk_bf16_f32 v123, v54, v55\n\t"
"ds_write_b128 v6, v[120:123] offset:12288\n\t"
"s_cmp_eq_u32 s64, 63\n\t"
"s_cbranch_scc1 CW3_fin8_%=\n\t"
"s_waitcnt lgkmcnt(0)\n\t"
"s_barrier\n\t"
"ds_read_b128 v[120:123], v0 offset:12288\n\t"
"ds_read_b128 v[124:127], v0 offset:13312\n\t"
"s_waitcnt lgkmcnt(1)\n\t"
"v_smfmac_f32_16x16x64_bf16 v[16:19], v[120:123], v[192:199], v9\n\t"
"v_smfmac_f32_16x16x64_bf16 v[20:23], v[120:123], v[200:207], v9\n\t"
"s_waitcnt lgkmcnt(0)\n\t"
"v_smfmac_f32_16x16x64_bf16 v[16:19], v[124:127], v[224:231], v9\n\t"
"s_nop 0\n\t"
"v_smfmac_f32_16x16x64_bf16 v[20:23], v[124:127], v[232:239], v9\n\t"
"s_nop 0\n\t"
"v_smfmac_f32_16x16x64_bf16 v[24:27], v[120:123], v[208:215], v9\n\t"
"s_nop 0\n\t"
"v_smfmac_f32_16x16x64_bf16 v[28:31], v[120:123], v[216:223], v9\n\t"
"s_nop 0\n\t"
"v_smfmac_f32_16x16x64_bf16 v[24:27], v[124:127], v[240:247], v9\n\t"
"v_add_f32 v16, v16, v17\n\t"
"v_smfmac_f32_16x16x64_bf16 v[28:31], v[124:127], v[248:255], v9\n\t"
"v_add_f32 v18, v18, v19\n\t"
"v_cvt_pk_bf16_f32 v68, v16, v18\n\t"
"v_add_f32 v20, v20, v21\n\t"
"v_add_f32 v22, v22, v23\n\t"
"v_cvt_pk_bf16_f32 v69, v20, v22\n\t"
"s_nop 0\n\t"
"v_add_f32 v24, v24, v25\n\t"
"v_add_f32 v26, v26, v27\n\t"
"v_cvt_pk_bf16_f32 v70, v24, v26\n\t"
"v_add_f32 v28, v28, v29\n\t"
"v_add_f32 v30, v30, v31\n\t"
"v_cvt_pk_bf16_f32 v71, v28, v30\n\t"
"buffer_store_dwordx4 v[68:71], v7, s[40:43], s65 offen sc1\n\t"
"s_memrealtime s[74:75]\n\t"
"s_waitcnt lgkmcnt(0)\n\t"
"s_mov_b32 s59, s74\n\t"
"ds_read_b64_tr_b16 v[68:69], v5 offset:11264\n\t"
"ds_read_b64_tr_b16 v[70:71], v5 offset:11776\n\t"
"s_and_b32 s74, s64, 3\n\t"
"s_lshl_b32 s74, s74, 16\n\t"
"s_add_u32 s69, s74, 1790976\n\t"
"s_add_u32 s70, s74, 1825792\n\t"
"buffer_load_dword v8, v10, s[40:43], s68 offen sc1\n\t"
"buffer_load_dwordx4 v0, s[44:47], s76 offen offset:0 sc1 lds\n\t"
"buffer_load_dwordx4 v0, s[44:47], s76 offen offset:1024 sc1 lds\n\t"
"buffer_load_dwordx4 v0, s[44:47], s76 offen offset:2048 sc1 lds\n\t"
"buffer_load_dwordx4 v0, s[44:47], s76 offen offset:3072 sc1 lds\n\t"
"ds_read_b128 v[56:59], v2 offset:49152\n\t"
"ds_read_b128 v[60:63], v2 offset:53248\n\t"
"ds_read_b128 v[72:75], v2 offset:57344\n\t"
"ds_read_b128 v[76:79], v2 offset:61440\n\t"
"ds_read_b128 v[80:83], v2 offset:0\n\t"
"ds_read_b128 v[84:87], v2 offset:4096\n\t"
"ds_read_b128 v[88:91], v2 offset:8192\n\t"
"ds_read_b128 v[92:95], v2 offset:12288\n\t"
"ds_read_b128 v[96:99], v2 offset:16384\n\t"
"ds_read_b128 v[100:103], v2 offset:20480\n\t"
"ds_read_b128 v[104:107], v2 offset:24576\n\t"
"ds_read_b128 v[108:111], v2 offset:28672\n\t"
"ds_read_b128 v[112:115], v2 offset:32768\n\t"
"ds_read_b128 v[116:119], v2 offset:36864\n\t"
"ds_read_b128 v[120:123], v2 offset:40960\n\t"
"ds_read_b128 v[124:127], v2 offset:45056\n\t"
"buffer_store_dwordx2 v[64:65], v12, s[52:55], s71 offen offset:3584 sc1\n\t"
"s_waitcnt lgkmcnt(15)\n\t"
"buffer_store_dwordx4 v[68:71], v11, s[52:55], s71 offen offset:1536 sc1\n\t"
"s_add_u32 s74, %[me7], 40960\n\t"
"v_mov_b32 v48, s74\n\t"
"v_mov_b32 v49, s64\n\t"
"s_cmp_eq_u32 %[kmsz], 0\n\t"
"s_cbranch_scc1 CW3_np12_%=\n\t"
"s_mov_b64 exec, 1\n\t"
"global_store_dword v48, v49, %[ctl] sc1\n\t"
"s_mov_b64 exec, -1\n\t"
"CW3_np12_%=:\n\t"
"s_add_u32 s74, s64, 2\n\t"
"s_cmp_eq_u32 s74, 4\n\t"
"s_cselect_b32 s74, 0, s74\n\t"
"s_and_b32 s75, s74, 0x47\n\t"
"s_cbranch_scc1 CW3_sk13_%=\n\t"
"s_lshr_b32 s74, s74, 3\n\t"
"s_mov_b32 s60, 0\n\t"
"s_lshl_b32 s75, s74, 12\n\t"
"s_add_u32 s75, s75, %[me7]\n\t"
"s_add_u32 s75, s75, 4096\n\t"
"v_mov_b32 v48, s75\n\t"
"CW3_kw14_%=:\n\t"
"global_load_dword v49, v48, %[ctl] sc1\n\t"
"s_waitcnt vmcnt(0)\n\t"
"CW3_kc16_%=:\n\t"
"v_readfirstlane_b32 s75, v49\n\t"
"s_nop 3\n\t"
"s_and_b32 s75, s75, 0xff\n\t"
"s_cmp_ge_u32 s75, 16\n\t"
"s_cbranch_scc1 CW3_kd15_%=\n\t"
"s_sleep 2\n\t"
"s_add_u32 s60, s60, 1\n\t"
"s_cmp_lt_u32 s60, 0x8000\n\t"
"s_cbranch_scc1 CW3_kw14_%=\n\t"
"CW3_kd15_%=:\n\t"
"CW3_sk13_%=:\n\t"
"v_mov_b64 v[48:49], 0\n\t"
"v_mov_b64 v[50:51], 0\n\t"
"v_mov_b64 v[52:53], 0\n\t"
"v_mov_b64 v[54:55], 0\n\t"
"v_mov_b64 v[64:65], 0\n\t"
"v_mov_b64 v[66:67], 0\n\t"
"v_mov_b64 v[68:69], 0\n\t"
"v_mov_b64 v[70:71], 0\n\t"
"s_add_u32 s64, s64, 1\n\t"
"s_add_u32 s65, s65, 524288\n\t"
"s_cmp_eq_u32 s65, 1572864\n\t"
"s_cselect_b32 s65, 0, s65\n\t"
"s_lshl_b32 s71, s64, 17\n\t"
"s_and_b32 s68, s64, 1\n\t"
"s_lshl_b32 s68, s68, 9\n\t"
"s_add_u32 s81, s81, 1048576\n\t"
"s_lshr_b32 s74, s81, 24\n\t"
"s_lshr_b32 s74, s80, s74\n\t"
"s_lshr_b32 s75, s81, 21\n\t"
"s_xor_b32 s74, s74, s75\n\t"
"s_and_b32 s74, s74, 1\n\t"
"s_lshl_b32 s74, s74, 13\n\t"
"s_or_b32 s76, s81, s74\n\t"
"s_waitcnt vmcnt(6)\n\t"
"v_max_i32_dpp v8, v8, v8 row_shl:4 row_mask:0xf bank_mask:0xf\n\t"
"s_nop 0\n\t"
"v_readlane_b32 s74, v8, 0\n\t"
"v_readlane_b32 s75, v8, 1\n\t"
"v_readlane_b32 s50, v8, 2\n\t"
"v_readlane_b32 s51, v8, 3\n\t"
"s_max_i32 s74, s74, s75\n\t"
"s_max_i32 s50, s50, s51\n\t"
"s_max_i32 s74, s74, s50\n\t"
"s_add_u32 s58, s74, s48\n\t"
"s_sub_i32 s74, s58, s59\n\t"
"s_min_i32 s74, s74, 60\n\t"
"s_max_i32 s74, s74, -2\n\t"
"s_add_u32 s58, s59, s74\n\t"
"s_waitcnt lgkmcnt(0)\n\t"
"s_add_u32 s75, s58, %[dC]\n\t"
"CW3_sp17_%=:\n\t"
"s_memrealtime s[50:51]\n\t"
"s_waitcnt lgkmcnt(0)\n\t"
"s_sub_i32 s60, s75, s50\n\t"
"s_cmp_gt_i32 s60, 4\n\t"
"s_cbranch_scc1 CW3_sp17_%=\n\t"
"s_cmp_lt_i32 s60, 2\n\t"
"s_cbranch_scc1 CW3_sd18_%=\n\t"
"s_sub_u32 s60, s60, 2\n\t"
"CW3_fd19_%=:\n\t"
"s_sub_u32 s60, s60, 1\n\t"
"s_cbranch_scc0 CW3_fd19_%=\n\t"
"CW3_sd18_%=:\n\t"
"buffer_load_dwordx4 v[28:31], v0, s[40:43], s69 offen sc1\n\t"
"buffer_load_dwordx4 v[44:47], v0, s[40:43], s70 offen sc1\n\t"
"s_mov_b32 s60, 0\n\t"
"s_branch CW3_top7_%=\n\t"
"CW3_art9_%=:\n\t"
"v_cmp_eq_u32 vcc, s62, v15\n\t"
"s_cbranch_vccnz CW3_go20_%=\n\t"
"s_mov_b64 s[74:75], 0\n\t"
"v_cmp_eq_u32 vcc, s62, v28\n\t"
"s_nop 1\n\t"
"s_or_b64 s[74:75], s[74:75], vcc\n\t"
"v_cmp_eq_u32 vcc, s62, v29\n\t"
"s_nop 1\n\t"
"s_or_b64 s[74:75], s[74:75], vcc\n\t"
"v_cmp_eq_u32 vcc, s62, v30\n\t"
"s_nop 1\n\t"
"s_or_b64 s[74:75], s[74:75], vcc\n\t"
"v_cmp_eq_u32 vcc, s62, v31\n\t"
"s_nop 1\n\t"
"s_or_b64 s[74:75], s[74:75], vcc\n\t"
"v_cmp_eq_u32 vcc, s62, v44\n\t"
"s_nop 1\n\t"
"s_or_b64 s[74:75], s[74:75], vcc\n\t"
"v_cmp_eq_u32 vcc, s62, v45\n\t"
"s_nop 1\n\t"
"s_or_b64 s[74:75], s[74:75], vcc\n\t"
"v_cmp_eq_u32 vcc, s62, v46\n\t"
"s_nop 1\n\t"
"s_or_b64 s[74:75], s[74:75], vcc\n\t"
"v_cmp_eq_u32 vcc, s62, v47\n\t"
"s_nop 1\n\t"
"s_or_b64 s[74:75], s[74:75], vcc\n\t"
"s_cbranch_scc0 CW3_aok10_%=\n\t"
"CW3_go20_%=:\n\t"
"s_add_u32 s60, s60, 1\n\t"
"s_cmp_ge_u32 s60, 0x4000\n\t"
"s_cbranch_scc1 CW3_aok10_%=\n\t"
"s_waitcnt lgkmcnt(0)\n\t"
"buffer_load_dwordx4 v[28:31], v0, s[40:43], s69 offen sc1\n\t"
"buffer_load_dwordx4 v[44:47], v0, s[40:43], s70 offen sc1\n\t"
"v_mov_b64 v[48:49], 0\n\t"
"v_mov_b64 v[50:51], 0\n\t"
"v_mov_b64 v[52:53], 0\n\t"
"v_mov_b64 v[54:55], 0\n\t"
"v_mov_b64 v[64:65], 0\n\t"
"v_mov_b64 v[66:67], 0\n\t"
"v_mov_b64 v[68:69], 0\n\t"
"v_mov_b64 v[70:71], 0\n\t"
"s_branch CW3_top7_%=\n\t"
"CW3_fin8_%=:\n\t"
"s_waitcnt lgkmcnt(0)\n\t"
"s_nop 9\n\t"
"ds_read_b64_tr_b16 v[68:69], v5 offset:11264\n\t"
"ds_read_b64_tr_b16 v[70:71], v5 offset:11776\n\t"
"buffer_store_dwordx2 v[64:65], v12, s[52:55], s71 offen offset:3584 sc1\n\t"
"s_waitcnt lgkmcnt(0)\n\t"
"buffer_store_dwordx4 v[68:71], v11, s[52:55], s71 offen offset:1536 sc1\n\t"
"s_waitcnt vmcnt(0)\n\t"
"s_barrier\n\t"
"s_add_u32 s74, %[me7], 40960\n\t"
"v_mov_b32 v48, s74\n\t"
"v_mov_b32 v49, 64\n\t"
"s_cmp_eq_u32 %[kmsz], 0\n\t"
"s_cbranch_scc1 CW3_np21_%=\n\t"
"s_mov_b64 exec, 1\n\t"
"global_store_dword v48, v49, %[ctl] sc1\n\t"
"s_mov_b64 exec, -1\n\t"
"CW3_np21_%=:\n\t"
"s_branch CHAIN_END_%=\n\t"
"CHAIN_END_%=:\n\t"
"s_waitcnt vmcnt(0) lgkmcnt(0)\n\t"
"s_endpgm\n\t"
"KMAIN_CODE_END:\n\t"
    :
    : [w] "s"(w), [wq] "s"(p.Wq + me * DH), [wk] "s"(p.Wk + me * DH), [wv] "s"(p.Wv + me * DH), [wo] "s"(p.Wo + (size_t)me * DH * DM + tw * 64), [kmsz] "s"(tw ? 0u : 0x800000u - 31u * 4096u),
      [mem0] "s"(p.mem0), [kv] "s"(ws + W_FAR), [kmv] "s"(ws + W_KMV + me * 4096),
      [comm] "s"(ws + W_COMM), [ctl] "s"(ws + W_CTL), [me14] "s"(me << 14), [me8] "s"(me << 8), [me7] "s"(me << 7), [dC] "s"(p.dC)
    : "memory", "vcc", "scc", "v255", "a255", CLOB_S);
  __builtin_unreachable();
}

DI void reducer_role(const Params& p, unsigned char* ws, int me, unsigned tag) {
  const int lane = lane_id(), g = lane >> 4, c = lane & 15;
  const unsigned one = c < 8 ? 0x3F80u << (16 * (c >> 2)) : 0u;
  const u4 sel = {(c & 3) == 0 ? one : 0u, (c & 3) == 1 ? one : 0u, (c & 3) == 2 ? one : 0u, (c & 3) == 3 ? one : 0u};
  const int t = me >> 2;
  const unsigned mo = (108 + (t >> 1)) * 16384u + (t & 1) * 4096u + ((t >> 1) & 3) * 1024u + (me & 3) * 256u;
  const unsigned stm = c < 8 ? mo + g * 64u + c * 8u : 0x7FFF0000u;
  const unsigned mw = lane < 8 ? 105 * 16384 + lane * 1024 : 0x7FFF0000;
  asm volatile(
"s_mov_b32 s60, 0\n\t"
"v_mov_b32 v104, 0\n\t"
"RD_pol1_%=:\n\t"
"global_load_dwordx2 v[106:107], v104, %[ctl] sc1\n\t"
"s_waitcnt vmcnt(0)\n\t"
"RD_polc3_%=:\n\t"
"v_readfirstlane_b32 s74, v106\n\t"
"v_readfirstlane_b32 s75, v107\n\t"
"s_nop 3\n\t"
"s_cmp_eq_u32 s75, %[tag]\n\t"
"s_cbranch_scc1 RD_pold2_%=\n\t"
"s_sleep 2\n\t"
"s_add_u32 s60, s60, 1\n\t"
"s_cmp_lt_u32 s60, 0x10000\n\t"
"s_cbranch_scc1 RD_pol1_%=\n\t"
"RD_pold2_%=:\n\t"
"s_andn2_b32 s48, s74, 0xfff\n\t"
"s_lshr_b32 s80, s74, 1\n\t"
"s_and_b32 s74, s74, 1\n\t"
"s_lshl_b32 s74, s74, 13\n\t"
"s_mov_b64 s[40:41], %[comm]\n\t"
"s_add_u32 s40, s40, s74\n\t"
"s_addc_u32 s41, s41, 0\n\t"
"s_and_b32 s41, s41, 0xffff\n\t"
"s_mov_b32 s42, 0x1fe000\n\t"
"s_mov_b32 s43, 0x20000\n\t"
"s_mov_b32 s62, 0xffbfffbf\n\t"
"s_mov_b32 s63, 0xffbfffbf\n\t"
"s_mov_b32 s56, 0xffff\n\t"
"s_mov_b32 s57, 0\n\t"
"v_mov_b64 v[100:101], s[62:63]\n\t"
"v_mov_b64 v[102:103], s[62:63]\n\t"
"s_mov_b32 s64, 0\n\t"
"s_mov_b32 s65, 0\n\t"
"s_mov_b32 s49, 0\n\t"
"RD_top4_%=:\n\t"
"s_add_u32 s65, s65, 524288\n\t"
"s_cmp_eq_u32 s65, 1572864\n\t"
"s_cselect_b32 s65, 0, s65\n\t"
"s_add_u32 s67, s65, %[me14]\n\t"
"s_add_u32 s68, s67, 4096\n\t"
"s_and_b32 s74, s64, 1\n\t"
"s_lshl_b32 s71, s74, 9\n\t"
"s_and_b32 s74, s64, 3\n\t"
"s_lshl_b32 s69, s74, 16\n\t"
"s_xor_b32 s73, s69, 131072\n\t"
"s_add_u32 s73, s73, %[me8]\n\t"
"s_mov_b32 s76, 0\n\t"
"RD_mp5_%=:\n\t"
"buffer_load_dword v110, %[mw], s[40:43], s71 offen sc1\n\t"
"s_add_u32 s76, s76, 1\n\t"
"s_cmp_gt_u32 s76, 400\n\t"
"s_cbranch_scc1 RD_fire6_%=\n\t"
"s_waitcnt vmcnt(0)\n\t"
"s_memrealtime s[50:51]\n\t"
"v_max_i32_dpp v110, v110, v110 row_shl:4 row_mask:0xf bank_mask:0xf\n\t"
"s_nop 0\n\t"
"v_readlane_b32 s74, v110, 0\n\t"
"v_readlane_b32 s75, v110, 1\n\t"
"v_readlane_b32 s58, v110, 2\n\t"
"v_readlane_b32 s59, v110, 3\n\t"
"s_nop 1\n\t"
"s_max_i32 s74, s74, s75\n\t"
"s_max_i32 s58, s58, s59\n\t"
"s_max_i32 s58, s58, s74\n\t"
"s_sub_i32 s74, s58, s49\n\t"
"s_waitcnt lgkmcnt(0)\n\t"
"s_cmp_lt_i32 s74, 60\n\t"
"s_cbranch_scc1 RD_slp10_%=\n\t"
"s_sub_u32 s50, s50, s48\n\t"
"s_sub_i32 s74, s58, s50\n\t"
"s_cmp_gt_i32 s74, %[dA]\n\t"
"s_cbranch_scc1 RD_mp5_%=\n\t"
"s_add_u32 s58, s58, s48\n\t"
"s_add_u32 s75, s58, %[dB]\n\t"
"RD_sp11_%=:\n\t"
"s_memrealtime s[50:51]\n\t"
"s_waitcnt lgkmcnt(0)\n\t"
"s_sub_i32 s60, s75, s50\n\t"
"s_cmp_gt_i32 s60, 4\n\t"
"s_cbranch_scc1 RD_sp11_%=\n\t"
"s_cmp_lt_i32 s60, 2\n\t"
"s_cbranch_scc1 RD_sd12_%=\n\t"
"s_sub_u32 s60, s60, 2\n\t"
"RD_fd13_%=:\n\t"
"s_sub_u32 s60, s60, 1\n\t"
"s_cbranch_scc0 RD_fd13_%=\n\t"
"RD_sd12_%=:\n\t"
"RD_fire6_%=:\n\t"
"s_mov_b32 s60, 0\n\t"
"RD_again9_%=:\n\t"
"buffer_load_dwordx4 v[64:67], %[l16], s[40:43], s67 offen offset:0 sc1\n\t"
"buffer_load_dwordx4 v[68:71], %[l16], s[40:43], s67 offen offset:1024 sc1\n\t"
"buffer_load_dwordx4 v[72:75], %[l16], s[40:43], s67 offen offset:2048 sc1\n\t"
"buffer_load_dwordx4 v[76:79], %[l16], s[40:43], s67 offen offset:3072 sc1\n\t"
"buffer_load_dwordx4 v[80:83], %[l16], s[40:43], s68 offen offset:0 sc1\n\t"
"buffer_load_dwordx4 v[84:87], %[l16], s[40:43], s68 offen offset:1024 sc1\n\t"
"buffer_load_dwordx4 v[88:91], %[l16], s[40:43], s68 offen offset:2048 sc1\n\t"
"buffer_load_dwordx4 v[92:95], %[l16], s[40:43], s68 offen offset:3072 sc1\n\t"
"s_waitcnt vmcnt(7)\n\t"
"v_mfma_f32_16x16x32_bf16 v[104:107], v[64:67], %[sel], 0\n\t"
"s_waitcnt vmcnt(6)\n\t"
"v_mfma_f32_16x16x32_bf16 v[104:107], v[68:71], %[sel], v[104:107]\n\t"
"s_waitcnt vmcnt(5)\n\t"
"v_mfma_f32_16x16x32_bf16 v[104:107], v[72:75], %[sel], v[104:107]\n\t"
"s_waitcnt vmcnt(4)\n\t"
"v_mfma_f32_16x16x32_bf16 v[104:107], v[76:79], %[sel], v[104:107]\n\t"
"s_waitcnt vmcnt(3)\n\t"
"v_mfma_f32_16x16x32_bf16 v[104:107], v[80:83], %[sel], v[104:107]\n\t"
"s_waitcnt vmcnt(2)\n\t"
"v_mfma_f32_16x16x32_bf16 v[104:107], v[84:87], %[sel], v[104:107]\n\t"
"s_waitcnt vmcnt(1)\n\t"
"v_mfma_f32_16x16x32_bf16 v[104:107], v[88:91], %[sel], v[104:107]\n\t"
"s_waitcnt vmcnt(0)\n\t"
"v_mfma_f32_16x16x32_bf16 v[104:107], v[92:95], %[sel], v[104:107]\n\t"
"s_nop 7\n\t"
"v_cmp_u_f32 vcc, v104, v105\n\t"
"v_cmp_u_f32 s[74:75], v106, v107\n\t"
"v_cvt_pk_bf16_f32 v108, v104, v105\n\t"
"v_cvt_pk_bf16_f32 v109, v106, v107\n\t"
"v_cndmask_b32 v98, v108, v100, vcc\n\t"
"v_cndmask_b32 v99, v109, v100, s[74:75]\n\t"
"buffer_store_dwordx2 v[98:99], %[stm], s[40:43], s69 offen sc1\n\t"
"s_or_b64 vcc, vcc, s[74:75]\n\t"
"s_cbranch_scc1 RD_slow8_%=\n\t"
"RD_done14_%=:\n\t"
"s_memrealtime s[50:51]\n\t"
"s_waitcnt lgkmcnt(0)\n\t"
"s_sub_u32 s49, s50, s48\n\t"
"s_sleep 16\n\t"
"buffer_store_dwordx4 v[100:103], %[l16], s[40:43], s67 offen offset:0 sc1\n\t"
"buffer_store_dwordx4 v[100:103], %[l16], s[40:43], s67 offen offset:1024 sc1\n\t"
"buffer_store_dwordx4 v[100:103], %[l16], s[40:43], s67 offen offset:2048 sc1\n\t"
"buffer_store_dwordx4 v[100:103], %[l16], s[40:43], s67 offen offset:3072 sc1\n\t"
"buffer_store_dwordx4 v[100:103], %[l16], s[40:43], s68 offen offset:0 sc1\n\t"
"buffer_store_dwordx4 v[100:103], %[l16], s[40:43], s68 offen offset:1024 sc1\n\t"
"buffer_store_dwordx4 v[100:103], %[l16], s[40:43], s68 offen offset:2048 sc1\n\t"
"buffer_store_dwordx4 v[100:103], %[l16], s[40:43], s68 offen offset:3072 sc1\n\t"
"s_mov_b64 exec, s[56:57]\n\t"
"buffer_store_dwordx4 v[100:103], %[l16], s[40:43], s73 offen sc1\n\t"
"s_mov_b64 exec, -1\n\t"
"s_sleep 16\n\t"
"s_add_u32 s64, s64, 1\n\t"
"s_cmp_lt_u32 s64, 63\n\t"
"s_cbranch_scc1 RD_top4_%=\n\t"
"s_endpgm\n\t"
"RD_ok7_%=:\n\t"
"s_waitcnt vmcnt(0)\n\t"
"buffer_store_dwordx2 v[108:109], %[stm], s[40:43], s69 offen sc1\n\t"
"s_branch RD_done14_%=\n\t"
"RD_slp10_%=:\n\t"
"s_sleep 1\n\t"
"s_branch RD_mp5_%=\n\t"
"RD_slow8_%=:\n\t"
"v_max3_u32 v110, v64, v65, v66\n\t"
"v_max_u32 v110, v110, v67\n\t"
"v_max3_u32 v110, v110, v68, v69\n\t"
"v_max3_u32 v110, v110, v70, v71\n\t"
"v_max3_u32 v110, v110, v72, v73\n\t"
"v_max3_u32 v110, v110, v74, v75\n\t"
"v_max3_u32 v110, v110, v76, v77\n\t"
"v_max3_u32 v110, v110, v78, v79\n\t"
"v_max3_u32 v110, v110, v80, v81\n\t"
"v_max3_u32 v110, v110, v82, v83\n\t"
"v_max3_u32 v110, v110, v84, v85\n\t"
"v_max3_u32 v110, v110, v86, v87\n\t"
"v_max3_u32 v110, v110, v88, v89\n\t"
"v_max3_u32 v110, v110, v90, v91\n\t"
"v_max3_u32 v110, v110, v92, v93\n\t"
"v_max3_u32 v110, v110, v94, v95\n\t"
"v_cmp_eq_u32 vcc, s62, v110\n\t"
"s_cbranch_vccnz RD_go15_%=\n\t"
"s_mov_b64 s[74:75], 0\n\t"
"v_cmp_eq_u32 vcc, s62, v64\n\t"
"s_nop 1\n\t"
"s_or_b64 s[74:75], s[74:75], vcc\n\t"
"v_cmp_eq_u32 vcc, s62, v65\n\t"
"s_nop 1\n\t"
"s_or_b64 s[74:75], s[74:75], vcc\n\t"
"v_cmp_eq_u32 vcc, s62, v66\n\t"
"s_nop 1\n\t"
"s_or_b64 s[74:75], s[74:75], vcc\n\t"
"v_cmp_eq_u32 vcc, s62, v67\n\t"
"s_nop 1\n\t"
"s_or_b64 s[74:75], s[74:75], vcc\n\t"
"v_cmp_eq_u32 vcc, s62, v68\n\t"
"s_nop 1\n\t"
"s_or_b64 s[74:75], s[74:75], vcc\n\t"
"v_cmp_eq_u32 vcc, s62, v69\n\t"
"s_nop 1\n\t"
"s_or_b64 s[74:75], s[74:75], vcc\n\t"
"v_cmp_eq_u32 vcc, s62, v70\n\t"
"s_nop 1\n\t"
"s_or_b64 s[74:75], s[74:75], vcc\n\t"
"v_cmp_eq_u32 vcc, s62, v71\n\t"
"s_nop 1\n\t"
"s_or_b64 s[74:75], s[74:75], vcc\n\t"
"v_cmp_eq_u32 vcc, s62, v72\n\t"
"s_nop 1\n\t"
"s_or_b64 s[74:75], s[74:75], vcc\n\t"
"v_cmp_eq_u32 vcc, s62, v73\n\t"
"s_nop 1\n\t"
"s_or_b64 s[74:75], s[74:75], vcc\n\t"
"v_cmp_eq_u32 vcc, s62, v74\n\t"
"s_nop 1\n\t"
"s_or_b64 s[74:75], s[74:75], vcc\n\t"
"v_cmp_eq_u32 vcc, s62, v75\n\t"
"s_nop 1\n\t"
"s_or_b64 s[74:75], s[74:75], vcc\n\t"
"v_cmp_eq_u32 vcc, s62, v76\n\t"
"s_nop 1\n\t"
"s_or_b64 s[74:75], s[74:75], vcc\n\t"
"v_cmp_eq_u32 vcc, s62, v77\n\t"
"s_nop 1\n\t"
"s_or_b64 s[74:75], s[74:75], vcc\n\t"
"v_cmp_eq_u32 vcc, s62, v78\n\t"
"s_nop 1\n\t"
"s_or_b64 s[74:75], s[74:75], vcc\n\t"
"v_cmp_eq_u32 vcc, s62, v79\n\t"
"s_nop 1\n\t"
"s_or_b64 s[74:75], s[74:75], vcc\n\t"
"v_cmp_eq_u32 vcc, s62, v80\n\t"
"s_nop 1\n\t"
"s_or_b64 s[74:75], s[74:75], vcc\n\t"
"v_cmp_eq_u32 vcc, s62, v81\n\t"
"s_nop 1\n\t"
"s_or_b64 s[74:75], s[74:75], vcc\n\t"
"v_cmp_eq_u32 vcc, s62, v82\n\t"
"s_nop 1\n\t"
"s_or_b64 s[74:75], s[74:75], vcc\n\t"
"v_cmp_eq_u32 vcc, s62, v83\n\t"
"s_nop 1\n\t"
"s_or_b64 s[74:75], s[74:75], vcc\n\t"
"v_cmp_eq_u32 vcc, s62, v84\n\t"
"s_nop 1\n\t"
"s_or_b64 s[74:75], s[74:75], vcc\n\t"
"v_cmp_eq_u32 vcc, s62, v85\n\t"
"s_nop 1\n\t"
"s_or_b64 s[74:75], s[74:75], vcc\n\t"
"v_cmp_eq_u32 vcc, s62, v86\n\t"
"s_nop 1\n\t"
"s_or_b64 s[74:75], s[74:75], vcc\n\t"
"v_cmp_eq_u32 vcc, s62, v87\n\t"
"s_nop 1\n\t"
"s_or_b64 s[74:75], s[74:75], vcc\n\t"
"v_cmp_eq_u32 vcc, s62, v88\n\t"
"s_nop 1\n\t"
"s_or_b64 s[74:75], s[74:75], vcc\n\t"
"v_cmp_eq_u32 vcc, s62, v89\n\t"
"s_nop 1\n\t"
"s_or_b64 s[74:75], s[74:75], vcc\n\t"
"v_cmp_eq_u32 vcc, s62, v90\n\t"
"s_nop 1\n\t"
"s_or_b64 s[74:75], s[74:75], vcc\n\t"
"v_cmp_eq_u32 vcc, s62, v91\n\t"
"s_nop 1\n\t"
"s_or_b64 s[74:75], s[74:75], vcc\n\t"
"v_cmp_eq_u32 vcc, s62, v92\n\t"
"s_nop 1\n\t"
"s_or_b64 s[74:75], s[74:75], vcc\n\t"
"v_cmp_eq_u32 vcc, s62, v93\n\t"
"s_nop 1\n\t"
"s_or_b64 s[74:75], s[74:75], vcc\n\t"
"v_cmp_eq_u32 vcc, s62, v94\n\t"
"s_nop 1\n\t"
"s_or_b64 s[74:75], s[74:75], vcc\n\t"
"v_cmp_eq_u32 vcc, s62, v95\n\t"
"s_nop 1\n\t"
"s_or_b64 s[74:75], s[74:75], vcc\n\t"
"s_cbranch_scc0 RD_ok7_%=\n\t"
"RD_go15_%=:\n\t"
"s_add_u32 s60, s60, 1\n\t"
"s_cmp_ge_u32 s60, 0x4000\n\t"
"s_cbranch_scc1 RD_ok7_%=\n\t"
"s_branch RD_again9_%=\n\t"
    :
    : [comm] "s"(ws + W_COMM), [ctl] "s"(ws + W_CTL), [me14] "s"(me << 14), [me8] "s"(__builtin_amdgcn_readfirstlane(mo)), [dB] "s"(p.dB), [dA] "s"(p.dA), [tag] "s"(tag),
      [l16] "v"(lane * 16), [stm] "v"(stm), [mw] "v"(mw), [sel] "v"(sel)
    : "memory", "vcc", "scc", CLOB_S, "v64", "v65", "v66", "v67", "v68", "v69", CL10("v",7), CL10("v",8), CL10("v",9), CL10("v",10), "v110", "v111");
  __builtin_unreachable();
}

__global__ void __launch_bounds__(256, 1) k_main(Params p) {
  extern __shared__ __attribute__((aligned(16))) unsigned char smem[];
  LAS unsigned char* lds = (LAS unsigned char*)smem;
  unsigned char* ws = ws_base(p.ws);
  const int b = blockIdx.x, x = b & 7, i = b >> 3;
  const int w = __builtin_amdgcn_readfirstlane(threadIdx.x >> 6);
  if (x < 4 && i == 31 && (w == 1 || w == 2)) { __builtin_amdgcn_s_sleep(127); __builtin_amdgcn_s_sleep(127); __builtin_amdgcn_s_sleep(64); }
  if ((i >= 30 && w == 3) || (x < 4 && i == 31 && w >= 1)) touch_code<10>((w == 3 ? i - 30 : w - 1) * 64 + lane_id(), 128);
  if (x < 4) {
    if ((i & 15) < 8 && i < 24) chain_role(p, ws, (i & 7) * 4 + x, i >> 4, w, lds);
    const int lane = lane_id(), tid = w * 64 + lane;
    const __amdgpu_buffer_rsrc_t rc = mkrsrc(ws, W_END);
    if (i >= 24) {
      if (i >= 27 && i < 31) {
        const int n = (i - 27) * 4 + x;
        const u4 pz = {POISON, POISON, POISON, POISON};
        #pragma unroll
        for (int j = 0; j < 31; ++j) { const unsigned o = (unsigned)(j * 16 + n) * 4096u + tid * 16u; if ((o >> 14) != CH_PROBE) st16(pz, rc, o, 0); }
        if (tid < 240) st16((u4){0u, 0u, 0u, 0u}, rc, (unsigned)W_CTL + 4096u + (unsigned)(n * 240 + tid) * 16u, 0);
        asm volatile("s_waitcnt vmcnt(0)" ::: "memory");
        BARRIER();
        if (tid == 0) set_done(ws, 64 + NSEC + 1 + n, get_tag(ws), 0u);
        unsigned sink = 0;
        #pragma unroll
        for (int r = 0; r < 12; ++r) {
          const int rnd = 2 + (r >> 1), h = ((i - 27) * 2 + (r & 1)) * 4 + x;
          #pragma unroll
          for (int j = 0; j < 2; ++j) {
            const int l = tid + 256 * j;
            const u16* src;
            if (rnd == 4 || rnd == 5) src = p.Wo + (size_t)(h * DH + (rnd == 5 ? 64 : 0) + (l >> 3)) * DM + (l & 7) * 64;
            else src = (rnd < 4 ? p.Wv : rnd == 6 ? p.Wq : p.Wk) + (size_t)((rnd == 3 ? 256 : 0) + (l >> 1)) * HD + h * DH + (l & 1) * 64;
            sink += *(const GAS unsigned*)src;
          }
        }
        asm volatile("" :: "v"(sink));
        return;
      }
      if (tid != 0 && !(i == 31 && x == 3 && tid < 64)) return;
      const unsigned tag = get_tag(ws);
      if (i == 31 && x == 3) {
        const unsigned v = wait_done(ws, 64, NSEC + 17, tag);
        const unsigned bits = (unsigned)__ballot((v & 1u) && lane <= (int)NSEC);
        if (tid == 0) __hip_atomic_store((GAS u64*)(ws + W_CTL), ((u64)tag << 32) | ((unsigned)__builtin_amdgcn_s_memrealtime() & ~0xFFFu) | bits, RLX_AGENT);
        return;
      }
      const int pr = (i - 24) * 4 + x;
      if (pr > (int)NSEC) return;
      const unsigned po = pr == 0 ? CH_PROBE * 16384u : W_FAR + ((unsigned)(pr - 1) << 24);
      unsigned m0 = 0xFFFFFFFFu, m1 = 0xFFFFFFFFu;
      #pragma unroll 1
      for (int it = 0; it < 5; ++it) {
        unsigned long long t0, t1, t2;
        asm volatile("s_waitcnt vmcnt(0)\n\ts_memtime %0\n\ts_waitcnt lgkmcnt(0)" : "=s"(t0) :: "memory");
        __builtin_amdgcn_raw_buffer_store_b32(POISON, rc, po, 0, 16);
        asm volatile("s_waitcnt vmcnt(0)\n\ts_memtime %0\n\ts_waitcnt lgkmcnt(0)" : "=s"(t1) :: "memory");
        __builtin_amdgcn_raw_buffer_store_b32(POISON, rc, po + 8192u, 0, 16);
        asm volatile("s_waitcnt vmcnt(0)\n\ts_memtime %0\n\ts_waitcnt lgkmcnt(0)" : "=s"(t2) :: "memory");
        m0 = min(m0, (unsigned)(t1 - t0)); m1 = min(m1, (unsigned)(t2 - t1));
      }
      set_done(ws, 64 + pr, tag, (m0 <= m1 ? 0u : 1u) ^ (pr > 4 ? 1u : 0u));
      return;
    }
    const int n = (i - 8) * 4 + x;
    auto ln_ld = [&](int row0, u4 (&a)[4]) {
      #pragma unroll
      for (int t = 0; t < 4; ++t) a[t] = *(const GAS u4*)(p.x + (size_t)(row0 + (lane & 3)) * DM + (lane >> 2) * 32 + t * 8);
    };
    auto ln4 = [&](int row0, u4 (&a)[4]) {
      const int row = row0 + (lane & 3), ks = lane >> 2;
      float s = 0.f;
      #pragma unroll
      for (int t = 0; t < 4; ++t)
        #pragma unroll
        for (int e = 0; e < 4; ++e) s += bf_lo(a[t][e]) + bf_hi(a[t][e]);
      s += __shfl_xor(s, 4); s += __shfl_xor(s, 8); s = allsum_g(s);
      const float mean = s * (1.0f / DM);
      float q = 0.f;
      #pragma unroll
      for (int t = 0; t < 4; ++t)
        #pragma unroll
        for (int e = 0; e < 4; ++e) { const float d0 = bf_lo(a[t][e]) - mean, d1 = bf_hi(a[t][e]) - mean; q = __builtin_fmaf(d0, d0, q); q = __builtin_fmaf(d1, d1, q); }
      q += __shfl_xor(q, 4); q += __shfl_xor(q, 8); q = allsum_g(q);
      const float m = rbf(mean);
      const float rden = 1.0f / rbf(sqrtf(rbf(rbf(q * (1.0f / DM)) + 1e-5f)));
      #pragma unroll
      for (int t = 0; t < 4; ++t)
        #pragma unroll
        for (int e = 0; e < 4; ++e) { const unsigned d = pk_bf16(bf_lo(a[t][e]) - m, bf_hi(a[t][e]) - m); a[t][e] = pk_bf16(bf_lo(d) * rden, bf_hi(d) * rden); }
      #pragma unroll
      for (int g = 0; g < 4; ++g) {
        const u4 o = {a[g >> 1][(g & 1) * 2], a[g >> 1][(g & 1) * 2 + 1], a[2 + (g >> 1)][(g & 1) * 2], a[2 + (g >> 1)][(g & 1) * 2 + 1]};
        st16(o, rc, (unsigned)W_XN + (unsigned)(row >> 4) * 16384u + (unsigned)ks * 1024u + (unsigned)(g * 16 + (row & 15)) * 16u, 0);
      }
    };
    if (i < 16) {
      u4 a[4][4];
      if (w == 1) __builtin_amdgcn_s_sleep(12);
      if (w >= 2) __builtin_amdgcn_s_sleep(64);
      ln_ld((w * 32 + n) * 4, a[0]);
      const unsigned tag = get_tag(ws);
      if (w >= 2) {
        #pragma unroll
        for (int j = 1; j < 4; ++j) ln_ld((j * 128 + w * 32 + n) * 4, a[j]);
      }
      ln4((w * 32 + n) * 4, a[0]);
      if (w < 2) {
        asm volatile("s_waitcnt vmcnt(0)" ::: "memory");
        if (lane == 0) set_done(ws, 96 + w * 32 + n, tag, 0u);
        #pragma unroll
        for (int j = 1; j < 4; ++j) ln_ld((j * 128 + w * 32 + n) * 4, a[j]);
      }
      #pragma unroll
      for (int j = 1; j < 4; ++j) ln4((j * 128 + w * 32 + n) * 4, a[j]);
      asm volatile("s_waitcnt vmcnt(0)" ::: "memory");
      BARRIER();
      if (tid == 0) set_done(ws, n, tag, 0u);
      if (w == 0) reducer_role(p, ws, n, tag);
    }
    return;
  }
  const unsigned tag = get_tag(ws);
  gemm_role(p, ws, i * 4 + (x - 4), w, tag, lds);
  t_role(p, ws, (x - 4) * 4 + (i & 3), i >> 2, w, tag, lds);
}

extern "C" void kernel_launch(void* const* d_in, const int* in_sizes, int n_in, void* d_out, int out_size, void* d_ws, size_t ws_size, hipStream_t stream) {
  (void)in_sizes; (void)n_in; (void)out_size; (void)ws_size;
  Params p;
  p.x = (const u16*)d_in[0]; p.mem0 = (const u16*)d_in[1]; p.Wq = (const u16*)d_in[2]; p.Wk = (const u16*)d_in[3];
  p.Wv = (const u16*)d_in[4]; p.Wo = (const u16*)d_in[5];
  p.wg = (const u16*)d_in[8]; p.wu = (const u16*)d_in[9]; p.wd = (const u16*)d_in[10];
  p.out = (u16*)d_out; p.ws = (unsigned char*)d_ws;
  p.dB = 7; p.dA = 14; p.dC = 48; p.pad = 0;
  (void)hipFuncSetAttribute((const void*)k_main, hipFuncAttributeMaxDynamicSharedMemorySize, LDS_BYTES);
  k_main<<<256, 256, LDS_BYTES, stream>>>(p);
}
```
